```python
import torch

def _softmax(x, axis=-1):
    return torch.softmax(x, dim=axis)
def _gelu(x):
    return torch.nn.functional.gelu(x, approximate="tanh")
def _silu(x):
    return torch.nn.functional.silu(x)
def _sigmoid(x):
    return torch.sigmoid(x)
def _layernorm(x, axis=-1, eps=1e-5):
    m = x.mean(dim=axis, keepdim=True)
    v = x.var(dim=axis, keepdim=True, unbiased=False)
    return (x - m) / torch.sqrt(v + eps)
def _scatter_add(tgt, idx, src, axis=0):
    return tgt.clone().index_add_(axis, idx, src)
def _segment_sum(src, seg, n):
    out = torch.zeros((n,) + src.shape[1:], dtype=src.dtype)
    return out.index_add_(0, seg, src)
def _pad(x, pw):
    flat = tuple(v for pair in reversed(pw) for v in pair)
    return torch.nn.functional.pad(x, flat)

kernel_name = 'gated_mix__sliding_local_dilated__mha_block__prompt'

def setup_inputs():

    _w = (lambda a: (lambda t: t.to(torch.bfloat16) if t.is_floating_point() else t)(torch.as_tensor(a)))
    def _A_setup():
        B, S, D = 1, 2048, 640
        return {
            "x": _w((lambda *s: torch.randn(*s, dtype=torch.bfloat16) * 0.05)(B, S, D)),
            "wqkv": _w((lambda *s: torch.randn(*s, dtype=torch.bfloat16) * 1.6)(D, 3 * D)),
            "wo": _w((lambda *s: torch.randn(*s, dtype=torch.bfloat16) * 0.05)(D, D)),
        }
    _A_in = _A_setup()

    def _B_setup():
        B, S, D, H = 1, 2048, 640, 32
        return {
            "x": _w((lambda *s: torch.randn(*s, dtype=torch.bfloat16) * 0.05)(B, S, D)),
            "wqkv": _w((lambda *s: torch.randn(*s, dtype=torch.bfloat16) * 1.6)(D, 3 * D)),
            "wo": _w((lambda *s: torch.randn(*s, dtype=torch.bfloat16) * 0.1)(D, D)),
        }
    _B_in = _B_setup()

    return {
        "A_x": _A_in["x"],
        "A_wqkv": _A_in["wqkv"],
        "A_wo": _A_in["wo"],
        "B_x": _B_in["x"],
        "B_wqkv": _B_in["wqkv"],
        "B_wo": _B_in["wo"],
        "gate_w": _w((lambda *s: torch.randn(*s, dtype=torch.bfloat16) * 0.05)(640) * 16.0),
    }

def reference(A_x, A_wqkv, A_wo, B_x, B_wqkv, B_wo, gate_w):
    def _A_ref(x, wqkv, wo):
        B, S, D = x.shape
        H = 32; Dh = D // H
        W, dil = 8, 2
        qkv = (lambda x, *d: x.permute(*d))((x @ wqkv).reshape(B, S, 3, H, Dh), 2, 0, 3, 1, 4)
        q, k, v = qkv[0], qkv[1], qkv[2]
        scale = Dh ** -0.5
        pos = torch.arange(S)
        off = torch.arange(W) * dil
        idx = pos[:, None] - off[None, :]
        mask = idx >= 0
        idx_c = torch.where(mask, idx, 0).reshape(-1)
        kg = k[:, :, idx_c, :].reshape(B, H, S, W, Dh)
        vg = v[:, :, idx_c, :].reshape(B, H, S, W, Dh)
        logits = torch.einsum("bhsd,bhswd->bhsw", q, kg) * scale
        logits = torch.where(mask[None, None, :, :], logits, -1e9)
        attn = _softmax(logits, axis=-1)
        o = torch.einsum("bhsw,bhswd->bhsd", attn, vg)
        return (lambda x, *d: x.permute(*d))(o, 0, 2, 1, 3).reshape(B, S, D) @ wo

    def _B_ref(x, wqkv, wo):
        B, S, D = x.shape
        H = 32
        Dh = D // H
        qkv = (lambda x, *d: x.permute(*d))((x @ wqkv).reshape(B, S, 3, H, Dh), 2, 0, 3, 1, 4)
        q, k, v = qkv[0], qkv[1], qkv[2]
        scores = torch.einsum("bhqd,bhkd->bhqk", q, k) * (Dh ** -0.5)
        p = _softmax(scores, axis=-1)
        o = torch.einsum("bhqk,bhkd->bhqd", p, v)
        return (lambda x, *d: x.permute(*d))(o, 0, 2, 1, 3).reshape(B, S, D) @ wo

    x = A_x
    g = _sigmoid((x * gate_w).sum(-1, keepdims=True))
    a = _A_ref(x, A_wqkv, A_wo)
    b = _B_ref(x, B_wqkv, B_wo)
    return g * a + (1 - g) * b
```

```cpp
#include <hip/hip_runtime.h>
#include <hip/hip_bf16.h>
#include <stdint.h>

typedef short bf16x8 __attribute__((ext_vector_type(8)));
typedef short s16x4 __attribute__((ext_vector_type(4)));
typedef __bf16 bfv8 __attribute__((ext_vector_type(8)));
typedef __bf16 bfv16 __attribute__((ext_vector_type(16)));
typedef float f32x16 __attribute__((ext_vector_type(16)));
typedef float f32x8 __attribute__((ext_vector_type(8)));
typedef float f32x4 __attribute__((ext_vector_type(4)));
typedef unsigned u32x4 __attribute__((ext_vector_type(4)));
typedef unsigned u32x2 __attribute__((ext_vector_type(2)));
#define LAS __attribute__((address_space(3)))
typedef LAS const char* lds_cptr;
typedef LAS char* lds_ptr;

constexpr int S_ = 2048, D_ = 640, H_ = 32, N3_ = 1920;
constexpr float QSCALE = 0.22360679774997896f * 1.4426950408889634f;

constexpr size_t WS_KV = 0;
constexpr size_t WS_Q = WS_KV + (size_t)32 * 64 * 4096;
constexpr size_t WS_QKVA = WS_Q + (size_t)32 * 64 * 2048;
constexpr size_t WS_OA = WS_QKVA + (size_t)32 * 2048 * 128;
constexpr size_t WS_OB = WS_OA + (size_t)2048 * 640 * 2;
constexpr size_t WS_G = WS_OB + (size_t)2048 * 640 * 2;

#define SBAR() __builtin_amdgcn_sched_barrier(0)
#define PIN(x) asm volatile("" : "+v"(x))
#define MFMA32(a, b, c) __builtin_amdgcn_mfma_f32_32x32x16_bf16(__builtin_bit_cast(bfv8, a), __builtin_bit_cast(bfv8, b), c, 0, 0, 0)
#define MFMA16(a, b, c) __builtin_amdgcn_mfma_f32_16x16x32_bf16(__builtin_bit_cast(bfv8, a), __builtin_bit_cast(bfv8, b), c, 0, 0, 0)
#define WAIT_BAR(N) asm volatile("s_waitcnt vmcnt(" #N ") lgkmcnt(0)\n\ts_barrier" ::: "memory")

__device__ __forceinline__ unsigned cvtpk(float lo, float hi) {
  typedef __bf16 bf2 __attribute__((ext_vector_type(2)));
  bf2 r; r[0] = (__bf16)lo; r[1] = (__bf16)hi;
  return __builtin_bit_cast(unsigned, r);
}
__device__ __forceinline__ float bf_lo(unsigned u) { return __uint_as_float(u << 16); }
__device__ __forceinline__ float bf_hi(unsigned u) { return __uint_as_float(u & 0xffff0000u); }

__device__ __forceinline__ void glds16(const void* g, unsigned lds_base) {
  unsigned sv;
  asm volatile("s_mov_b32 %0, m0\n\ts_mov_b32 m0, %2\n\ts_nop 0\n\tglobal_load_lds_dwordx4 %1, off\n\ts_mov_b32 m0, %0"
               : "=&s"(sv) : "v"(g), "s"(lds_base) : "memory");
}
__device__ __forceinline__ void glds16x4(const void* g, unsigned lds_base) {
  unsigned sv;
  asm volatile("s_mov_b32 %0, m0\n\ts_mov_b32 m0, %2\n\ts_nop 0\n\t"
               "global_load_lds_dwordx4 %1, off\n\t"
               "global_load_lds_dwordx4 %1, off offset:1024\n\t"
               "global_load_lds_dwordx4 %1, off offset:2048\n\t"
               "global_load_lds_dwordx4 %1, off offset:3072\n\t"
               "s_mov_b32 m0, %0"
               : "=&s"(sv) : "v"(g), "s"(lds_base) : "memory");
}
typedef short v4i16_t __attribute__((ext_vector_type(4)));
__device__ __forceinline__ s16x4 trd(lds_cptr p) {
  return __builtin_bit_cast(s16x4, __builtin_amdgcn_ds_read_tr16_b64_v4i16((LAS v4i16_t*)p));
}
__device__ __forceinline__ bf16x8 cat8(s16x4 a, s16x4 b) { return __builtin_shufflevector(a, b, 0, 1, 2, 3, 4, 5, 6, 7); }
__device__ __forceinline__ bf16x8 ldsr128(lds_cptr p) { return *(const LAS bf16x8*)p; }

__device__ __forceinline__ void block_hc(int b, int& h, int& c) {
  const int x = b & 7, slot = b >> 3;
  h = 8 * (x >> 1) + (slot & 7);
  c = 4 * (x & 1) + (slot >> 3);
}
__device__ __forceinline__ unsigned o_off(int token, int k0) {
  const int ks = k0 >> 5, kk = k0 & 31;
  const int g = 2 * (kk >> 4) + ((kk >> 2) & 1), t = (kk >> 3) & 1;
  return (unsigned)((((token >> 4) * 20 + ks) * 64 + (token & 15) + 16 * g) * 16 + 8 * t);
}

constexpr int G1_XB = 256 * 128, G1_WROW = 320, G1_WB = 64 * G1_WROW, G1_STAGE = G1_XB + G1_WB;
constexpr int G1_LDS = 3 * G1_STAGE;

__global__ __launch_bounds__(512, 2) void k1_qkv(const __hip_bfloat16* __restrict__ x, const __hip_bfloat16* __restrict__ wA,
                                                 const __hip_bfloat16* __restrict__ wB, const __hip_bfloat16* __restrict__ gw,
                                                 char* __restrict__ ws) {
  extern __shared__ __attribute__((aligned(16))) char lds[];
  const int tid = threadIdx.x, lane = tid & 63;
  const int w = __builtin_amdgcn_readfirstlane(tid >> 6);
  int h, c; block_hc(blockIdx.x, h, c);
  const int e = h & 1;
  const unsigned lds0 = (unsigned)(uintptr_t)lds;

  const int gtok = 256 * c + 8 * h + w;
  unsigned gx[5], gg[5];
  {
    const unsigned* xp = (const unsigned*)(x + (size_t)gtok * D_) + lane * 5;
    const unsigned* gp = (const unsigned*)gw + lane * 5;
#pragma unroll
    for (int i = 0; i < 5; ++i) { gx[i] = xp[i]; gg[i] = gp[i]; }
  }

  const char* xs[4];
#pragma unroll
  for (int i = 0; i < 4; ++i) {
    const int row = 32 * w + 8 * i + (lane >> 3);
    const int ch = (lane & 7) ^ ((row >> 1) & 7);
    xs[i] = (const char*)x + (size_t)(256 * c + row) * (D_ * 2) + ch * 16;
  }
  const char* wsrc[3];
#pragma unroll
  for (int i = 0; i < 3; ++i) {
    const int ins = (i < 2) ? (2 * w + i) : (16 + (w & 3));
    const int n = ins * 64 + lane, row = n / 20, slot = n % 20;
    const int br = slot / 10; int sl = slot % 10; if (sl == 9) sl = 8;
    const int seg = (sl < 6) ? (sl >> 1) : (sl - 6);
    const int piece = (sl < 6) ? ((sl & 1) + e) : (2 - 2 * e);
    wsrc[i] = (const char*)(br ? wA : wB) + (size_t)row * (N3_ * 2) + seg * 1280 + 40 * h - 8 * e + piece * 16;
  }
  auto issue = [&](int st, int buf) {
    const unsigned base = lds0 + buf * G1_STAGE;
    const size_t wadv = (size_t)st * 64 * (N3_ * 2);
    glds16(wsrc[0] + wadv, base + G1_XB + (2 * w) * 1024);
    glds16(wsrc[1] + wadv, base + G1_XB + (2 * w + 1) * 1024);
    if (w < 4) glds16(wsrc[2] + wadv, base + G1_XB + (16 + w) * 1024);
#pragma unroll
    for (int i = 0; i < 4; ++i) glds16(xs[i] + st * 128, base + (4 * w + i) * 1024);
  };
  issue(0, 0); issue(1, 1); issue(2, 2);

  const int br = w >> 2, tq = w & 3;
  const int r = lane & 31, hh = lane >> 5, g16 = (lane >> 4) & 1, q4 = (lane & 15) >> 2, p4 = lane & 3;
  unsigned xoff[4];
#pragma unroll
  for (int ks = 0; ks < 4; ++ks) xoff[ks] = (unsigned)((64 * tq + r) * 128 + (((2 * ks + hh) ^ ((r >> 1) & 7)) * 16));
  unsigned woff[2];
  {
    const int fg = 4 * g16 + p4;
    const int rowb = (8 * hh + q4) * G1_WROW + G1_XB + br * 160;
    woff[0] = (unsigned)(rowb + fg * 8);
    const int o1 = (fg < 4) ? (64 + fg * 8) : ((fg == 4 ? 96 : (fg == 5 ? 128 : 112)) + 8 * e);
    woff[1] = (unsigned)(rowb + o1);
  }
  f32x16 acc[2][2];
#pragma unroll
  for (int a = 0; a < 2; ++a)
#pragma unroll
    for (int b = 0; b < 2; ++b) acc[a][b] = f32x16{};

  int buf = 0;
  for (int st = 0; st < 10; ++st) {
    if (st == 0) { WAIT_BAR(12); } else if (st < 9) { WAIT_BAR(6); } else { WAIT_BAR(0); }
    if (st >= 1 && st + 2 < 10) issue(st + 2, buf == 0 ? 2 : buf - 1);
    const lds_cptr sb = (lds_cptr)lds + buf * G1_STAGE;
#pragma unroll
    for (int ks = 0; ks < 4; ++ks) {
      const bf16x8 x0 = ldsr128(sb + xoff[ks]);
      const bf16x8 x1 = ldsr128(sb + xoff[ks] + 32 * 128);
      const bf16x8 w0 = cat8(trd(sb + woff[0] + ks * 16 * G1_WROW), trd(sb + woff[0] + (ks * 16 + 4) * G1_WROW));
      const bf16x8 w1 = cat8(trd(sb + woff[1] + ks * 16 * G1_WROW), trd(sb + woff[1] + (ks * 16 + 4) * G1_WROW));
      acc[0][0] = MFMA32(w0, x0, acc[0][0]);
      acc[1][0] = MFMA32(w0, x1, acc[1][0]);
      acc[0][1] = MFMA32(w1, x0, acc[0][1]);
      acc[1][1] = MFMA32(w1, x1, acc[1][1]);
    }
    buf = (buf == 2) ? 0 : buf + 1;
  }
  WAIT_BAR(0);

  if (br == 0) {
    char* scr = lds + w * 4096;
#pragma unroll
    for (int tt = 0; tt < 2; ++tt) {
      const f32x16 a0 = acc[tt][0], a1 = acc[tt][1];
      const int tile = 8 * c + 2 * tq + tt;
      u32x4 q0 = {cvtpk(a0[0] * QSCALE, a0[1] * QSCALE), cvtpk(a0[2] * QSCALE, a0[3] * QSCALE),
                  cvtpk(a0[4] * QSCALE, a0[5] * QSCALE), cvtpk(a0[6] * QSCALE, a0[7] * QSCALE)};
      u32x4 q1 = {hh ? 0u : cvtpk(a1[8] * QSCALE, a1[9] * QSCALE), hh ? 0u : cvtpk(a1[10] * QSCALE, a1[11] * QSCALE), 0u, 0u};
      u32x4 k0 = {cvtpk(a0[8], a0[9]), cvtpk(a0[10], a0[11]), cvtpk(a0[12], a0[13]), cvtpk(a0[14], a0[15])};
      u32x4 k1 = {hh ? 0u : cvtpk(a1[12], a1[13]), hh ? 0u : cvtpk(a1[14], a1[15]), hh ? 0u : 0x3f803f80u, hh ? 0u : 0x00003f80u};
      char* qd = ws + WS_Q + (size_t)(h * 64 + tile) * 2048 + lane * 16;
      *(u32x4*)qd = q0; *(u32x4*)(qd + 1024) = q1;
      char* kd = ws + WS_KV + (size_t)(h * 64 + tile) * 4096 + lane * 16;
      *(u32x4*)kd = k0; *(u32x4*)(kd + 1024) = k1;
      char* vr = scr + tt * 2048 + r * 64;
      u32x2 va = {cvtpk(a1[0], a1[1]), cvtpk(a1[2], a1[3])};
      u32x2 vb = {cvtpk(a1[4], a1[5]), cvtpk(a1[6], a1[7])};
      u32x2 vc = {hh ? cvtpk(a1[8], a1[9]) : 0x00003f80u, hh ? cvtpk(a1[10], a1[11]) : 0u};
      *(u32x2*)(vr + (hh + e) * 8) = va;
      *(u32x2*)(vr + (2 + hh + e) * 8) = vb;
      *(u32x2*)(vr + (hh ? (e ? 0 : 4) : 5) * 8) = vc;
      *(u32x2*)(vr + (6 + hh) * 8) = u32x2{0u, 0u};
    }
    asm volatile("s_waitcnt lgkmcnt(0)" ::: "memory");
#pragma unroll
    for (int tt = 0; tt < 2; ++tt) {
      const int tile = 8 * c + 2 * tq + tt;
      char* kd = ws + WS_KV + (size_t)(h * 64 + tile) * 4096 + lane * 16;
      const lds_cptr vp = (lds_cptr)lds + w * 4096 + tt * 2048 + (8 * hh + q4) * 64 + 32 * g16 + 8 * p4;
#pragma unroll
      for (int s = 0; s < 2; ++s) {
        const bf16x8 vf = cat8(trd(vp + s * 1024), trd(vp + s * 1024 + 256));
        *(bf16x8*)(kd + 2048 + s * 1024) = vf;
      }
    }
  } else {
#pragma unroll
    for (int tt = 0; tt < 2; ++tt) {
      const f32x16 a0 = acc[tt][0], a1 = acc[tt][1];
      const int tok = 256 * c + 64 * tq + 32 * tt + r;
      char* d = ws + WS_QKVA + ((size_t)h * 2048 + tok) * 128;
      const int s4 = e ? 0 : 4;
      *(u32x2*)(d + (hh + e) * 8) = u32x2{cvtpk(a0[0], a0[1]), cvtpk(a0[2], a0[3])};
      *(u32x2*)(d + (2 + hh + e) * 8) = u32x2{cvtpk(a0[4], a0[5]), cvtpk(a0[6], a0[7])};
      *(u32x2*)(d + 40 + (hh + e) * 8) = u32x2{cvtpk(a0[8], a0[9]), cvtpk(a0[10], a0[11])};
      *(u32x2*)(d + 40 + (2 + hh + e) * 8) = u32x2{cvtpk(a0[12], a0[13]), cvtpk(a0[14], a0[15])};
      *(u32x2*)(d + 80 + (hh + e) * 8) = u32x2{cvtpk(a1[0], a1[1]), cvtpk(a1[2], a1[3])};
      *(u32x2*)(d + 80 + (2 + hh + e) * 8) = u32x2{cvtpk(a1[4], a1[5]), cvtpk(a1[6], a1[7])};
      *(u32x2*)(d + (hh ? 80 : 0) + s4 * 8) = u32x2{cvtpk(a1[8], a1[9]), cvtpk(a1[10], a1[11])};
      *(u32x2*)(d + (hh ? 120 : 40 + s4 * 8)) = u32x2{cvtpk(a1[12], a1[13]), cvtpk(a1[14], a1[15])};
    }
  }
  {
    float s = 0.f;
#pragma unroll
    for (int i = 0; i < 5; ++i) s += bf_lo(gx[i]) * bf_lo(gg[i]) + bf_hi(gx[i]) * bf_hi(gg[i]);
#pragma unroll
    for (int o = 32; o > 0; o >>= 1) s += __shfl_xor(s, o);
    if (lane == 0) ((float*)(ws + WS_G))[gtok] = 1.f / (1.f + __expf(-s));
  }
}

constexpr int AT_STAGE = 32768, AT_LDS = 3 * AT_STAGE + 64;

__device__ __forceinline__ f32x16 smf(bf16x8 a, const f32x16& p, int s, f32x16 o, int idx) {
  f32x8 b = s ? __builtin_shufflevector(p, p, 8, 9, 10, 11, 12, 13, 14, 15) : __builtin_shufflevector(p, p, 0, 1, 2, 3, 4, 5, 6, 7);
  return __builtin_amdgcn_smfmac_f32_32x32x32_bf16(__builtin_bit_cast(bfv8, a), __builtin_bit_cast(bfv16, b), o, idx, 0, 0);
}
__device__ __forceinline__ float max16(const f32x16& s) {
  float a = fmaxf(fmaxf(s[0], s[1]), s[2]), b = fmaxf(fmaxf(s[3], s[4]), s[5]);
  a = fmaxf(fmaxf(a, s[6]), s[7]); b = fmaxf(fmaxf(b, s[8]), s[9]);
  a = fmaxf(fmaxf(a, s[10]), s[11]); b = fmaxf(fmaxf(b, s[12]), s[13]);
  a = fmaxf(fmaxf(a, s[14]), s[15]);
  return fmaxf(a, b);
}
__device__ __forceinline__ float swap_max(float m) {
  auto rr = __builtin_amdgcn_permlane32_swap(__float_as_uint(m), __float_as_uint(m), false, false);
  return fmaxf(__uint_as_float(rr[0]), __uint_as_float(rr[1]));
}
__device__ __forceinline__ void set_ref(bf16x8& q1, float m, int hh) {
  const float n = -m;
  const unsigned a = __float_as_uint(n) & 0xffff0000u;
  const float r1 = n - __uint_as_float(a);
  const unsigned b = __float_as_uint(r1) & 0xffff0000u;
  const float r2 = r1 - __uint_as_float(b);
  const unsigned cc = __float_as_uint(r2) & 0xffff0000u;
  u32x4 v = __builtin_bit_cast(u32x4, q1);
  v[2] = hh ? 0u : ((a >> 16) | b);
  v[3] = hh ? 0u : (cc >> 16);
  q1 = __builtin_bit_cast(bf16x8, v);
}

__global__ __launch_bounds__(512, 2) void k2_attn(char* __restrict__ ws) {
  extern __shared__ __attribute__((aligned(16))) char lds[];
  const int tid = threadIdx.x, lane = tid & 63;
  const int w = __builtin_amdgcn_readfirstlane(tid >> 6);
  int h, c; block_hc(blockIdx.x, h, c);
  const unsigned lds0 = (unsigned)(uintptr_t)lds;
  const int kh = w >> 2, tq = w & 3, hh = lane >> 5, r = lane & 31;
  const char* kvh = ws + WS_KV + (size_t)h * 64 * 4096;
  const char* kvsrc = kvh + (size_t)(32 * kh + tq) * 4096 + lane * 16;
  auto issue = [&](int st, int buf) { glds16x4(kvsrc + (size_t)st * 4 * 4096, lds0 + buf * AT_STAGE + w * 4096); };
  issue(0, 0); issue(1, 1); issue(2, 2);

  bf16x8 qf[2][2];
#pragma unroll
  for (int t = 0; t < 2; ++t)
#pragma unroll
    for (int s = 0; s < 2; ++s)
      qf[t][s] = *(const bf16x8*)(ws + WS_Q + (size_t)(h * 64 + 8 * c + 2 * tq + t) * 2048 + s * 1024 + lane * 16);

  {
    const int tok = 256 * c + (tid >> 1), hf = tid & 1;
    const char* base = ws + WS_QKVA + (size_t)h * 2048 * 128;
    const int c0 = hf ? 24 : 0;
    const int c2 = hf ? 32 : 16;
    float qv[12];
    {
      const char* rp = base + (size_t)tok * 128;
      const u32x2 a = *(const u32x2*)(rp + c0), b = *(const u32x2*)(rp + c0 + 8), d = *(const u32x2*)(rp + c2);
      qv[0] = bf_lo(a[0]); qv[1] = bf_hi(a[0]); qv[2] = bf_lo(a[1]); qv[3] = bf_hi(a[1]);
      qv[4] = bf_lo(b[0]); qv[5] = bf_hi(b[0]); qv[6] = bf_lo(b[1]); qv[7] = bf_hi(b[1]);
      qv[8] = hf ? 0.f : bf_lo(d[0]); qv[9] = hf ? 0.f : bf_hi(d[0]); qv[10] = hf ? 0.f : bf_lo(d[1]); qv[11] = hf ? 0.f : bf_hi(d[1]);
    }
    float lg[8];
    u32x2 vv[8][3];
#pragma unroll
    for (int j = 0; j < 8; ++j) {
      const int kt = tok - 2 * j;
      const char* rp = base + (size_t)(kt < 0 ? 0 : kt) * 128;
      const u32x2 a = *(const u32x2*)(rp + 40 + c0), b = *(const u32x2*)(rp + 40 + c0 + 8), d = *(const u32x2*)(rp + 40 + c2);
      vv[j][0] = *(const u32x2*)(rp + 80 + c0); vv[j][1] = *(const u32x2*)(rp + 80 + c0 + 8); vv[j][2] = *(const u32x2*)(rp + 80 + c2);
      float s = qv[0] * bf_lo(a[0]) + qv[1] * bf_hi(a[0]) + qv[2] * bf_lo(a[1]) + qv[3] * bf_hi(a[1]);
      s += qv[4] * bf_lo(b[0]) + qv[5] * bf_hi(b[0]) + qv[6] * bf_lo(b[1]) + qv[7] * bf_hi(b[1]);
      s += qv[8] * bf_lo(d[0]) + qv[9] * bf_hi(d[0]) + qv[10] * bf_lo(d[1]) + qv[11] * bf_hi(d[1]);
      s += __shfl_xor(s, 1);
      lg[j] = (kt < 0) ? -1e9f * 1.4426950408889634f : s * QSCALE;
    }
    float m = lg[0];
#pragma unroll
    for (int j = 1; j < 8; ++j) m = fmaxf(m, lg[j]);
    float l = 0.f;
#pragma unroll
    for (int j = 0; j < 8; ++j) { lg[j] = __builtin_amdgcn_exp2f(lg[j] - m); l += lg[j]; }
    const float rl = 1.f / l;
    float o[12];
#pragma unroll
    for (int i = 0; i < 12; ++i) o[i] = 0.f;
#pragma unroll
    for (int j = 0; j < 8; ++j) {
      const float p = lg[j] * rl;
#pragma unroll
      for (int u = 0; u < 3; ++u) {
        o[4 * u + 0] += p * bf_lo(vv[j][u][0]); o[4 * u + 1] += p * bf_hi(vv[j][u][0]);
        o[4 * u + 2] += p * bf_lo(vv[j][u][1]); o[4 * u + 3] += p * bf_hi(vv[j][u][1]);
      }
    }
    char* od = ws + WS_OA;
    const int kb = 20 * h + (hf ? 12 : 0);
    *(u32x2*)(od + o_off(tok, kb)) = u32x2{cvtpk(o[0], o[1]), cvtpk(o[2], o[3])};
    *(u32x2*)(od + o_off(tok, kb + 4)) = u32x2{cvtpk(o[4], o[5]), cvtpk(o[6], o[7])};
    if (!hf) *(u32x2*)(od + o_off(tok, kb + 8)) = u32x2{cvtpk(o[8], o[9]), cvtpk(o[10], o[11])};
  }

  int idx = 0xDDDD; PIN(idx);
  const f32x16 zero = f32x16{};
  volatile LAS unsigned* flag = (volatile LAS unsigned*)((lds_ptr)lds + 3 * AT_STAGE);
  float ofin[12];
  for (int attempt = 0;; ++attempt) {
    if (tid == 0) *flag = 0u;
    WAIT_BAR(0);
    if (attempt == 0) {
      const bf16x8 k0 = ldsr128((lds_cptr)lds + lane * 16), k1 = ldsr128((lds_cptr)lds + 1024 + lane * 16);
#pragma unroll
      for (int t = 0; t < 2; ++t) {
        f32x16 s = MFMA32(k0, qf[t][0], zero); s = MFMA32(k1, qf[t][1], s);
        set_ref(qf[t][1], swap_max(max16(s)), hh);
      }
    }
    f32x16 oa = zero, ob = zero, sa, sb;
    const lds_cptr fb = (lds_cptr)lds + kh * 16384 + lane * 16;
    bf16x8 k0 = ldsr128(fb), k1 = ldsr128(fb + 1024);
    sa = MFMA32(k0, qf[0][0], zero); sa = MFMA32(k1, qf[0][1], sa);
    int buf = 0;
    for (int st = 0; st < 8; ++st) {
      const lds_cptr sb0 = fb + buf * AT_STAGE;
      const int nbuf = (buf == 2) ? 0 : buf + 1;
#pragma unroll
      for (int i = 0; i < 4; ++i) {
        const bf16x8 v0 = ldsr128(sb0 + i * 4096 + 2048), v1 = ldsr128(sb0 + i * 4096 + 3072);
        sb = MFMA32(k0, qf[1][0], zero);
#pragma unroll
        for (int j = 0; j < 4; ++j) sa[j] = __builtin_amdgcn_exp2f(sa[j]);
        sb = MFMA32(k1, qf[1][1], sb);
        if (i == 3) {
          if (st < 6) { WAIT_BAR(4); } else { WAIT_BAR(0); }
          if (st >= 0 && st + 3 < 8) {   }
        }
        const lds_cptr nk = (i < 3) ? (sb0 + (i + 1) * 4096) : (fb + nbuf * AT_STAGE);
        k0 = ldsr128(nk); k1 = ldsr128(nk + 1024);
        if (i == 3 && st + 3 < 8) issue(st + 3, buf);
#pragma unroll
        for (int j = 4; j < 8; ++j) sa[j] = __builtin_amdgcn_exp2f(sa[j]);
        oa = smf(v0, sa, 0, oa, idx);
#pragma unroll
        for (int j = 8; j < 16; ++j) sa[j] = __builtin_amdgcn_exp2f(sa[j]);
        oa = smf(v1, sa, 1, oa, idx);
        sa = MFMA32(k0, qf[0][0], zero);
#pragma unroll
        for (int j = 0; j < 4; ++j) sb[j] = __builtin_amdgcn_exp2f(sb[j]);
        sa = MFMA32(k1, qf[0][1], sa);
#pragma unroll
        for (int j = 4; j < 8; ++j) sb[j] = __builtin_amdgcn_exp2f(sb[j]);
        ob = smf(v0, sb, 0, ob, idx);
#pragma unroll
        for (int j = 8; j < 16; ++j) sb[j] = __builtin_amdgcn_exp2f(sb[j]);
        ob = smf(v1, sb, 1, ob, idx);
      }
      buf = nbuf;
    }
    WAIT_BAR(0);
    {
      float* xw = (float*)(lds + (size_t)(tq * 2 + kh) * 3072) + lane;
      const float* xr = (const float*)(lds + (size_t)(tq * 2 + (kh ^ 1)) * 3072) + lane;
#pragma unroll
      for (int i = 0; i < 12; ++i) xw[i * 64] = kh ? oa[i] : ob[i];
      __syncthreads();
#pragma unroll
      for (int i = 0; i < 12; ++i) ofin[i] = (kh ? ob[i] : oa[i]) + xr[i * 64];
    }
    auto rr = __builtin_amdgcn_permlane32_swap(__float_as_uint(ofin[8]), __float_as_uint(ofin[8]), false, false);
    const float l = __uint_as_float(rr[1]);
    const bool bad = !(l > 8.67e-19f && l < 1.15e18f);
    const float rl = 1.f / l;
#pragma unroll
    for (int i = 0; i < 12; ++i) ofin[i] *= rl;
    if (attempt == 1) break;
    if (bad) *flag = 1u;
    __syncthreads();
    if (*flag == 0u) break;
    __syncthreads();
    issue(0, 0); issue(1, 1); issue(2, 2);
#pragma unroll
    for (int t = 0; t < 2; ++t) {
      set_ref(qf[t][1], 0.f, hh);
      float m = -INFINITY;
      for (int kt = 0; kt < 64; ++kt) {
        const bf16x8 a0 = *(const bf16x8*)(kvh + (size_t)kt * 4096 + lane * 16);
        const bf16x8 a1 = *(const bf16x8*)(kvh + (size_t)kt * 4096 + 1024 + lane * 16);
        f32x16 s = MFMA32(a0, qf[t][0], zero); s = MFMA32(a1, qf[t][1], s);
        m = fmaxf(m, max16(s));
      }
      set_ref(qf[t][1], swap_max(m), hh);
    }
  }
  {
    const int tok = 256 * c + 64 * tq + 32 * kh + r;
    char* od = ws + WS_OB;
    const int kb = 20 * h + 4 * hh;
    *(u32x2*)(od + o_off(tok, kb)) = u32x2{cvtpk(ofin[0], ofin[1]), cvtpk(ofin[2], ofin[3])};
    *(u32x2*)(od + o_off(tok, kb + 8)) = u32x2{cvtpk(ofin[4], ofin[5]), cvtpk(ofin[6], ofin[7])};
    if (!hh) *(u32x2*)(od + o_off(tok, kb + 16)) = u32x2{cvtpk(ofin[8], ofin[9]), cvtpk(ofin[10], ofin[11])};
  }
}

constexpr int OP_HALF = 128 * 160, OP_STAGE = 2 * OP_HALF, OP_LDS = 3 * OP_STAGE;

__global__ __launch_bounds__(512, 2) void k3_out(const __hip_bfloat16* __restrict__ woA, const __hip_bfloat16* __restrict__ woB,
                                                 const char* __restrict__ ws, __hip_bfloat16* __restrict__ out) {
  extern __shared__ __attribute__((aligned(16))) char lds[];
  const int tid = threadIdx.x, lane = tid & 63;
  const int w = __builtin_amdgcn_readfirstlane(tid >> 6);
  const int xcd = blockIdx.x & 7, slot = blockIdx.x >> 3;
  const int rt = 8 * (xcd >> 1) + (slot & 7), ct = 4 * (xcd & 1) + (slot >> 3);
  const unsigned lds0 = (unsigned)(uintptr_t)lds;
  const int br = w >> 2, rq = w & 3;
  const char* wsrc[5];
#pragma unroll
  for (int i = 0; i < 5; ++i) {
    const int n = (rq * 5 + i) * 64 + lane, row = n / 10, pc = n % 10;
    wsrc[i] = (const char*)(br ? woB : woA) + (size_t)row * (D_ * 2) + ct * 160 + pc * 16;
  }
  auto issue = [&](int st, int buf) {
#pragma unroll
    for (int i = 0; i < 5; ++i)
      glds16(wsrc[i] + (size_t)st * 128 * (D_ * 2), lds0 + buf * OP_STAGE + br * OP_HALF + (rq * 5 + i) * 1024);
  };
  issue(0, 0);
  bf16x8 of[20];
  const char* osrc = ws + (br ? WS_OB : WS_OA) + (size_t)(rt * 4 + rq) * 20 * 1024 + lane * 16;
#pragma unroll
  for (int ks = 0; ks < 20; ++ks) of[ks] = *(const bf16x8*)(osrc + ks * 1024);
  const float gate = ((const float*)(ws + WS_G))[rt * 64 + rq * 16 + (lane & 15)];
  issue(1, 1); issue(2, 2);

  const int g = lane >> 4, q4 = (lane & 15) >> 2, p4 = lane & 3;
  const unsigned woff = (unsigned)(br * OP_HALF + (16 * (g >> 1) + 4 * (g & 1) + q4) * 160 + 8 * p4);
  f32x4 acc[5];
#pragma unroll
  for (int i = 0; i < 5; ++i) acc[i] = f32x4{};
#pragma unroll
  for (int st = 0; st < 5; ++st) {
    if (st == 0) { WAIT_BAR(10); } else if (st < 4) { WAIT_BAR(5); } else { WAIT_BAR(0); }
    if (st >= 1 && st + 2 < 5) issue(st + 2, (st + 2) % 3);
    const lds_cptr sb = (lds_cptr)lds + (st % 3) * OP_STAGE + woff;
#pragma unroll
    for (int k4 = 0; k4 < 4; ++k4) {
#pragma unroll
      for (int n = 0; n < 5; ++n) {
        const bf16x8 wf = cat8(trd(sb + k4 * 32 * 160 + n * 32), trd(sb + (k4 * 32 + 8) * 160 + n * 32));
        acc[n] = MFMA16(wf, of[st * 4 + k4], acc[n]);
      }
    }
  }
  WAIT_BAR(0);
  float* xch = (float*)lds + rq * 20 * 64 + lane;
  if (br == 0) {
#pragma unroll
    for (int n = 0; n < 5; ++n)
#pragma unroll
      for (int j = 0; j < 4; ++j) xch[(n * 4 + j) * 64] = acc[n][j];
  }
  __syncthreads();
  if (br == 1) {
    char* op = (char*)out + (size_t)(rt * 64 + rq * 16 + (lane & 15)) * (D_ * 2) + ct * 160 + g * 8;
#pragma unroll
    for (int n = 0; n < 5; ++n) {
      float v[4];
#pragma unroll
      for (int j = 0; j < 4; ++j) v[j] = gate * xch[(n * 4 + j) * 64] + (1.f - gate) * acc[n][j];
      *(u32x2*)(op + n * 32) = u32x2{cvtpk(v[0], v[1]), cvtpk(v[2], v[3])};
    }
  }
}

extern "C" void kernel_launch(void* const* d_in, const int* in_sizes, int n_in, void* d_out, int out_size, void* d_ws,
                              size_t ws_size, hipStream_t stream) {
  const __hip_bfloat16* x = (const __hip_bfloat16*)d_in[0];
  const __hip_bfloat16* wA = (const __hip_bfloat16*)d_in[1];
  const __hip_bfloat16* woA = (const __hip_bfloat16*)d_in[2];
  const __hip_bfloat16* wB = (const __hip_bfloat16*)d_in[4];
  const __hip_bfloat16* woB = (const __hip_bfloat16*)d_in[5];
  const __hip_bfloat16* gw = (const __hip_bfloat16*)d_in[6];
  (void)hipFuncSetAttribute((const void*)k1_qkv, hipFuncAttributeMaxDynamicSharedMemorySize, G1_LDS);
  (void)hipFuncSetAttribute((const void*)k2_attn, hipFuncAttributeMaxDynamicSharedMemorySize, AT_LDS);
  (void)hipFuncSetAttribute((const void*)k3_out, hipFuncAttributeMaxDynamicSharedMemorySize, OP_LDS);
  k1_qkv<<<256, 512, G1_LDS, stream>>>(x, wA, wB, gw, (char*)d_ws);
  k2_attn<<<256, 512, AT_LDS, stream>>>((char*)d_ws);
  k3_out<<<256, 512, OP_LDS, stream>>>(woA, woB, (const char*)d_ws, (__hip_bfloat16*)d_out);
}
```

```cpp
#include <hip/hip_runtime.h>
#include <hip/hip_bf16.h>
#include <stdint.h>

typedef short bf16x8 __attribute__((ext_vector_type(8)));
typedef short s16x4 __attribute__((ext_vector_type(4)));
typedef __bf16 bfv8 __attribute__((ext_vector_type(8)));
typedef __bf16 bfv2 __attribute__((ext_vector_type(2)));
typedef __bf16 bfv16 __attribute__((ext_vector_type(16)));
typedef float f32x16 __attribute__((ext_vector_type(16)));
typedef float f32x8 __attribute__((ext_vector_type(8)));
typedef float f32x4 __attribute__((ext_vector_type(4)));
typedef unsigned u32x4 __attribute__((ext_vector_type(4)));
typedef unsigned u32x2 __attribute__((ext_vector_type(2)));
#define LAS __attribute__((address_space(3)))
typedef LAS const char* lds_cptr;
typedef LAS char* lds_ptr;

constexpr int D_ = 640, N3_ = 1920;
constexpr float QSCALE = 0.22360679774997896f * 1.4426950408889634f;

constexpr size_t UNIT = 2048 * 16;
constexpr int VRUN = 336, VTILE = 4 * VRUN;
constexpr size_t WS_QKB = 0;
constexpr size_t WS_VB = WS_QKB + 160 * UNIT;
constexpr size_t WS_QKVA = WS_VB + (size_t)32 * 64 * VTILE + 4096;
constexpr size_t WS_OA = WS_QKVA + 240 * UNIT;
constexpr size_t WS_OB = WS_OA + (size_t)2048 * 640 * 2;
constexpr size_t WS_G = WS_OB + (size_t)2048 * 640 * 2;

#define STAMP(kid, i) do {} while (0)
#define SBAR() __builtin_amdgcn_sched_barrier(0)
#define PIN(x) asm volatile("" : "+v"(x))
#define MFMA32(a, b, c) __builtin_amdgcn_mfma_f32_32x32x16_bf16(__builtin_bit_cast(bfv8, a), __builtin_bit_cast(bfv8, b), c, 0, 0, 0)
#define MFMA8(a, b, c) __builtin_amdgcn_mfma_f32_32x32x8bf16_1k(a, b, c, 0, 0, 0)
#define MFMA16(a, b, c) __builtin_amdgcn_mfma_f32_16x16x32_bf16(__builtin_bit_cast(bfv8, a), __builtin_bit_cast(bfv8, b), c, 0, 0, 0)
#define WAIT_BAR(N) asm volatile("s_waitcnt vmcnt(" #N ") lgkmcnt(0)\n\ts_barrier" ::: "memory")

__device__ __forceinline__ unsigned cvtpk(float lo, float hi) {
  bfv2 r; r[0] = (__bf16)lo; r[1] = (__bf16)hi;
  return __builtin_bit_cast(unsigned, r);
}
__device__ __forceinline__ float bf_lo(unsigned u) { return __uint_as_float(u << 16); }
__device__ __forceinline__ float bf_hi(unsigned u) { return __uint_as_float(u & 0xffff0000u); }
__device__ __forceinline__ float dot2(unsigned a, unsigned b, float c) {
  return __builtin_amdgcn_fdot2_f32_bf16(__builtin_bit_cast(bfv2, a), __builtin_bit_cast(bfv2, b), c, false);
}

__device__ __forceinline__ void glds16(const void* g, unsigned lds_base) {
  unsigned sv;
  asm volatile("s_mov_b32 %0, m0\n\ts_mov_b32 m0, %2\n\ts_nop 0\n\tglobal_load_lds_dwordx4 %1, off\n\ts_mov_b32 m0, %0"
               : "=&s"(sv) : "v"(g), "s"(lds_base) : "memory");
}
typedef short v4i16_t __attribute__((ext_vector_type(4)));
__device__ __forceinline__ s16x4 trd(lds_cptr p) {
  return __builtin_bit_cast(s16x4, __builtin_amdgcn_ds_read_tr16_b64_v4i16((LAS v4i16_t*)p));
}
__device__ __forceinline__ bf16x8 cat8(s16x4 a, s16x4 b) { return __builtin_shufflevector(a, b, 0, 1, 2, 3, 4, 5, 6, 7); }
__device__ __forceinline__ bf16x8 ldsr128(lds_cptr p) { return *(const LAS bf16x8*)p; }
__device__ __forceinline__ s16x4 ldsr64(lds_cptr p) { return *(const LAS s16x4*)p; }
__device__ __forceinline__ u32x4 pack8(const f32x16& a, int s, float sc) {
  return u32x4{cvtpk(a[8 * s] * sc, a[8 * s + 1] * sc), cvtpk(a[8 * s + 2] * sc, a[8 * s + 3] * sc),
               cvtpk(a[8 * s + 4] * sc, a[8 * s + 5] * sc), cvtpk(a[8 * s + 6] * sc, a[8 * s + 7] * sc)};
}
__device__ __forceinline__ int swap23(int r) { return (r & 0x13) | ((r & 4) << 1) | ((r & 8) >> 1); }

__device__ __forceinline__ unsigned o_off(int token, int k0) {
  const int ks = k0 >> 5, kk = k0 & 31;
  const int g = 2 * (kk >> 4) + ((kk >> 2) & 1), t = (kk >> 3) & 1;
  return (unsigned)((((token >> 4) * 20 + ks) * 64 + (token & 15) + 16 * g) * 16 + 8 * t);
}

constexpr int G1_STAGE = 32768, G1_NBUF = 4, G1_LDS = G1_NBUF * G1_STAGE;

template <bool VT>
__device__ __forceinline__ void g1_body(const char* x, const char* wA, const char* wB, char* ws, char* lds, int ti, int cj) {
  const int tid = threadIdx.x, lane = tid & 63;
  const int w = __builtin_amdgcn_readfirstlane(tid >> 6);
  const unsigned lds0 = (unsigned)(uintptr_t)lds;
  const int rotB = (ti + 2 * cj) % 10, rotA = rotB % 5;
  const char* xs[2];
#pragma unroll
  for (int i = 0; i < 2; ++i) {
    const int row = 16 * w + 8 * i + (lane >> 3);
    xs[i] = x + (size_t)(128 * ti + row) * (D_ * 2) + (((lane & 7) ^ ((row >> 1) & 7)) * 16);
  }
  const unsigned wlo = (unsigned)((lane >> 4) * (N3_ * 2) + cj * 256 + (((lane & 15) ^ (4 * (lane >> 4))) * 16));
  auto issue = [&](int st) {
    const unsigned base = lds0 + (st & (G1_NBUF - 1)) * G1_STAGE;
    if (st < 10) {
      int kb = st + rotB; if (kb >= 10) kb -= 10;
      const char* wp = wB + (size_t)(kb * 64 + 8 * w) * (N3_ * 2) + wlo;
      glds16(wp, base + 16384 + (2 * w) * 1024);
      glds16(wp + 4 * N3_ * 2, base + 16384 + (2 * w + 1) * 1024);
      glds16(xs[0] + kb * 128, base + (2 * w) * 1024);
      glds16(xs[1] + kb * 128, base + (2 * w + 1) * 1024);
    } else {
      const int u = st - 10;
#pragma unroll
      for (int hlf = 0; hlf < 2; ++hlf) {
        int kb = 2 * u + hlf + rotB; if (kb >= 10) kb -= 10;
        const char* wp = wA + (size_t)(kb * 64 + 8 * w) * (N3_ * 2) + wlo;
        glds16(wp, base + hlf * 16384 + (2 * w) * 1024);
        glds16(wp + 4 * N3_ * 2, base + hlf * 16384 + (2 * w + 1) * 1024);
      }
    }
  };
  (void)rotA;
  issue(0); issue(1); issue(2);

  const int tg = w & 3, cg = w >> 2;
  const int r = lane & 31, hh = lane >> 5, g16 = (lane >> 4) & 1, q4 = (lane & 15) >> 2, p4 = lane & 3;
  const int tr = VT ? swap23(r) : r;
  unsigned xoff[4];
#pragma unroll
  for (int ks = 0; ks < 4; ++ks) xoff[ks] = (unsigned)((32 * tg + tr) * 128 + (((2 * ks + hh) ^ ((tr >> 1) & 7)) * 16));
  const int psw = ((p4 & 1) << 1) | (p4 >> 1);
  unsigned woffB[2], woffA[2];
#pragma unroll
  for (int t2 = 0; t2 < 2; ++t2) {
    const unsigned rb = (unsigned)((8 * hh + q4) * 256 + (((2 * cg + t2) ^ q4) * 64) + 32 * g16);
    woffB[t2] = rb + 8 * (VT ? p4 : psw);
    woffA[t2] = rb + 8 * psw;
  }
  bf16x8 xf[40];
  f32x16 acc[2];
  acc[0] = f32x16{}; acc[1] = f32x16{};
#pragma unroll
  for (int st = 0; st < 10; ++st) {
    WAIT_BAR(8);
    STAMP(0, 1 + st);
    issue(st + 3);
    const lds_cptr sb = (lds_cptr)lds + (st & (G1_NBUF - 1)) * G1_STAGE;
#pragma unroll
    for (int ks = 0; ks < 4; ++ks) {
      xf[st * 4 + ks] = ldsr128(sb + xoff[ks]);
#pragma unroll
      for (int t2 = 0; t2 < 2; ++t2) {
        const bf16x8 wf = cat8(trd(sb + 16384 + woffB[t2] + ks * 4096), trd(sb + 16384 + woffB[t2] + ks * 4096 + 1024));
        acc[t2] = VT ? MFMA32(xf[st * 4 + ks], wf, acc[t2]) : MFMA32(wf, xf[st * 4 + ks], acc[t2]);
      }
    }
  }
  STAMP(0, 11);
  const int cs = (cj % 5) * 128 + 64 * cg;
  if (VT) {
#pragma unroll
    for (int t2 = 0; t2 < 2; ++t2) {
      const int F = cs + 32 * t2 + r, hd = F / 20, d = F - 20 * hd;
      char* dst = ws + WS_VB + (size_t)(hd * 64 + 4 * ti + tg) * VTILE + hh * VRUN + d * 16;
#pragma unroll
      for (int s = 0; s < 2; ++s) {
        *(u32x4*)(dst + 2 * s * VRUN) = pack8(acc[t2], s, 1.f);
        if (d == 19) *(u32x4*)(dst + 2 * s * VRUN + 16) = u32x4{0x3f803f80u, 0x3f803f80u, 0x3f803f80u, 0x3f803f80u};
      }
    }
  } else {
    const float sc = (cj < 5) ? QSCALE : 1.f;
    char* dst = ws + WS_QKB + (size_t)(cj * 16 + 8 * cg + hh) * UNIT + (size_t)(128 * ti + 32 * tg + r) * 16;
#pragma unroll
    for (int t2 = 0; t2 < 2; ++t2)
#pragma unroll
      for (int s = 0; s < 2; ++s) *(u32x4*)(dst + (size_t)(4 * t2 + 2 * s) * UNIT) = pack8(acc[t2], s, sc);
  }
  acc[0] = f32x16{}; acc[1] = f32x16{};
#pragma unroll
  for (int u = 0; u < 5; ++u) {
    const int st = 10 + u;
    if (u < 3) { WAIT_BAR(8); } else if (u == 3) { WAIT_BAR(4); } else { WAIT_BAR(0); }
    if (st + 3 < 15) issue(st + 3);
    const lds_cptr sb = (lds_cptr)lds + (st & (G1_NBUF - 1)) * G1_STAGE;
#pragma unroll
    for (int ks = 0; ks < 8; ++ks) {
#pragma unroll
      for (int t2 = 0; t2 < 2; ++t2) {
        const bf16x8 wf = cat8(trd(sb + woffA[t2] + ks * 4096), trd(sb + woffA[t2] + ks * 4096 + 1024));
        acc[t2] = MFMA32(wf, xf[u * 8 + ks], acc[t2]);
      }
    }
  }
  STAMP(0, 12);
  {
    char* dst = ws + WS_QKVA + (size_t)(cj * 16 + 8 * cg + hh) * UNIT + (size_t)(128 * ti + 32 * tg + tr) * 16;
#pragma unroll
    for (int t2 = 0; t2 < 2; ++t2)
#pragma unroll
      for (int s = 0; s < 2; ++s) *(u32x4*)(dst + (size_t)(4 * t2 + 2 * s) * UNIT) = pack8(acc[t2], s, 1.f);
  }
  STAMP(0, 13);
}

__global__ __launch_bounds__(512, 2) void k1_qkv(const char* __restrict__ x, const char* __restrict__ wA,
                                                 const char* __restrict__ wB, const char* __restrict__ gw,
                                                 char* __restrict__ ws) {
  extern __shared__ __attribute__((aligned(16))) char lds[];
  const int xcd = blockIdx.x & 7, slot = blockIdx.x >> 3;
  const int ti = 8 * (xcd & 1) + (slot & 7), cj = 4 * (xcd >> 1) + (slot >> 3);
  STAMP(0, 0);
  if (cj < 10) g1_body<false>(x, wA, wB, ws, lds, ti, cj);
  else if (cj < 15) g1_body<true>(x, wA, wB, ws, lds, ti, cj);
  else {
    const int lane = threadIdx.x & 63, w = threadIdx.x >> 6;
    unsigned gg[5];
#pragma unroll
    for (int i = 0; i < 5; ++i) gg[i] = ((const unsigned*)gw)[i * 64 + lane];
#pragma unroll 4
    for (int t = 0; t < 16; ++t) {
      const int tok = 128 * ti + 16 * w + t;
      const unsigned* xp = (const unsigned*)(x + (size_t)tok * (D_ * 2));
      float s = 0.f;
#pragma unroll
      for (int i = 0; i < 5; ++i) s = dot2(xp[i * 64 + lane], gg[i], s);
#pragma unroll
      for (int o = 32; o > 0; o >>= 1) s += __shfl_xor(s, o);
      if (lane == 0) ((float*)(ws + WS_G))[tok] = 1.f / (1.f + __expf(-s));
    }
  }
}

constexpr int AT_HALF = 12288, AT_STAGE = 2 * AT_HALF, AT_NBUF = 3;
constexpr int AT_LA = AT_NBUF * AT_STAGE, AT_FLAG = AT_LA + 6 * 5120, AT_LDS = AT_FLAG + 64;

__device__ __forceinline__ f32x16 smf(bf16x8 a, const f32x16& p, int s, f32x16 o, int idx) {
  f32x8 b = s ? __builtin_shufflevector(p, p, 8, 9, 10, 11, 12, 13, 14, 15) : __builtin_shufflevector(p, p, 0, 1, 2, 3, 4, 5, 6, 7);
  return __builtin_amdgcn_smfmac_f32_32x32x32_bf16(__builtin_bit_cast(bfv8, a), __builtin_bit_cast(bfv16, b), o, idx, 0, 0);
}
__device__ __forceinline__ float max16(const f32x16& s) {
  float a = fmaxf(fmaxf(s[0], s[1]), s[2]), b = fmaxf(fmaxf(s[3], s[4]), s[5]);
  a = fmaxf(fmaxf(a, s[6]), s[7]); b = fmaxf(fmaxf(b, s[8]), s[9]);
  a = fmaxf(fmaxf(a, s[10]), s[11]); b = fmaxf(fmaxf(b, s[12]), s[13]);
  a = fmaxf(fmaxf(a, s[14]), s[15]);
  return fmaxf(a, b);
}
__device__ __forceinline__ float swap_max(float m) {
  auto rr = __builtin_amdgcn_permlane32_swap(__float_as_uint(m), __float_as_uint(m), false, false);
  return fmaxf(__uint_as_float(rr[0]), __uint_as_float(rr[1]));
}
__device__ __forceinline__ float swap_add(float m) {
  auto rr = __builtin_amdgcn_permlane32_swap(__float_as_uint(m), __float_as_uint(m), false, false);
  return __uint_as_float(rr[0]) + __uint_as_float(rr[1]);
}
__device__ __forceinline__ f32x16 splat16(float v) { f32x16 r; for (int i = 0; i < 16; ++i) r[i] = v; return r; }

__global__ __launch_bounds__(512, 2) void k2_attn(char* __restrict__ ws) {
  extern __shared__ __attribute__((aligned(16))) char lds[];
  const int tid = threadIdx.x, lane = tid & 63;
  const int w = __builtin_amdgcn_readfirstlane(tid >> 6);
  const int xcd = blockIdx.x & 7, slot = blockIdx.x >> 3;
  const int h = 4 * xcd + (slot & 3), c = slot >> 2;
  const int e = h & 1, ua = 5 * (h >> 1) + 3 * e, us = 5 * (h >> 1) + 2;
  const unsigned lds0 = (unsigned)(uintptr_t)lds;
  const int kh = w >> 2, tq = w & 3, hh = lane >> 5, r = lane & 31;
  STAMP(1, 0);
  const char* src[3]; int adv;
  if (tq < 2) {
#pragma unroll
    for (int i = 0; i < 3; ++i) {
      const int p = 3 * tq + i, u = p >> 1;
      src[i] = ws + WS_QKB + (size_t)(80 + (u == 2 ? us : ua + u)) * UNIT + (size_t)(1024 * kh + 64 * (p & 1) + lane) * 16;
    }
    adv = 128 * 16;
  } else {
#pragma unroll
    for (int i = 0; i < 3; ++i)
      src[i] = ws + WS_VB + (size_t)(h * 64 + 32 * kh) * VTILE + (3 * (tq - 2) + i) * 1024 + lane * 16;
    adv = 4 * VTILE;
  }
  auto issue = [&](int st, int buf) {
#pragma unroll
    for (int i = 0; i < 3; ++i) glds16(src[i] + st * adv, lds0 + buf * AT_STAGE + kh * AT_HALF + (3 * tq + i) * 1024);
  };
  for (int p = w; p < 30; p += 8) {
    const int u = p / 5, pc = p - 5 * u, u3 = u % 3;
    const int unit = 80 * (1 + u / 3) + (u3 == 2 ? us : ua + u3);
    glds16(ws + WS_QKVA + (size_t)unit * UNIT + (ptrdiff_t)(256 * c - 64 + 64 * pc + lane) * 16, lds0 + AT_LA + p * 1024);
  }
  issue(0, 0); issue(1, 1); issue(2, 2);

  bf16x8 q0[2]; s16x4 q1[2];
#pragma unroll
  for (int t = 0; t < 2; ++t) {
    const size_t to = (size_t)(256 * c + 64 * tq + 32 * t + r) * 16;
    q0[t] = *(const bf16x8*)(ws + WS_QKB + (size_t)(ua + hh) * UNIT + to);
    const s16x4 tl = *(const s16x4*)(ws + WS_QKB + (size_t)us * UNIT + to + 8 * e);
    q1[t] = (hh == e) ? tl : s16x4{0, 0, 0, 0};
  }
  {
    const int tl = 32 * w + r, tok = 256 * c + tl;
    const u32x4 qa = *(const u32x4*)(ws + WS_QKVA + (size_t)(ua + hh) * UNIT + (size_t)tok * 16);
    const unsigned qt = *(const unsigned*)(ws + WS_QKVA + (size_t)us * UNIT + (size_t)tok * 16 + 8 * e + 4 * hh);
    WAIT_BAR(9);
    STAMP(1, 1);
    const lds_cptr la = (lds_cptr)lds + AT_LA;
    float lg[8];
    u32x4 vm[8]; unsigned vt[8];
#pragma unroll
    for (int j = 0; j < 8; ++j) {
      const int kt = tok - 2 * j;
      const unsigned ro = (unsigned)(((kt < 0 ? 0 : kt) - 256 * c + 64) * 16);
      const u32x4 km = *(const LAS u32x4*)(la + hh * 5120 + ro);
      const unsigned ktl = *(const LAS unsigned*)(la + 2 * 5120 + ro + 8 * e + 4 * hh);
      vm[j] = *(const LAS u32x4*)(la + (3 + hh) * 5120 + ro);
      vt[j] = *(const LAS unsigned*)(la + 5 * 5120 + ro + 8 * e + 4 * hh);
      float s = dot2(qa[0], km[0], 0.f);
      s = dot2(qa[1], km[1], s); s = dot2(qa[2], km[2], s); s = dot2(qa[3], km[3], s); s = dot2(qt, ktl, s);
      s = swap_add(s);
      lg[j] = (kt < 0) ? -1e9f * 1.4426950408889634f : s * QSCALE;
    }
    float m = lg[0];
#pragma unroll
    for (int j = 1; j < 8; ++j) m = fmaxf(m, lg[j]);
    float l = 0.f;
#pragma unroll
    for (int j = 0; j < 8; ++j) { lg[j] = __builtin_amdgcn_exp2f(lg[j] - m); l += lg[j]; }
    const float rl = __builtin_amdgcn_rcpf(l);
    float o[10];
#pragma unroll
    for (int i = 0; i < 10; ++i) o[i] = 0.f;
#pragma unroll
    for (int j = 0; j < 8; j += 2) {
      const unsigned pp = cvtpk(lg[j] * rl, lg[j + 1] * rl);
#pragma unroll
      for (int u = 0; u < 5; ++u) {
        const unsigned a = u < 4 ? vm[j][u & 3] : vt[j], b = u < 4 ? vm[j + 1][u & 3] : vt[j + 1];
        o[2 * u] = dot2(pp, __builtin_amdgcn_perm(b, a, 0x05040100u), o[2 * u]);
        o[2 * u + 1] = dot2(pp, __builtin_amdgcn_perm(b, a, 0x07060302u), o[2 * u + 1]);
      }
    }
    char* od = ws + WS_OA;
    const int km0 = 8 * (ua + hh), kt0 = 8 * us + 4 * e;
    *(u32x2*)(od + o_off(tok, km0)) = u32x2{cvtpk(o[0], o[1]), cvtpk(o[2], o[3])};
    *(u32x2*)(od + o_off(tok, km0 + 4)) = u32x2{cvtpk(o[4], o[5]), cvtpk(o[6], o[7])};
    *(unsigned*)(od + o_off(tok, kt0) + 4 * hh) = cvtpk(o[8], o[9]);
  }
  STAMP(1, 2);

  int idx = 0xDDDD; PIN(idx);
  volatile LAS unsigned* flag = (volatile LAS unsigned*)((lds_ptr)lds + AT_FLAG);
  const unsigned ko0 = (unsigned)(hh * 2048 + r * 16), ko1 = (unsigned)(2 * 2048 + r * 16 + 8 * hh);
  const unsigned vo = (unsigned)(6144 + hh * VRUN + r * 16);
  float ofin[12];
  f32x16 ca, cb;
  for (int attempt = 0;; ++attempt) {
    if (tid == 0) *flag = 0u;
    if (attempt == 0) { WAIT_BAR(9); } else { WAIT_BAR(6); }
    if (attempt == 0) {
      const bf16x8 k0 = ldsr128((lds_cptr)lds + ko0); const s16x4 k1 = ldsr64((lds_cptr)lds + ko1);
      f32x16 s = MFMA32(k0, q0[0], f32x16{}); s = MFMA8(k1, q1[0], s);
      ca = splat16(-swap_max(max16(s)));
      s = MFMA32(k0, q0[1], f32x16{}); s = MFMA8(k1, q1[1], s);
      cb = splat16(-swap_max(max16(s)));
    }
    PIN(ca); PIN(cb);
    STAMP(1, 3);
    f32x16 oa = f32x16{}, ob = f32x16{}, sa, sb;
    const lds_cptr fb = (lds_cptr)lds + kh * AT_HALF;
    bf16x8 k0 = ldsr128(fb + ko0); s16x4 k1 = ldsr64(fb + ko1);
    bf16x8 v0 = ldsr128(fb + vo), v1 = ldsr128(fb + vo + 2 * VRUN);
    bf16x8 v1p = v1;
    sa = MFMA32(k0, q0[0], ca); sa = MFMA8(k1, q1[0], sa);
    sb = f32x16{};
#define EX(S, j) S[j] = __builtin_amdgcn_exp2f(S[j])
#define EX2(S, j) do { EX(S, j); EX(S, j + 1); } while (0)
#define EX4(S, j) do { EX(S, j); EX(S, j + 1); EX(S, j + 2); EX(S, j + 3); } while (0)
    int buf = 0;
#pragma unroll 1
    for (int st = 0; st < 8; ++st) {
      const lds_cptr sb0 = fb + buf * AT_STAGE;
      const int nbuf = (buf == AT_NBUF - 1) ? 0 : buf + 1;
      const lds_cptr sn0 = fb + nbuf * AT_STAGE;
#pragma unroll
      for (int i = 0; i < 4; ++i) {
        const lds_cptr nk = (i < 3) ? (sb0 + (i + 1) * 512) : sn0;
        const lds_cptr nv = (i < 3) ? (sb0 + (i + 1) * VTILE) : sn0;
        SBAR();
        EX2(sa, 0); PIN(sa); SBAR();
        ob = smf(v1p, sb, 1, ob, idx); EX2(sa, 2); PIN(sa); SBAR();
        sb = MFMA32(k0, q0[1], cb); EX4(sa, 4); PIN(sa); SBAR();
        sb = MFMA8(k1, q1[1], sb);
        if (i == 3) {
          if (st < 6) { WAIT_BAR(3); } else { WAIT_BAR(0); }
          if (st + 3 < 8) issue(st + 3, buf);
        }
        k0 = ldsr128(nk + ko0); k1 = ldsr64(nk + ko1);
        EX4(sa, 8); PIN(sa); SBAR();
        oa = smf(v0, sa, 0, oa, idx); EX4(sa, 12); PIN(sa); SBAR();
        EX2(sb, 0); PIN(sb); SBAR();
        oa = smf(v1, sa, 1, oa, idx); EX2(sb, 2); PIN(sb); SBAR();
        sa = MFMA32(k0, q0[0], ca); EX4(sb, 4); PIN(sb); SBAR();
        sa = MFMA8(k1, q1[0], sa); EX4(sb, 8); PIN(sb); SBAR();
        ob = smf(v0, sb, 0, ob, idx);
        v1p = v1;
        v0 = ldsr128(nv + vo); v1 = ldsr128(nv + vo + 2 * VRUN);
        EX4(sb, 12); PIN(sb); SBAR();
      }
      buf = nbuf;
      STAMP(1, 4 + st);
    }
    ob = smf(v1p, sb, 1, ob, idx);
    WAIT_BAR(0);
    {
      float* xw = (float*)(lds + (size_t)(tq * 2 + kh) * 3072) + lane;
      const float* xr = (const float*)(lds + (size_t)(tq * 2 + (kh ^ 1)) * 3072) + lane;
#pragma unroll
      for (int i = 0; i < 12; ++i) xw[i * 64] = kh ? oa[i] : ob[i];
      __syncthreads();
#pragma unroll
      for (int i = 0; i < 12; ++i) ofin[i] = (kh ? ob[i] : oa[i]) + xr[i * 64];
    }
    auto rr = __builtin_amdgcn_permlane32_swap(__float_as_uint(ofin[8]), __float_as_uint(ofin[8]), false, false);
    const float l = __uint_as_float(rr[1]);
    const bool bad = !(l > 8.67e-19f && l < 1.15e18f);
    const float rl = 1.f / l;
#pragma unroll
    for (int i = 0; i < 12; ++i) ofin[i] *= rl;
    if (attempt == 1) break;
    if (bad) *flag = 1u;
    __syncthreads();
    if (*flag == 0u) break;
    __syncthreads();
    {
      float ma = -INFINITY, mb = -INFINITY;
      for (int kt = 0; kt < 64; ++kt) {
        const size_t to = (size_t)(32 * kt + r) * 16;
        const bf16x8 a0 = *(const bf16x8*)(ws + WS_QKB + (size_t)(80 + ua + hh) * UNIT + to);
        const s16x4 a1 = *(const s16x4*)(ws + WS_QKB + (size_t)(80 + us) * UNIT + to + 8 * hh);
        f32x16 s = MFMA32(a0, q0[0], f32x16{}); s = MFMA8(a1, q1[0], s);
        ma = fmaxf(ma, max16(s));
        s = MFMA32(a0, q0[1], f32x16{}); s = MFMA8(a1, q1[1], s);
        mb = fmaxf(mb, max16(s));
      }
      ca = splat16(-swap_max(ma)); cb = splat16(-swap_max(mb));
    }
    issue(0, 0); issue(1, 1); issue(2, 2);
  }
  {
    const int tok = 256 * c + 64 * tq + 32 * kh + r;
    char* od = ws + WS_OB;
    const int kb = 20 * h + 4 * hh;
    *(u32x2*)(od + o_off(tok, kb)) = u32x2{cvtpk(ofin[0], ofin[1]), cvtpk(ofin[2], ofin[3])};
    *(u32x2*)(od + o_off(tok, kb + 8)) = u32x2{cvtpk(ofin[4], ofin[5]), cvtpk(ofin[6], ofin[7])};
    if (!hh) *(u32x2*)(od + o_off(tok, kb + 16)) = u32x2{cvtpk(ofin[8], ofin[9]), cvtpk(ofin[10], ofin[11])};
  }
  STAMP(1, 12);
}

constexpr int OP_HALF = 128 * 160, OP_STAGE = 2 * OP_HALF, OP_LDS = 3 * OP_STAGE;

__global__ __launch_bounds__(512, 2) void k3_out(const char* __restrict__ woA, const char* __restrict__ woB,
                                                 char* __restrict__ ws, char* __restrict__ out) {
  extern __shared__ __attribute__((aligned(16))) char lds[];
  const int tid = threadIdx.x, lane = tid & 63;
  const int w = __builtin_amdgcn_readfirstlane(tid >> 6);
  const int xcd = blockIdx.x & 7, slot = blockIdx.x >> 3;
  const int rt = 8 * (xcd >> 1) + (slot & 7), ct = 4 * (xcd & 1) + (slot >> 3);
  const unsigned lds0 = (unsigned)(uintptr_t)lds;
  const int br = w >> 2, rq = w & 3;
  const char* wsrc[5];
#pragma unroll
  for (int i = 0; i < 5; ++i) {
    const int n = (rq * 5 + i) * 64 + lane, row = n / 10, pc = n % 10;
    wsrc[i] = (br ? woB : woA) + (size_t)row * (D_ * 2) + ct * 160 + pc * 16;
  }
  auto issue = [&](int st, int buf) {
#pragma unroll
    for (int i = 0; i < 5; ++i)
      glds16(wsrc[i] + (size_t)st * 128 * (D_ * 2), lds0 + buf * OP_STAGE + br * OP_HALF + (rq * 5 + i) * 1024);
  };
  STAMP(2, 0);
  issue(0, 0);
  bf16x8 of[20];
  const char* osrc = ws + (br ? WS_OB : WS_OA) + (size_t)(rt * 4 + rq) * 20 * 1024 + lane * 16;
#pragma unroll
  for (int ks = 0; ks < 20; ++ks) of[ks] = *(const bf16x8*)(osrc + ks * 1024);
  const float gate = ((const float*)(ws + WS_G))[rt * 64 + rq * 16 + (lane & 15)];
  issue(1, 1); issue(2, 2);

  const int g = lane >> 4, q4 = (lane & 15) >> 2, p4 = lane & 3;
  const unsigned woff = (unsigned)(br * OP_HALF + (16 * (g >> 1) + 4 * (g & 1) + q4) * 160 + 8 * p4);
  f32x4 acc[5];
#pragma unroll
  for (int i = 0; i < 5; ++i) acc[i] = f32x4{};
#pragma unroll
  for (int st = 0; st < 5; ++st) {
    if (st == 0) { WAIT_BAR(10); } else if (st < 4) { WAIT_BAR(5); } else { WAIT_BAR(0); }
    STAMP(2, 1 + st);
    if (st >= 1 && st + 2 < 5) issue(st + 2, (st + 2) % 3);
    const lds_cptr sb = (lds_cptr)lds + (st % 3) * OP_STAGE + woff;
#pragma unroll
    for (int k4 = 0; k4 < 4; ++k4) {
#pragma unroll
      for (int n = 0; n < 5; ++n) {
        const bf16x8 wf = cat8(trd(sb + k4 * 32 * 160 + n * 32), trd(sb + (k4 * 32 + 8) * 160 + n * 32));
        acc[n] = MFMA16(wf, of[st * 4 + k4], acc[n]);
      }
    }
  }
  WAIT_BAR(0);
  STAMP(2, 6);
  float* xch = (float*)lds + rq * 20 * 64 + lane;
  if (br == 0) {
#pragma unroll
    for (int n = 0; n < 5; ++n)
#pragma unroll
      for (int j = 0; j < 4; ++j) xch[(n * 4 + j) * 64] = acc[n][j];
  }
  __syncthreads();
  if (br == 1) {
    char* op = out + (size_t)(rt * 64 + rq * 16 + (lane & 15)) * (D_ * 2) + ct * 160 + g * 8;
#pragma unroll
    for (int n = 0; n < 5; ++n) {
      float v[4];
#pragma unroll
      for (int j = 0; j < 4; ++j) v[j] = gate * xch[(n * 4 + j) * 64] + (1.f - gate) * acc[n][j];
      *(u32x2*)(op + n * 32) = u32x2{cvtpk(v[0], v[1]), cvtpk(v[2], v[3])};
    }
  }
  STAMP(2, 7);
}

extern "C" void kernel_launch(void* const* d_in, const int* in_sizes, int n_in, void* d_out, int out_size, void* d_ws,
                              size_t ws_size, hipStream_t stream) {
  const char* x = (const char*)d_in[0];
  const char* wA = (const char*)d_in[1];
  const char* woA = (const char*)d_in[2];
  const char* wB = (const char*)d_in[4];
  const char* woB = (const char*)d_in[5];
  const char* gw = (const char*)d_in[6];
  (void)hipFuncSetAttribute((const void*)k1_qkv, hipFuncAttributeMaxDynamicSharedMemorySize, G1_LDS);
  (void)hipFuncSetAttribute((const void*)k2_attn, hipFuncAttributeMaxDynamicSharedMemorySize, AT_LDS);
  (void)hipFuncSetAttribute((const void*)k3_out, hipFuncAttributeMaxDynamicSharedMemorySize, OP_LDS);
  k1_qkv<<<256, 512, G1_LDS, stream>>>(x, wA, wB, gw, (char*)d_ws);
  k2_attn<<<256, 512, AT_LDS, stream>>>((char*)d_ws);
  k3_out<<<256, 512, OP_LDS, stream>>>(woA, woB, (char*)d_ws, (char*)d_out);
}
```

```cpp
#include <hip/hip_runtime.h>
#include <hip/hip_bf16.h>
#include <stdint.h>

typedef short bf16x8 __attribute__((ext_vector_type(8)));
typedef short s16x4 __attribute__((ext_vector_type(4)));
typedef __bf16 bfv8 __attribute__((ext_vector_type(8)));
typedef __bf16 bfv2 __attribute__((ext_vector_type(2)));
typedef __bf16 bfv16 __attribute__((ext_vector_type(16)));
typedef float f32x16 __attribute__((ext_vector_type(16)));
typedef float f32x8 __attribute__((ext_vector_type(8)));
typedef float f32x4 __attribute__((ext_vector_type(4)));
typedef unsigned u32x4 __attribute__((ext_vector_type(4)));
typedef unsigned u32x2 __attribute__((ext_vector_type(2)));
typedef unsigned long long u64;
#define LAS __attribute__((address_space(3)))
#define GAS __attribute__((address_space(1)))
typedef LAS const char* lds_cptr;
typedef LAS char* lds_ptr;
typedef GAS u64 gu64;

constexpr int D_ = 640, N3_ = 1920;
constexpr u64 TAG_STEP = 0x9E3779B97F4A7C15ull;
constexpr float QSCALE = 0.22360679774997896f * 1.4426950408889634f;

constexpr unsigned UNIT = 2048 * 16;
constexpr unsigned WS_FLG = 0;
constexpr unsigned F1B = 256, F1A = 512, FG = 768, FOA = 1024;
constexpr unsigned WS_QKB = 2048 * 128;
constexpr unsigned WS_VB = WS_QKB + 160 * UNIT;
constexpr unsigned WS_QKVA = WS_VB + 32 * 64 * 2048;
constexpr unsigned WS_OA = WS_QKVA + 240 * UNIT;
constexpr unsigned WS_OB = WS_OA + 2048 * 640 * 2;
constexpr unsigned WS_G = WS_OB + 2048 * 640 * 2;
constexpr unsigned WS_END = WS_G + 2048 * 4;

constexpr int G1_STAGE = 32768;
constexpr int L_R = 0;
constexpr int L_RING = 106496, AT_HALF = 14336, AT_STAGE = 2 * AT_HALF;
constexpr int L_FL = 104448, L_EP = 131072, L_Q = 76864;
constexpr int L_LA = 76864, LA_UNIT = 4352;
constexpr int LDS_BYTES = 163840;

#define STAMP(kid, i) do {} while (0)
#define DBG_DECL
#define DBG_ARG
#define DBG_PASS
#define DBG_OUT() do {} while (0)
#define AB_NOEXTRA 0
#define AB_NOWO 0
#define SBAR() __builtin_amdgcn_sched_barrier(0)
#define PIN(x) asm volatile("" : "+v"(x))
#define MFMA32(a, b, c) __builtin_amdgcn_mfma_f32_32x32x16_bf16(__builtin_bit_cast(bfv8, a), __builtin_bit_cast(bfv8, b), c, 0, 0, 0)
#define MFMA8(a, b, c) __builtin_amdgcn_mfma_f32_32x32x8bf16_1k(a, b, c, 0, 0, 0)
#define MFMA16(a, b, c) __builtin_amdgcn_mfma_f32_16x16x32_bf16(__builtin_bit_cast(bfv8, a), __builtin_bit_cast(bfv8, b), c, 0, 0, 0)
#define WAIT_BAR(N) asm volatile("s_waitcnt vmcnt(" #N ") lgkmcnt(0)\n\ts_barrier" ::: "memory")
#define WAITV_BAR(N) asm volatile("s_waitcnt vmcnt(" #N ")\n\ts_barrier" ::: "memory")
#define RLX_AGENT __ATOMIC_RELAXED, __HIP_MEMORY_SCOPE_AGENT
#define SC1 16

template <int V> struct IC { static constexpr int value = V; };
template <int I, int N, class F> __device__ __forceinline__ void sfor(F&& f) { if constexpr (I < N) { f(IC<I>{}); sfor<I + 1, N>(f); } }

__device__ __forceinline__ unsigned cvtpk(float lo, float hi) {
  bfv2 r; r[0] = (__bf16)lo; r[1] = (__bf16)hi;
  return __builtin_bit_cast(unsigned, r);
}
__device__ __forceinline__ float dot2(unsigned a, unsigned b, float c) {
  return __builtin_amdgcn_fdot2_f32_bf16(__builtin_bit_cast(bfv2, a), __builtin_bit_cast(bfv2, b), c, false);
}
template <int OFF> __device__ __forceinline__ void dma(const char* base, unsigned voff, unsigned lds_base) {
  asm volatile("s_add_u32 m0, %2, %3\n\ts_nop 0\n\tglobal_load_lds_dwordx4 %0, %1" :: "v"(voff), "s"(base), "s"(lds_base), "i"(OFF) : "memory", "scc");
}
template <int OFF> __device__ __forceinline__ void dma_sc1(const char* base, unsigned voff, unsigned lds_base) {
  asm volatile("s_add_u32 m0, %2, %3\n\ts_nop 0\n\tglobal_load_lds_dwordx4 %0, %1 sc1" :: "v"(voff), "s"(base), "s"(lds_base), "i"(OFF) : "memory", "scc");
}
template <int OFF> __device__ __forceinline__ void dma2_sc1(const char* base, unsigned voff, unsigned lds_base) {
  asm volatile("s_add_u32 m0, %2, %3\n\ts_nop 0\n\tglobal_load_lds_dwordx4 %0, %1 sc1\n\tglobal_load_lds_dwordx4 %0, %1 offset:1024 sc1"
               :: "v"(voff), "s"(base), "s"(lds_base), "i"(OFF) : "memory", "scc");
}
typedef short v4i16_t __attribute__((ext_vector_type(4)));
__device__ __forceinline__ s16x4 trd(lds_cptr p) {
  return __builtin_bit_cast(s16x4, __builtin_amdgcn_ds_read_tr16_b64_v4i16((LAS v4i16_t*)p));
}
__device__ __forceinline__ bf16x8 cat8(s16x4 a, s16x4 b) { return __builtin_shufflevector(a, b, 0, 1, 2, 3, 4, 5, 6, 7); }
__device__ __forceinline__ bf16x8 ldsr128(lds_cptr p) { return *(const LAS bf16x8*)p; }
__device__ __forceinline__ s16x4 ldsr64(lds_cptr p) { return *(const LAS s16x4*)p; }
__device__ __forceinline__ u32x4 pack8(const f32x16& a, int s, float sc) {
  return u32x4{cvtpk(a[8 * s] * sc, a[8 * s + 1] * sc), cvtpk(a[8 * s + 2] * sc, a[8 * s + 3] * sc),
               cvtpk(a[8 * s + 4] * sc, a[8 * s + 5] * sc), cvtpk(a[8 * s + 6] * sc, a[8 * s + 7] * sc)};
}
__device__ __forceinline__ int swap23(int r) { return (r & 0x13) | ((r & 4) << 1) | ((r & 8) >> 1); }

typedef __amdgpu_buffer_rsrc_t rsrc_t;
__device__ __forceinline__ void st128(rsrc_t rs, unsigned off, u32x4 v) { __builtin_amdgcn_raw_buffer_store_b128(v, rs, off, 0, SC1); }
__device__ __forceinline__ void st64(rsrc_t rs, unsigned off, u32x2 v) { __builtin_amdgcn_raw_buffer_store_b64(v, rs, off, 0, SC1); }
__device__ __forceinline__ u32x4 ld128(rsrc_t rs, unsigned off) { return __builtin_amdgcn_raw_buffer_load_b128(rs, off, 0, SC1); }
__device__ __forceinline__ u32x2 ld64(rsrc_t rs, unsigned off) { return __builtin_amdgcn_raw_buffer_load_b64(rs, off, 0, SC1); }
__device__ __forceinline__ unsigned ld32(rsrc_t rs, unsigned off) { return __builtin_amdgcn_raw_buffer_load_b32(rs, off, 0, SC1); }

__device__ __forceinline__ void wait_flags(gu64* flg, unsigned idx, u64 tag) {
#pragma unroll 1
  for (unsigned spins = 0; spins < (1u << 18); ++spins) {
    const u64 v = __hip_atomic_load(flg + idx * 16, RLX_AGENT);
    if (__all(v == tag)) break;
    __builtin_amdgcn_s_sleep(1);
  }
  asm volatile("" ::: "memory");
}

__device__ __forceinline__ unsigned o_main(int tok, int h, int hh) {
  const int m = h >> 1;
  return (unsigned)((((tok >> 4) * 20 + 10 * (m >> 3) + (m & 7)) * 64 + (2 * (h & 1) + hh) * 16 + (tok & 15)) * 16);
}
__device__ __forceinline__ unsigned o_tail(int tok, int h) {
  const int m = h >> 1;
  return (unsigned)((((tok >> 4) * 20 + 10 * (m >> 3) + 8 + ((m >> 2) & 1)) * 64 + (2 * ((m >> 1) & 1) + (h & 1)) * 16 + (tok & 15)) * 16 + 8 * (m & 1));
}

constexpr int g1_buf(int st) { return (st < 10 ? (st & 3) : st == 10 ? 4 : ((st - 1) & 3)) * G1_STAGE; }
template <bool VT, class FStart>
__device__ __forceinline__ void g1_body(const char* x, const char* wA, const char* wB, char* ws, rsrc_t rs, char* lds,
                                        int ti, int cj, int w, int lane, u64& tag, bool& early, unsigned fiKV, unsigned fiQ, FStart&& att_start DBG_ARG) {
  const unsigned lds0 = (unsigned)(uintptr_t)lds;
#define ROTMODE 0
  const int rot = ROTMODE == 0 ? (ti + 2 * cj) % 10 : ROTMODE == 1 ? 0 : ROTMODE == 2 ? 5 * ((ti + cj) & 1) : 3 * ((ti + cj) % 3);
  unsigned xlo[2], wlo[2];
#pragma unroll
  for (int i = 0; i < 2; ++i) {
    const int row = 16 * w + 8 * i + (lane >> 3);
    xlo[i] = (unsigned)(row * (D_ * 2) + (((lane & 7) ^ ((row >> 1) & 7)) * 16));
    wlo[i] = (unsigned)((8 * w + 4 * i + (lane >> 4)) * (N3_ * 2) + (((lane & 15) ^ (4 * (lane >> 4))) * 16));
  }
  const char* xb = x + (size_t)(128 * ti) * (D_ * 2);
  const char* wBb = wB + cj * 256;
  const char* wAb = wA + cj * 256;
  const unsigned ldw = lds0 + w * 2048;
  auto piece = [&](auto stc, auto pc) __attribute__((always_inline)) {
    constexpr int st = decltype(stc)::value, p = decltype(pc)::value, base = g1_buf(st) + (p & 1) * 1024;
    int kb = (st < 10 ? st : 2 * (st - 10) + (p >> 1)) + rot; if (kb >= 10) kb -= 10;
    if constexpr (st < 10 && p < 2) dma<base + 16384>(wBb + (size_t)kb * (64 * N3_ * 2), wlo[p & 1], ldw);
    else if constexpr (st < 10) dma<base>(xb + kb * 128, xlo[p & 1], ldw);
    else dma<base + (p >> 1) * 16384>(wAb + (size_t)kb * (64 * N3_ * 2), wlo[p & 1], ldw);
  };
  sfor<0, 8>([&](auto pc) __attribute__((always_inline)) { constexpr int p = decltype(pc)::value; piece(IC<(p >> 2)>{}, IC<(p & 3)>{}); });
  if (w == 7) dma_sc1<L_EP>(ws, lane * 16, lds0);
  STAMP(0, 14);

  const int tg = w & 3, cg = w >> 2;
  const int r = lane & 31, hh = lane >> 5, g16 = (lane >> 4) & 1, q4 = (lane & 15) >> 2, p4 = lane & 3;
  const int tr = VT ? swap23(r) : r;
  unsigned xoff[4];
#pragma unroll
  for (int ks = 0; ks < 4; ++ks) xoff[ks] = (unsigned)((32 * tg + tr) * 128 + (((2 * ks + hh) ^ ((tr >> 1) & 7)) * 16));
  const int psw = ((p4 & 1) << 1) | (p4 >> 1);
  unsigned woffB[2], woffA[2];
#pragma unroll
  for (int t2 = 0; t2 < 2; ++t2) {
    const unsigned rb = (unsigned)((8 * hh + q4) * 256 + (((2 * cg + t2) ^ q4) * 64) + 32 * g16);
    woffB[t2] = rb + 8 * (VT ? p4 : psw);
    woffA[t2] = rb + 8 * psw;
  }
  bf16x8 xf[40], wf[3][2];
  u64 fa, fb;
  f32x16 acc[2];
  acc[0] = f32x16{}; acc[1] = f32x16{};
  sfor<0, 82>([&](auto ic) __attribute__((always_inline)) {
    constexpr int nn = decltype(ic)::value, n = nn - 2;
    constexpr int st = nn < 40 ? (nn >> 2) : 10 + ((nn - 40) >> 3), ks = nn < 40 ? (nn & 3) : ((nn - 40) & 7);
    if constexpr (nn < 80) {
      if constexpr (ks == 0) {
        if constexpr (st == 3 || st == 13) { WAIT_BAR(4); }
        else if constexpr (st == 4 || st == 5 || st == 10) { WAITV_BAR(8); }
        else if constexpr (st == 11 && VT) { WAITV_BAR(16); }
        else if constexpr (st == 11) { WAITV_BAR(12); }
        else if constexpr (st == 12) { WAITV_BAR(6); }
        else if constexpr (st < 14) { WAITV_BAR(4); }
        else { WAITV_BAR(0); }
        if constexpr (st == 2) {
          const u32x2 ep = *(const LAS u32x2*)((lds_cptr)lds + L_EP);
          tag = (((u64)(unsigned)__builtin_amdgcn_readfirstlane(ep[1]) << 32) | (unsigned)__builtin_amdgcn_readfirstlane(ep[0])) + TAG_STEP;
        }
        if constexpr (st == 12) { if (w == 0 && lane == 0) __hip_atomic_store((gu64*)ws + (F1B + cj * 16 + ti) * 16, tag, RLX_AGENT); }
        if constexpr (st == 14)
          asm volatile("global_load_dwordx2 %0, %2, %4 sc1\n\tglobal_load_dwordx2 %1, %3, %4 sc1" : "=&v"(fa), "=&v"(fb) : "v"(fiKV * 128), "v"(fiQ * 128), "s"(ws + F1B * 128) : "memory");
        if constexpr (st < 12) STAMP(0, 1 + st);
      }
      const lds_cptr sb = (lds_cptr)lds + g1_buf(st);
      if constexpr (nn < 40) xf[nn] = ldsr128(sb + xoff[ks]);
#pragma unroll
      for (int t2 = 0; t2 < 2; ++t2) {
        const lds_cptr wp = sb + (nn < 40 ? 16384 + woffB[t2] : woffA[t2]) + ks * 4096;
        wf[nn % 3][t2] = cat8(trd(wp), trd(wp + 1024));
      }
      if constexpr (st == 3) { piece(IC<(ks < 2 ? 5 : 10)>{}, IC<2 * (ks & 1)>{}); piece(IC<(ks < 2 ? 5 : 10)>{}, IC<2 * (ks & 1) + 1>{}); }
      else if constexpr (st < 8) piece(IC<st + 2>{}, IC<ks>{});
      else if constexpr (st < 12 && ks < 4) piece(IC<st + 3>{}, IC<ks>{});
    }
    if constexpr (n >= 0) {
#pragma unroll
      for (int t2 = 0; t2 < 2; ++t2)
        acc[t2] = (VT && n < 40) ? MFMA32(xf[n % 40], wf[n % 3][t2], acc[t2]) : MFMA32(wf[n % 3][t2], xf[n % 40], acc[t2]);
    }
    SBAR();
    if constexpr (n == 39) {
      if (VT) {
#pragma unroll
        for (int t2 = 0; t2 < 2; ++t2) {
          const int F = (cj - 10) * 128 + 64 * cg + 32 * t2 + r, hd = F / 20;
          int d = F - 20 * hd - 4 * (hd & 1); if (d < 0) d += 20;
          const unsigned dst = WS_VB + (unsigned)((hd * 64 + 4 * ti + tg) * 2048 + hh * 512 + d * 16);
          st128(rs, dst, pack8(acc[t2], 0, 1.f)); st128(rs, dst + 1024, pack8(acc[t2], 1, 1.f));
          const unsigned cv = d == 18 ? 0x3f803f80u : 0u, cdst = d >= 18 ? dst + 32 : 0x7ffff000u;
          st128(rs, cdst, u32x4{cv, cv, cv, cv}); st128(rs, cdst + 1024, u32x4{cv, cv, cv, cv});
        }
      } else {
        const float sc = (cj < 5) ? QSCALE : 1.f;
        const unsigned dst = WS_QKB + (unsigned)(cj * 16 + 8 * cg + hh) * UNIT + (unsigned)(128 * ti + 32 * tg + r) * 16;
#pragma unroll
        for (int t2 = 0; t2 < 2; ++t2) { st128(rs, dst + (4 * t2) * UNIT, pack8(acc[t2], 0, sc)); st128(rs, dst + (4 * t2 + 2) * UNIT, pack8(acc[t2], 1, sc)); }
      }
      acc[0] = f32x16{}; acc[1] = f32x16{};
      SBAR();
    }
  });
  STAMP(0, 13);
  asm volatile("s_waitcnt vmcnt(0)" : "+v"(fa), "+v"(fb) :: "memory");
  early = __all(fa == tag && fb == tag);
  if (early) att_start();
  const unsigned dst = WS_QKVA + (unsigned)(cj * 16 + 8 * cg + hh) * UNIT + (unsigned)(128 * ti + 32 * tg + tr) * 16;
#pragma unroll
  for (int t2 = 0; t2 < 2; ++t2) { st128(rs, dst + (4 * t2) * UNIT, pack8(acc[t2], 0, 1.f)); st128(rs, dst + (4 * t2 + 2) * UNIT, pack8(acc[t2], 1, 1.f)); }
}

__device__ __forceinline__ f32x16 smf(bf16x8 a, const f32x16& p, int s, f32x16 o, int idx) {
  f32x8 b = s ? __builtin_shufflevector(p, p, 8, 9, 10, 11, 12, 13, 14, 15) : __builtin_shufflevector(p, p, 0, 1, 2, 3, 4, 5, 6, 7);
  return __builtin_amdgcn_smfmac_f32_32x32x32_bf16(__builtin_bit_cast(bfv8, a), __builtin_bit_cast(bfv16, b), o, idx, 0, 0);
}
__device__ __forceinline__ float max16(const f32x16& s) {
  float a = fmaxf(fmaxf(s[0], s[1]), s[2]), b = fmaxf(fmaxf(s[3], s[4]), s[5]);
  a = fmaxf(fmaxf(a, s[6]), s[7]); b = fmaxf(fmaxf(b, s[8]), s[9]);
  a = fmaxf(fmaxf(a, s[10]), s[11]); b = fmaxf(fmaxf(b, s[12]), s[13]);
  a = fmaxf(fmaxf(a, s[14]), s[15]);
  return fmaxf(a, b);
}
__device__ __forceinline__ float swap_max(float m) {
  auto rr = __builtin_amdgcn_permlane32_swap(__float_as_uint(m), __float_as_uint(m), false, false);
  return fmaxf(__uint_as_float(rr[0]), __uint_as_float(rr[1]));
}
__device__ __forceinline__ float swap_add(float m) {
  auto rr = __builtin_amdgcn_permlane32_swap(__float_as_uint(m), __float_as_uint(m), false, false);
  return __uint_as_float(rr[0]) + __uint_as_float(rr[1]);
}
__device__ __forceinline__ f32x16 splat16(float v) { f32x16 r; for (int i = 0; i < 16; ++i) r[i] = v; return r; }

__device__ __forceinline__ void outproj(f32x4 (&acc)[5], const u32x4 (&of)[10], lds_cptr wm, lds_cptr wt) {
  bf16x8 wf[2][5];
  sfor<0, 11>([&](auto jc) __attribute__((always_inline)) {
    constexpr int j = decltype(jc)::value;
    if constexpr (j < 10) {
#pragma unroll
      for (int n = 0; n < 5; ++n)
        wf[j & 1][n] = j < 8 ? cat8(trd(wm + (40 * j) * 160 + n * 32), trd(wm + (40 * j + 8) * 160 + n * 32))
                             : cat8(trd(wt + (160 * (j - 8)) * 160 + n * 32), trd(wt + (160 * (j - 8) + 40) * 160 + n * 32));
    }
    if constexpr (j > 0) {
#pragma unroll
      for (int n = 0; n < 5; ++n) acc[n] = MFMA16(wf[(j - 1) & 1][n], of[j - 1], j == 1 ? f32x4{} : acc[n]);
    }
    SBAR();
  });
}

__global__ __launch_bounds__(512, 2) void fused_kernel(const char* __restrict__ x, const char* __restrict__ wA, const char* __restrict__ wB,
                                                       const char* __restrict__ woA, const char* __restrict__ woB,
                                                       const char* __restrict__ gw, char* ws, char* __restrict__ out) {
  extern __shared__ __attribute__((aligned(16))) char lds[];
  const int tid = threadIdx.x, lane = tid & 63;
  const int w = __builtin_amdgcn_readfirstlane(tid >> 6);
  const int xcd = blockIdx.x & 7, slot = blockIdx.x >> 3;
  const unsigned lds0 = (unsigned)(uintptr_t)lds;
  gu64* flg = (gu64*)ws;
  const rsrc_t rs = __builtin_amdgcn_make_buffer_rsrc(ws, 0, WS_END, 0x00020000);
  DBG_DECL
  STAMP(0, 0);
  u64 tag;
  const int h = 4 * xcd + (slot & 3), c = slot >> 2;
  const int e = h & 1, ua = 5 * (h >> 1) + 3 * e, us = 5 * (h >> 1) + 2;
  const int jlo = (5 * (h >> 1) + 2 * e) >> 4, jhi = (5 * (h >> 1) + 2 * e + 2) >> 4;
  const int kh = w >> 2, tq = w & 3;
  const char* kbase = ws + WS_QKB + (size_t)(80 + (tq >= 2 ? us : ua + tq)) * UNIT + 1024 * kh * 16;
  const char* vbase = ws + WS_VB + (size_t)(h * 64 + 32 * kh + tq) * 2048;
  const unsigned ldr = lds0 + kh * AT_HALF + tq * 2048, ldk = lds0 + kh * AT_HALF + min(tq, 2) * 2048;
  auto kpiece = [&](auto stc) __attribute__((always_inline)) {
    constexpr int st = decltype(stc)::value;
    if (tq < 3) dma2_sc1<L_RING + (st & 1) * AT_STAGE>(kbase + st * 2048, lane * 16, ldk);
  };
  auto vpiece = [&](auto stc) __attribute__((always_inline)) {
    constexpr int st = decltype(stc)::value;
    dma2_sc1<L_RING + (st & 1) * AT_STAGE + 6144>(vbase + st * 8192, (unsigned)((lane & 32) + min(lane & 31, 21)) * 16, ldr);
  };
  const unsigned fiKV = (5 + 5 * (lane >> 5) + ((lane & 16) ? jhi : jlo)) * 16 + (lane & 15), fiQ = ((lane & 1) ? jhi : jlo) * 16 + 2 * c + ((lane >> 1) & 1);
  auto att_start = [&]() __attribute__((always_inline)) {
    kpiece(IC<0>{});
    if (tq == 3) {
      const char* qb = ws + WS_QKB + (256 * c + 128 * kh) * 16;
      dma2_sc1<L_Q>(qb + (size_t)ua * UNIT, lane * 16, lds0 + kh * 2048);
      dma2_sc1<L_Q + 4096>(qb + (size_t)(ua + 1) * UNIT, lane * 16, lds0 + kh * 2048);
      dma2_sc1<L_Q + 8192>(qb + (size_t)us * UNIT, lane * 16, lds0 + kh * 2048);
    }
    vpiece(IC<0>{});
  };

  const int ti = 8 * (xcd & 1) + (slot & 7), cj = 4 * (xcd >> 1) + (slot >> 3);
  bool early = false;
  if (cj < 10) g1_body<false>(x, wA, wB, ws, rs, lds, ti, cj, w, lane, tag, early, fiKV, fiQ, att_start DBG_PASS);
  else if (cj < 15) g1_body<true>(x, wA, wB, ws, rs, lds, ti, cj, w, lane, tag, early, fiKV, fiQ, att_start DBG_PASS);
  else {
    const u64 ep = __hip_atomic_load(flg, RLX_AGENT);
    tag = (((u64)(unsigned)__builtin_amdgcn_readfirstlane((unsigned)(ep >> 32)) << 32) | (unsigned)__builtin_amdgcn_readfirstlane((unsigned)ep)) + TAG_STEP;
    unsigned gg[5];
#pragma unroll
    for (int i = 0; i < 5; ++i) gg[i] = ((const unsigned*)gw)[i * 64 + lane];
    float mine = 0.f;
#pragma unroll 4
    for (int t = 0; t < 16; ++t) {
      const unsigned* xp = (const unsigned*)(x + (size_t)(128 * ti + 16 * w + t) * (D_ * 2));
      float s = 0.f;
#pragma unroll
      for (int i = 0; i < 5; ++i) s = dot2(xp[i * 64 + lane], gg[i], s);
#pragma unroll
      for (int o = 32; o > 0; o >>= 1) s += __shfl_xor(s, o);
      if (lane == t) mine = s;
    }
    if (lane < 16) __builtin_amdgcn_raw_buffer_store_b32(__float_as_uint(1.f / (1.f + __expf(-mine))), rs, WS_G + (128 * ti + 16 * w + lane) * 4, 0, SC1);
  }
  const unsigned flag1 = cj < 15 ? F1A + cj * 16 + ti : FG + ti;

  STAMP(1, 0);
  const int hh = lane >> 5, r = lane & 31;
  if (!early) {
    wait_flags(flg, F1B + fiKV, tag);
    wait_flags(flg, F1B + fiQ, tag);
    att_start();
  }
  STAMP(1, 1);
  WAIT_BAR(0);
  if (tid == 0) __hip_atomic_store(flg + flag1 * 16, tag, RLX_AGENT);
  kpiece(IC<1>{}); vpiece(IC<1>{});
  const unsigned obm = WS_OB + o_main(256 * c + 64 * tq + 32 * kh + r, h, hh), obt = hh ? 0x7ffff000u : WS_OB + o_tail(256 * c + 64 * tq + 32 * kh + r, h);
  st128(rs, obm, u32x4{~0u, ~0u, ~0u, ~0u}); st64(rs, obt, u32x2{~0u, ~0u});
  STAMP(1, 2);
  bf16x8 q0[2]; s16x4 q1[2];
#pragma unroll
  for (int t = 0; t < 2; ++t) {
    const lds_cptr qp = (lds_cptr)lds + L_Q + (64 * tq + 32 * t + r) * 16;
    q0[t] = ldsr128(qp + hh * 4096);
    const s16x4 tl = ldsr64(qp + 8192 + 8 * e);
    q1[t] = (hh == e) ? tl : s16x4{0, 0, 0, 0};
  }

  const int rt = 4 * xcd + (slot & 3), ct = slot >> 2;
  const int g4 = lane >> 4, q4 = (lane & 15) >> 2, p4 = lane & 3;
  const unsigned wolo = (unsigned)((lane / 10) * (D_ * 2) + (lane % 10) * 16);
  const unsigned wolo13 = (w == 7 && lane >= 60) ? 0u : wolo;
  const unsigned ldo1 = lds0 + w * (60 * 160), ldo2 = lds0 + (480 + 20 * w) * 160;
  const unsigned wo1 = (unsigned)(60 * w) * (D_ * 2) + ct * 160, wo2 = (unsigned)(480 + 20 * w) * (D_ * 2) + ct * 160;
  auto wopiece = [&](const char* wo, auto pc) __attribute__((always_inline)) {
    constexpr int p = decltype(pc)::value, row = p < 10 ? 6 * p : p == 13 ? 14 : 6 * (p - 10);
    if constexpr (p < 14 && !AB_NOWO) dma<L_R + row * 160>(wo + (p < 10 ? wo1 : wo2) + row * (D_ * 2), p == 13 ? wolo13 : wolo, p < 10 ? ldo1 : ldo2);
  };
  const lds_cptr wm = (lds_cptr)lds + L_R + (320 * kh + 24 * (g4 >> 1) + 4 * (g4 & 1) + q4) * 160 + 8 * p4;
  const lds_cptr wt = (lds_cptr)lds + L_R + (320 * kh + 80 * (g4 >> 1) + 4 * (g4 & 1) + 16 + q4) * 160 + 8 * p4;
  const unsigned osrc = (unsigned)((rt * 4 + tq) * 20 + 10 * kh) * 1024 + lane * 16;
  f32x4 accA[5];
  float gate = 0.f;
  auto fiA = [&]() __attribute__((always_inline)) { const int l18 = min(lane, 17), tt = 2 * c - 1 + l18 % 3; return F1A + (5 * (l18 / 6) + (((l18 / 3) & 1) ? jhi : jlo)) * 16 + (tt < 0 ? 0 : tt); };
  auto fiOA = [&]() __attribute__((always_inline)) { return lane < 32 ? FOA + (rt >> 2) * 32 + lane : FG + (rt >> 1); };

  int idx = 0xDDDD; PIN(idx);
  const lds_cptr ring = (lds_cptr)lds + L_RING + kh * AT_HALF;
  const lds_cptr kp0 = ring + hh * 2048 + r * 16;
  const lds_cptr kp1 = (hh == e) ? ring + 2 * 2048 + r * 16 + 8 * hh : ring + 6144 + 21 * 16;
  const lds_cptr vp = ring + 6144 + hh * 512 + r * 16;
  f32x16 ca, cb;
  {
    const bf16x8 k0 = ldsr128(kp0 - kh * AT_HALF); const s16x4 k1 = ldsr64(kp1 - kh * AT_HALF);
    f32x16 s = MFMA32(k0, q0[0], f32x16{}); s = MFMA8(k1, q1[0], s);
    f32x16 t = MFMA32(k0, q0[1], f32x16{}); t = MFMA8(k1, q1[1], t);
    ca = splat16(-swap_max(max16(s)));
    cb = splat16(-swap_max(max16(t)));
  }
  PIN(ca); PIN(cb);
  f32x16 oa = f32x16{}, ob = f32x16{}, sa, sb;
  bf16x8 k0 = ldsr128(kp0); s16x4 k1 = ldsr64(kp1);
  bf16x8 v0 = ldsr128(vp), v1 = ldsr128(vp + 1024);
  bf16x8 v1p = v1;
  sa = MFMA32(k0, q0[0], ca); sa = MFMA8(k1, q1[0], sa);
  sb = f32x16{};
  STAMP(1, 3);
  u32x4 lq; unsigned lqt;
  const char* labase = ws + WS_QKVA + (size_t)(80 * (1 + w / 3) + (w % 3 == 2 ? us : ua + w % 3)) * UNIT + (256 * c - 16) * 16;
  u32x4 of[10];
#define EX(S, j) S[j] = __builtin_amdgcn_exp2f(S[j])
#define EX2(S, j) do { EX(S, j); EX(S, j + 1); } while (0)
#define EX4(S, j) do { EX(S, j); EX(S, j + 1); EX(S, j + 2); EX(S, j + 3); } while (0)
  sfor<0, 8>([&](auto stc) __attribute__((always_inline)) {
    constexpr int st = decltype(stc)::value;
    constexpr int cur = (st & 1) * AT_STAGE, nxt = ((st + 1) & 1) * AT_STAGE;
    sfor<0, 4>([&](auto ic) __attribute__((always_inline)) {
      constexpr int i = decltype(ic)::value;
      constexpr int nk = (i < 3) ? cur + (i + 1) * 512 : nxt, nv = (i < 3) ? cur + (i + 1) * 2048 : nxt;
      constexpr int sl = (i + 1) & 3, sst = (i == 3) ? st + 1 : st;
      SBAR();
      EX2(sa, 0); PIN(sa); SBAR();
      ob = smf(v1p, sb, 1, ob, idx); EX4(sa, 2); PIN(sa); SBAR();
      sb = MFMA32(k0, q0[1], cb); EX4(sa, 6); PIN(sa); SBAR();
      sb = MFMA8(k1, q1[1], sb);
      if constexpr (i == 3) {
        { if constexpr (st == 0) { WAIT_BAR(3); } else if constexpr (st == 2) { WAIT_BAR(5); } else if constexpr (st == 3) { WAIT_BAR(2); } else if constexpr (st == 6) { WAIT_BAR(6); } else if constexpr (st < 7) { WAIT_BAR(0); } else { WAIT_BAR(63); } }
        if constexpr (st == 1) { PIN(lq); PIN(lqt); }
        if constexpr (st == 4) {
#pragma unroll
          for (int j = 0; j < 10; ++j) PIN(of[j]);
          PIN(gate);
        }
        if constexpr (st == 2) { if (tid == 0) __hip_atomic_store(flg + (FOA + c * 32 + h) * 16, tag, RLX_AGENT); }
        if constexpr (st == 0 || st == 3) {
          if (!__all(*(const LAS u64*)((lds_cptr)lds + L_FL + lane * 16) == tag)) wait_flags(flg, st == 0 ? fiA() : fiOA(), tag);
        }
      }
      __builtin_amdgcn_s_setprio(3 - sl);
      k0 = ldsr128(kp0 + nk); k1 = ldsr64(kp1 + nk);
      if constexpr (sst >= 1 && sst + 1 < 8 && sl == 0) kpiece(IC<sst + 1>{});
      if constexpr ((sst == 0 || sst == 3) && sl == 2) { if (w == 7) dma_sc1<L_FL>(ws, (sst == 0 ? fiA() : fiOA()) * 128, lds0); }
      if constexpr (sst == 1 && sl == 1) { if (w < 6) dma_sc1<L_LA>(labase, (c == 0 ? max(lane, 16) : lane) * 16, lds0 + w * LA_UNIT); }
      if constexpr (sst == 1 && sl == 2) { if (w < 6) dma2_sc1<L_LA + 2304>(labase + 2304, lane * 16, lds0 + w * LA_UNIT); }
      if constexpr ((sst == 0 || sst == 2) && sl > 0) wopiece(woA, IC<(sst == 0 ? 0 : 5) + 2 * sl - 2>{});
      if constexpr (sst == 3 && (sl == 1 || sl == 3)) wopiece(woA, IC<9 + sl>{});
      if constexpr ((sst == 6 && sl > 0) || sst == 7) wopiece(woB, IC<8 * (sst - 6) + 2 * sl - 2>{});
      EX4(sa, 10); PIN(sa); SBAR();
      oa = smf(v0, sa, 0, oa, idx); EX2(sa, 14); PIN(sa); SBAR();
      EX2(sb, 0); PIN(sb); SBAR();
      oa = smf(v1, sa, 1, oa, idx); EX4(sb, 2); PIN(sb); SBAR();
      sa = MFMA32(k0, q0[0], ca); EX4(sb, 6); PIN(sb); SBAR();
      sa = MFMA8(k1, q1[0], sa);
      if constexpr (sst >= 1 && sst + 1 < 8 && sl == 0) vpiece(IC<sst + 1>{});
      if constexpr (sst == 1 && sl == 1) {
        if (w < 6) dma2_sc1<L_LA + 1024>(labase + 1024, lane * 16, lds0 + w * LA_UNIT);
        const unsigned to = (unsigned)(256 * c + 32 * w + r) * 16;
        lq = ld128(rs, WS_QKVA + (ua + hh) * UNIT + to);
        lqt = ld32(rs, WS_QKVA + us * UNIT + to + 8 * e + 4 * hh);
      }
      if constexpr ((sst == 0 || sst == 2) && (sl == 1 || sl == 2)) wopiece(woA, IC<(sst == 0 ? 0 : 5) + 2 * sl - 1>{});
      if constexpr (sst == 3 && (sl == 1 || sl == 3)) wopiece(woA, IC<10 + sl>{});
      if constexpr (sst == 4 && sl < 3) {
#pragma unroll
        for (int j = 4 * sl; j < 4 * sl + 4 && j < 10; ++j) of[j] = ld128(rs, WS_OA + osrc + j * 1024);
        if constexpr (sl == 2) gate = __uint_as_float(ld32(rs, WS_G + (rt * 64 + tq * 16 + (lane & 15)) * 4));
      }
      if constexpr ((sst == 6 && sl > 0) || sst == 7) wopiece(woB, IC<8 * (sst - 6) + 2 * sl - 1>{});
      v1p = v1;
      const bf16x8 v0n = ldsr128(vp + nv); v1 = ldsr128(vp + nv + 1024);
      EX4(sb, 10); PIN(sb); SBAR();
      ob = smf(v0, sb, 0, ob, idx);
      v0 = v0n;
      EX2(sb, 14); PIN(sb); SBAR();
      if constexpr ((st == 4 && i == 3) || (st == 5 && i == 1)) {
        if (kh == (st == 5)) {
          if constexpr (!AB_NOEXTRA) outproj(accA, of, wm, wt);
#pragma unroll
          for (int n = 0; n < 5; ++n) { accA[n] *= gate; PIN(accA[n]); }
        }
        SBAR();
      }
    });
    if constexpr (st == 1 && !AB_NOEXTRA) {
      const int tok = 256 * c + 32 * w + r;
      const lds_cptr lm = (lds_cptr)lds + L_LA + hh * LA_UNIT + (32 * w + r + 2) * 16;
      const lds_cptr lt = (lds_cptr)lds + L_LA + 2 * LA_UNIT + (32 * w + r + 2) * 16 + 8 * e + 4 * hh;
      float lg[4];
      u32x4 vm[8]; unsigned vt[8];
      float sp[8];
#pragma unroll
      for (int j = 0; j < 8; ++j) {
        const u32x4 km = *(const LAS u32x4*)(lm + 32 * (7 - j));
        const unsigned ktl = *(const LAS unsigned*)(lt + 32 * (7 - j));
        vm[j] = *(const LAS u32x4*)(lm + 3 * LA_UNIT + 32 * (7 - j));
        vt[j] = *(const LAS unsigned*)(lt + 3 * LA_UNIT + 32 * (7 - j));
        float s = dot2(lq[0], km[0], 0.f);
        s = dot2(lq[1], km[1], s); s = dot2(lq[2], km[2], s); s = dot2(lq[3], km[3], s); sp[j] = dot2(lqt, ktl, s);
      }
#pragma unroll
      for (int j = 0; j < 4; ++j) {
        auto rr = __builtin_amdgcn_permlane32_swap(__float_as_uint(sp[2 * j]), __float_as_uint(sp[2 * j + 1]), false, false);
        lg[j] = (__uint_as_float(rr[0]) + __uint_as_float(rr[1])) * QSCALE;
        if (c == 0 && w == 0) { if (tok - 2 * (2 * j + hh) < 0) lg[j] = -1e9f * 1.4426950408889634f; }
      }
      const float m = swap_max(fmaxf(fmaxf(lg[0], lg[1]), fmaxf(lg[2], lg[3])));
#pragma unroll
      for (int j = 0; j < 4; ++j) lg[j] = __builtin_amdgcn_exp2f(lg[j] - m);
      const float rl = __builtin_amdgcn_rcpf(swap_add((lg[0] + lg[1]) + (lg[2] + lg[3])));
      float o[10];
#pragma unroll
      for (int j = 0; j < 4; ++j) {
        const unsigned pj = __float_as_uint(lg[j] * rl);
        auto rr = __builtin_amdgcn_permlane32_swap(pj, pj, false, false);
        const unsigned pp = cvtpk(__uint_as_float(rr[0]), __uint_as_float(rr[1]));
#pragma unroll
        for (int u = 0; u < 5; ++u) {
          const unsigned a = u < 4 ? vm[2 * j][u & 3] : vt[2 * j], b = u < 4 ? vm[2 * j + 1][u & 3] : vt[2 * j + 1];
          o[2 * u] = dot2(pp, __builtin_amdgcn_perm(b, a, 0x05040100u), j ? o[2 * u] : 0.f);
          o[2 * u + 1] = dot2(pp, __builtin_amdgcn_perm(b, a, 0x07060302u), j ? o[2 * u + 1] : 0.f);
        }
      }
      auto s0 = __builtin_amdgcn_permlane32_swap(cvtpk(o[0], o[1]), cvtpk(o[4], o[5]), false, false);
      auto s1 = __builtin_amdgcn_permlane32_swap(cvtpk(o[2], o[3]), cvtpk(o[6], o[7]), false, false);
      const unsigned tl = cvtpk(o[8], o[9]);
      auto s2 = __builtin_amdgcn_permlane32_swap(tl, tl, false, false);
      st128(rs, WS_OA + o_main(tok, h, hh), u32x4{s0[0], s1[0], s0[1], s1[1]});
      if (!hh) st64(rs, WS_OA + o_tail(tok, h), u32x2{s2[0], s2[1]});
      SBAR();
    }
    STAMP(1, 4 + st);
  });
  ob = smf(v1p, sb, 1, ob, idx);
  __builtin_amdgcn_s_setprio(0);
  float ofin[12];
  {
    float* xw = (float*)(lds + L_RING + AT_STAGE + (tq * 2 + kh) * 3072) + lane;
    const float* xr = (const float*)(lds + L_RING + AT_STAGE + (tq * 2 + (kh ^ 1)) * 3072) + lane;
#pragma unroll
    for (int i = 0; i < 12; ++i) xw[i * 64] = kh ? oa[i] : ob[i];
    WAIT_BAR(63);
#pragma unroll
    for (int i = 0; i < 12; ++i) ofin[i] = (kh ? ob[i] : oa[i]) + xr[i * 64];
  }
  {
    auto rr = __builtin_amdgcn_permlane32_swap(__float_as_uint(ofin[8]), __float_as_uint(ofin[8]), false, false);
    float l = __uint_as_float(rr[1]);
    if (__builtin_expect(__any(!(l > 8.67e-19f && l < 1.15e18f)), 0)) {
      const bf16x8 qa = kh ? q0[1] : q0[0]; const s16x4 qb = kh ? q1[1] : q1[0];
      float m = -INFINITY;
      for (int pass = 0; pass < 2; ++pass) {
        const f32x16 cc = splat16(pass ? -m : 0.f);
        f32x16 o = f32x16{};
        for (int kt = 0; kt < 64; ++kt) {
          const unsigned to = (unsigned)(32 * kt + r) * 16;
          const bf16x8 a0 = __builtin_bit_cast(bf16x8, ld128(rs, WS_QKB + (80 + ua + hh) * UNIT + to));
          const s16x4 a1 = __builtin_bit_cast(s16x4, ld64(rs, WS_QKB + (80 + us) * UNIT + to + 8 * e));
          f32x16 s = MFMA32(a0, qa, cc); s = MFMA8(a1, qb, s);
          if (pass == 0) { m = fmaxf(m, max16(s)); continue; }
#pragma unroll
          for (int j = 0; j < 16; ++j) EX(s, j);
#pragma unroll
          for (int s2 = 0; s2 < 2; ++s2) {
            const u32x4 vv = ld128(rs, WS_VB + (h * 64 + kt) * 2048 + (2 * s2 + hh) * 512 + min(r, 21) * 16);
            o = smf(__builtin_bit_cast(bf16x8, vv), s, s2, o, idx);
          }
        }
        if (pass == 0) m = swap_max(m);
        else {
#pragma unroll
          for (int i = 0; i < 12; ++i) ofin[i] = o[i];
        }
      }
      auto r2 = __builtin_amdgcn_permlane32_swap(__float_as_uint(ofin[8]), __float_as_uint(ofin[8]), false, false);
      l = __uint_as_float(r2[1]);
    }
    const float rl = 1.f / l;
    unsigned ow[6];
#pragma unroll
    for (int i = 0; i < 6; ++i) ow[i] = min(cvtpk(ofin[2 * i] * rl, ofin[2 * i + 1] * rl), 0xfffffffeu);
    st128(rs, obm, u32x4{ow[0], ow[1], ow[2], ow[3]});
    st64(rs, obt, u32x2{ow[4], ow[5]});
  }
  STAMP(1, 12);

  WAITV_BAR(0);
  STAMP(2, 0);
  {
    unsigned pend = 0x3ffu;
#pragma unroll 1
    for (unsigned spins = 0; pend != 0 && spins < (1u << 16); ++spins) {
      asm volatile("s_bitcmp1_b32 %14, 0\n\ts_cbranch_scc0 1f\n\tglobal_load_dwordx4 %0, %10, %13 offset:0 sc1\n1:\n\t"
                   "s_bitcmp1_b32 %14, 1\n\ts_cbranch_scc0 1f\n\tglobal_load_dwordx4 %1, %10, %13 offset:1024 sc1\n1:\n\t"
                   "s_bitcmp1_b32 %14, 2\n\ts_cbranch_scc0 1f\n\tglobal_load_dwordx4 %2, %10, %13 offset:2048 sc1\n1:\n\t"
                   "s_bitcmp1_b32 %14, 3\n\ts_cbranch_scc0 1f\n\tglobal_load_dwordx4 %3, %10, %13 offset:3072 sc1\n1:\n\t"
                   "s_bitcmp1_b32 %14, 4\n\ts_cbranch_scc0 1f\n\tglobal_load_dwordx4 %4, %11, %13 offset:0 sc1\n1:\n\t"
                   "s_bitcmp1_b32 %14, 5\n\ts_cbranch_scc0 1f\n\tglobal_load_dwordx4 %5, %11, %13 offset:1024 sc1\n1:\n\t"
                   "s_bitcmp1_b32 %14, 6\n\ts_cbranch_scc0 1f\n\tglobal_load_dwordx4 %6, %11, %13 offset:2048 sc1\n1:\n\t"
                   "s_bitcmp1_b32 %14, 7\n\ts_cbranch_scc0 1f\n\tglobal_load_dwordx4 %7, %11, %13 offset:3072 sc1\n1:\n\t"
                   "s_bitcmp1_b32 %14, 8\n\ts_cbranch_scc0 1f\n\tglobal_load_dwordx4 %8, %12, %13 offset:0 sc1\n1:\n\t"
                   "s_bitcmp1_b32 %14, 9\n\ts_cbranch_scc0 1f\n\tglobal_load_dwordx4 %9, %12, %13 offset:1024 sc1\n1:\n\t"
                   "s_waitcnt vmcnt(0)"
                   : "+v"(of[0]), "+v"(of[1]), "+v"(of[2]), "+v"(of[3]), "+v"(of[4]), "+v"(of[5]), "+v"(of[6]), "+v"(of[7]), "+v"(of[8]), "+v"(of[9])
                   : "v"(osrc), "v"(osrc + 4096), "v"(osrc + 8192), "s"(ws + WS_OB), "s"(pend) : "memory", "scc");
      pend = 0;
#pragma unroll
      for (int j = 0; j < 10; ++j) pend |= __any(max(max(of[j][0], of[j][1]), max(of[j][2], of[j][3])) == ~0u) ? (1u << j) : 0u;
      if (pend) __builtin_amdgcn_s_sleep(4);
    }
  }
  STAMP(2, 1);
  f32x4 accB[5];
  outproj(accB, of, wm, wt);
  STAMP(2, 2);
  f32x4* xch = (f32x4*)(lds + L_RING) + tq * 5 * 64 + lane;
#pragma unroll
  for (int n = 0; n < 5; ++n) accB[n] = accA[n] + accB[n] * (1.f - gate);
  if (kh) {
#pragma unroll
    for (int n = 0; n < 5; ++n) xch[n * 64] = accB[n];
  }
  WAIT_BAR(0);
  if (!kh) {
    char* op = out + (size_t)(rt * 64 + tq * 16 + (lane & 15)) * (D_ * 2) + ct * 160 + g4 * 8;
#pragma unroll
    for (int n = 0; n < 5; ++n) {
      const f32x4 v = accB[n] + xch[n * 64];
      *(u32x2*)(op + n * 32) = u32x2{cvtpk(v[0], v[1]), cvtpk(v[2], v[3])};
    }
  }
  STAMP(2, 3);
  DBG_OUT();
  if (blockIdx.x == 0 && w == 7) {
#pragma unroll 1
    for (int i = 0; i < 4; ++i) wait_flags(flg, FOA + i * 64 + lane, tag);
    if (lane == 0) __hip_atomic_store(flg, tag, RLX_AGENT);
  }
}

extern "C" void kernel_launch(void* const* d_in, const int* in_sizes, int n_in, void* d_out, int out_size, void* d_ws,
                              size_t ws_size, hipStream_t stream) {
  (void)hipFuncSetAttribute((const void*)fused_kernel, hipFuncAttributeMaxDynamicSharedMemorySize, LDS_BYTES);
  fused_kernel<<<256, 512, LDS_BYTES, stream>>>((const char*)d_in[0], (const char*)d_in[1], (const char*)d_in[4], (const char*)d_in[2],
                                                (const char*)d_in[5], (const char*)d_in[6], (char*)d_ws, (char*)d_out);
}
```

```cpp
#include <hip/hip_runtime.h>
#include <hip/hip_bf16.h>
#include <stdint.h>

typedef short bf16x8 __attribute__((ext_vector_type(8)));
typedef short s16x4 __attribute__((ext_vector_type(4)));
typedef __bf16 bfv8 __attribute__((ext_vector_type(8)));
typedef __bf16 bfv2 __attribute__((ext_vector_type(2)));
typedef __bf16 bfv16 __attribute__((ext_vector_type(16)));
typedef float f32x16 __attribute__((ext_vector_type(16)));
typedef float f32x8 __attribute__((ext_vector_type(8)));
typedef float f32x4 __attribute__((ext_vector_type(4)));
typedef unsigned u32x4 __attribute__((ext_vector_type(4)));
typedef unsigned u32x2 __attribute__((ext_vector_type(2)));
typedef unsigned long long u64;
#define LAS __attribute__((address_space(3)))
#define GAS __attribute__((address_space(1)))
#define AINL __attribute__((always_inline))
typedef LAS const char* lds_cptr;
typedef LAS char* lds_ptr;
typedef GAS u64 gu64;

constexpr int D_ = 640, N3_ = 1920;
constexpr u64 TAG_STEP = 0x9E3779B97F4A7C15ull;
constexpr float QSCALE = 0.22360679774997896f * 1.4426950408889634f;
constexpr unsigned OOB = 0x7ffff000u;

constexpr unsigned UNIT = 2048 * 16;
constexpr unsigned F1B = 256, F1A = 512, FG = 768, FOA = 1024;
constexpr unsigned WS_QKB = 2048 * 128;
constexpr unsigned WS_VB = WS_QKB + 160 * UNIT;
constexpr unsigned WS_QKVA = WS_VB + 32 * 64 * 2048;
constexpr unsigned WS_OA = WS_QKVA + 240 * UNIT;
constexpr unsigned WS_OB = WS_OA + 2048 * 640 * 2;
constexpr unsigned WS_WO = WS_OB + 2048 * 640 * 2;
constexpr unsigned WS_G = WS_WO + 2 * 640 * 640 * 2;
constexpr unsigned WS_END = WS_G + 2048 * 4;

constexpr int G1_STAGE = 32768;
constexpr int L_R = 0, WQ = 25600;
constexpr int L_RING = 106496, AT_HALF = 14336, AT_STAGE = 2 * AT_HALF;
constexpr int L_FL = 104448, L_EP = 131072, L_Q = 76800;
constexpr int L_LA = 76800, LA_UNIT = 4352;
constexpr int LDS_BYTES = 163840;

#define STAMP(kid, i) do {} while (0)
#define DBG_DECL
#define DBG_ARG
#define DBG_PASS
#define DBG_OUT() do {} while (0)
#define BST 2
#define OB_AT 5
#define SBAR() __builtin_amdgcn_sched_barrier(0)
#define PIN(x) asm volatile("" : "+v"(x))
#define MFMA32(a, b, c) __builtin_amdgcn_mfma_f32_32x32x16_bf16(__builtin_bit_cast(bfv8, a), __builtin_bit_cast(bfv8, b), c, 0, 0, 0)
#define MFMA8(a, b, c) __builtin_amdgcn_mfma_f32_32x32x8bf16_1k(a, b, c, 0, 0, 0)
#define MFMA16(a, b, c) __builtin_amdgcn_mfma_f32_16x16x32_bf16(__builtin_bit_cast(bfv8, a), __builtin_bit_cast(bfv8, b), c, 0, 0, 0)
#define RLX_AGENT __ATOMIC_RELAXED, __HIP_MEMORY_SCOPE_AGENT
#define SC1 16

template <int V> struct IC { static constexpr int value = V; };
template <int I, int N, class F> __device__ __forceinline__ void sfor(F&& f) { if constexpr (I < N) { f(IC<I>{}); sfor<I + 1, N>(f); } }
template <int N> __device__ __forceinline__ void wait_bar() { asm volatile("s_waitcnt vmcnt(%0) lgkmcnt(0)\n\ts_barrier" :: "i"(N) : "memory"); }
template <int N> __device__ __forceinline__ void waitv_bar() { asm volatile("s_waitcnt vmcnt(%0)\n\ts_barrier" :: "i"(N) : "memory"); }

__device__ __forceinline__ unsigned cvtpk(float lo, float hi) {
  bfv2 r; r[0] = (__bf16)lo; r[1] = (__bf16)hi;
  return __builtin_bit_cast(unsigned, r);
}
__device__ __forceinline__ float dot2(unsigned a, unsigned b, float c) {
  return __builtin_amdgcn_fdot2_f32_bf16(__builtin_bit_cast(bfv2, a), __builtin_bit_cast(bfv2, b), c, false);
}
template <int OFF> __device__ __forceinline__ void dma(const char* base, unsigned voff, unsigned lds_base) {
  asm volatile("s_add_u32 m0, %2, %3\n\ts_nop 0\n\tglobal_load_lds_dwordx4 %0, %1" :: "v"(voff), "s"(base), "s"(lds_base), "i"(OFF) : "memory", "scc");
}
template <int OFF> __device__ __forceinline__ void dma_sc1(const char* base, unsigned voff, unsigned lds_base) {
  asm volatile("s_add_u32 m0, %2, %3\n\ts_nop 0\n\tglobal_load_lds_dwordx4 %0, %1 sc1" :: "v"(voff), "s"(base), "s"(lds_base), "i"(OFF) : "memory", "scc");
}
template <int OFF> __device__ __forceinline__ void dma2_sc1(const char* base, unsigned voff, unsigned lds_base) {
  asm volatile("s_add_u32 m0, %2, %3\n\ts_nop 0\n\tglobal_load_lds_dwordx4 %0, %1 sc1\n\tglobal_load_lds_dwordx4 %0, %1 offset:1024 sc1"
               :: "v"(voff), "s"(base), "s"(lds_base), "i"(OFF) : "memory", "scc");
}
template <int OFF> __device__ __forceinline__ void dma3_sc1(const char* base, unsigned voff, unsigned lds_base) {
  asm volatile("s_add_u32 m0, %2, %3\n\ts_nop 0\n\tglobal_load_lds_dwordx4 %0, %1 sc1\n\tglobal_load_lds_dwordx4 %0, %1 offset:1024 sc1\n\t"
               "global_load_lds_dwordx4 %0, %1 offset:2048 sc1" :: "v"(voff), "s"(base), "s"(lds_base), "i"(OFF) : "memory", "scc");
}
typedef short v4i16_t __attribute__((ext_vector_type(4)));
__device__ __forceinline__ s16x4 trd(lds_cptr p) {
  return __builtin_bit_cast(s16x4, __builtin_amdgcn_ds_read_tr16_b64_v4i16((LAS v4i16_t*)p));
}
__device__ __forceinline__ bf16x8 cat8(s16x4 a, s16x4 b) { return __builtin_shufflevector(a, b, 0, 1, 2, 3, 4, 5, 6, 7); }
__device__ __forceinline__ bf16x8 ldsr128(lds_cptr p) { return *(const LAS bf16x8*)p; }
__device__ __forceinline__ s16x4 ldsr64(lds_cptr p) { return *(const LAS s16x4*)p; }
__device__ __forceinline__ u32x4 pack8(const f32x16& a, int s, float sc) {
  return u32x4{cvtpk(a[8 * s] * sc, a[8 * s + 1] * sc), cvtpk(a[8 * s + 2] * sc, a[8 * s + 3] * sc),
               cvtpk(a[8 * s + 4] * sc, a[8 * s + 5] * sc), cvtpk(a[8 * s + 6] * sc, a[8 * s + 7] * sc)};
}
__device__ __forceinline__ int swap23(int r) { return (r & 0x13) | ((r & 4) << 1) | ((r & 8) >> 1); }
__device__ __forceinline__ u64 uni64(unsigned lo, unsigned hi) {
  return ((u64)(unsigned)__builtin_amdgcn_readfirstlane(hi) << 32) | (unsigned)__builtin_amdgcn_readfirstlane(lo);
}

typedef __amdgpu_buffer_rsrc_t rsrc_t;
__device__ __forceinline__ void st128(rsrc_t rs, unsigned off, u32x4 v) { __builtin_amdgcn_raw_buffer_store_b128(v, rs, off, 0, SC1); }
__device__ __forceinline__ void st64(rsrc_t rs, unsigned off, u32x2 v) { __builtin_amdgcn_raw_buffer_store_b64(v, rs, off, 0, SC1); }
__device__ __forceinline__ u32x4 ld128(rsrc_t rs, unsigned off) { return __builtin_amdgcn_raw_buffer_load_b128(rs, off, 0, SC1); }
__device__ __forceinline__ u32x2 ld64(rsrc_t rs, unsigned off) { return __builtin_amdgcn_raw_buffer_load_b64(rs, off, 0, SC1); }
__device__ __forceinline__ unsigned ld32(rsrc_t rs, unsigned off) { return __builtin_amdgcn_raw_buffer_load_b32(rs, off, 0, SC1); }

__device__ __forceinline__ void wait_flags(gu64* flg, unsigned idx, u64 tag) {
#pragma unroll 1
  for (unsigned spins = 0; spins < (1u << 18); ++spins) {
    const u64 v = __hip_atomic_load(flg + idx * 16, RLX_AGENT);
    if (__all(v == tag)) break;
    __builtin_amdgcn_s_sleep(2);
  }
  asm volatile("" ::: "memory");
}
__device__ __forceinline__ void sample_flags(u64& fa, u64& fb, unsigned ia, unsigned ib, const char* base) {
  asm volatile("global_load_dwordx2 %0, %2, %4 sc1\n\tglobal_load_dwordx2 %1, %3, %4 sc1" : "=&v"(fa), "=&v"(fb) : "v"(ia * 128), "v"(ib * 128), "s"(base) : "memory");
}

__device__ __forceinline__ unsigned o_main(int tok, int h, int hh) {
  const int m = h >> 1;
  return (unsigned)((((tok >> 4) * 20 + 10 * (m >> 3) + (m & 7)) * 64 + (2 * (h & 1) + hh) * 16 + (tok & 15)) * 16);
}
__device__ __forceinline__ unsigned o_tail(int tok, int h) {
  const int m = h >> 1;
  return (unsigned)((((tok >> 4) * 20 + 10 * (m >> 3) + 8 + ((m >> 2) & 1)) * 64 + (2 * ((m >> 1) & 1) + (h & 1)) * 16 + (tok & 15)) * 16 + 8 * (m & 1));
}

constexpr int g1_buf(int st) { return (st < 10 ? (st & 3) : st == 10 ? 4 : ((st - 1) & 3)) * G1_STAGE; }
template <bool VT, class FPiece, class FStart>
__device__ __forceinline__ void g1_body(char* ws, rsrc_t rs, char* lds, int ti, int cj, int w, int lane, u64& tag, bool& early, unsigned fiKV, unsigned fiQ,
                                        u32x4 (&pk)[4], unsigned& dstA, FPiece&& piece, FStart&& att_start DBG_ARG) {
  const int tg = w & 3, cg = w >> 2;
  const int r = lane & 31, hh = lane >> 5, g16 = (lane >> 4) & 1, q4 = (lane & 15) >> 2, p4 = lane & 3;
  const int tr = VT ? swap23(r) : r;
  unsigned dstB[2];
#pragma unroll
  for (int t2 = 0; t2 < 2; ++t2) {
    if (VT) {
      const int F = (cj - 10) * 128 + 64 * cg + 32 * t2 + r, hd = F / 20;
      int d = F - 20 * hd - 4 * (hd & 1); if (d < 0) d += 20;
      dstB[t2] = WS_VB + (unsigned)((hd * 64 + 4 * ti + tg) * 2048 + hh * 512 + d * 16);
      const unsigned cv = d == 18 ? 0x3f803f80u : 0u, cdst = d >= 18 ? dstB[t2] + 32 : OOB;
      st128(rs, cdst, u32x4{cv, cv, cv, cv}); st128(rs, cdst + 1024, u32x4{cv, cv, cv, cv});
    } else {
      dstB[t2] = WS_QKB + (unsigned)(cj * 16 + 8 * cg + 4 * t2 + hh) * UNIT + (unsigned)(128 * ti + 32 * tg + r) * 16;
    }
  }
  unsigned xoff[4];
#pragma unroll
  for (int ks = 0; ks < 4; ++ks) xoff[ks] = (unsigned)((32 * tg + tr) * 128 + (((2 * ks + hh) ^ ((tr >> 1) & 7)) * 16));
  const int psw = ((p4 & 1) << 1) | (p4 >> 1);
  unsigned woffB[2], woffA[2];
#pragma unroll
  for (int t2 = 0; t2 < 2; ++t2) {
    const unsigned rb = (unsigned)((8 * hh + q4) * 256 + (((2 * cg + t2) ^ q4) * 64) + 32 * g16);
    woffB[t2] = rb + 8 * (VT ? p4 : psw);
    woffA[t2] = rb + 8 * psw;
  }
  bf16x8 xf[40], wf[3][2];
  u64 fa, fb;
  f32x16 acc[2];
  sfor<0, 82>([&](auto ic) AINL {
    constexpr int nn = decltype(ic)::value, n = nn - 2;
    constexpr int st = nn < 40 ? (nn >> 2) : 10 + ((nn - 40) >> 3), ks = nn < 40 ? (nn & 3) : ((nn - 40) & 7);
    if constexpr (nn < 80) {
      if constexpr (ks == 0) {
        if constexpr (st == 3 || st == 13) wait_bar<4>();
        else waitv_bar<(st < 2 ? (VT ? 8 : 4) : (st == 4 || st == 5 || st == 10) ? 8 : st == 11 ? 12 : st == 14 ? 0 : 4)>();
        if constexpr (st == 2) { const u32x2 ep = *(const LAS u32x2*)((lds_cptr)lds + L_EP); tag = uni64(ep[0], ep[1]) + TAG_STEP; }
        if constexpr (st == 12) { if (w == 0 && lane == 0) __hip_atomic_store((gu64*)ws + (F1B + cj * 16 + ti) * 16, tag, RLX_AGENT); }
        if constexpr (st == 13) sample_flags(fa, fb, fiKV, fiQ, ws + F1B * 128);
        if constexpr (st == 14) {
          asm volatile("" : "+v"(fa), "+v"(fb));
          early = __all(fa == tag && fb == tag);
          if (early) att_start(); else sample_flags(fa, fb, fiKV, fiQ, ws + F1B * 128);
        }
        if constexpr (st < 12) STAMP(0, 1 + st);
      }
      const lds_cptr sb = (lds_cptr)lds + g1_buf(st);
      if constexpr (nn < 40) xf[nn] = ldsr128(sb + xoff[ks]);
#pragma unroll
      for (int t2 = 0; t2 < 2; ++t2) {
        const lds_cptr wp = sb + (nn < 40 ? 16384 + woffB[t2] : woffA[t2]) + ks * 4096;
        wf[nn % 3][t2] = cat8(trd(wp), trd(wp + 1024));
      }
      if constexpr (st == 3) { piece(IC<(ks < 2 ? 5 : 10)>{}, IC<2 * (ks & 1)>{}); piece(IC<(ks < 2 ? 5 : 10)>{}, IC<2 * (ks & 1) + 1>{}); }
      else if constexpr (st < 8) piece(IC<st + 2>{}, IC<ks>{});
      else if constexpr (st < 12 && ks < 4) piece(IC<st + 3>{}, IC<ks>{});
    }
    if constexpr (n >= 0) {
#pragma unroll
      for (int t2 = 0; t2 < 2; ++t2) {
        const f32x16 cin = (n == 0 || n == 40) ? f32x16{} : acc[t2];
        acc[t2] = (VT && n < 40) ? MFMA32(xf[n % 40], wf[n % 3][t2], cin) : MFMA32(wf[n % 3][t2], xf[n % 40], cin);
      }
    }
    SBAR();
    if constexpr (n == 39) {
      const float sc = (!VT && cj < 5) ? QSCALE : 1.f;
#pragma unroll
      for (int i = 0; i < 4; ++i) { pk[i] = VT ? pack8(acc[i >> 1], i & 1, 1.f) : pack8(acc[i >> 1], i & 1, sc); PIN(pk[i]); }
      SBAR();
    }
    if constexpr (n >= 39 && n < 39 + 4 * BST && (n - 39) % BST == 0) {
      constexpr int i = (n - 39) / BST;
      st128(rs, dstB[i >> 1] + (i & 1) * (VT ? 1024 : 2 * UNIT), pk[i]);
      SBAR();
    }
  });
  STAMP(0, 13);
  if (!early) {
    asm volatile("s_waitcnt vmcnt(0)" : "+v"(fa), "+v"(fb) :: "memory");
    early = __all(fa == tag && fb == tag);
    if (early) att_start();
  }
  dstA = WS_QKVA + (unsigned)(cj * 16 + 8 * cg + hh) * UNIT + (unsigned)(128 * ti + 32 * tg + tr) * 16;
#pragma unroll
  for (int i = 0; i < 4; ++i) pk[i] = pack8(acc[i >> 1], i & 1, 1.f);
}

__device__ __forceinline__ f32x16 smf(bf16x8 a, const f32x16& p, int s, f32x16 o, int idx) {
  f32x8 b = s ? __builtin_shufflevector(p, p, 8, 9, 10, 11, 12, 13, 14, 15) : __builtin_shufflevector(p, p, 0, 1, 2, 3, 4, 5, 6, 7);
  return __builtin_amdgcn_smfmac_f32_32x32x32_bf16(__builtin_bit_cast(bfv8, a), __builtin_bit_cast(bfv16, b), o, idx, 0, 0);
}
__device__ __forceinline__ float max16(const f32x16& s) {
  float a = fmaxf(fmaxf(s[0], s[1]), s[2]), b = fmaxf(fmaxf(s[3], s[4]), s[5]);
  a = fmaxf(fmaxf(a, s[6]), s[7]); b = fmaxf(fmaxf(b, s[8]), s[9]);
  a = fmaxf(fmaxf(a, s[10]), s[11]); b = fmaxf(fmaxf(b, s[12]), s[13]);
  a = fmaxf(fmaxf(a, s[14]), s[15]);
  return fmaxf(a, b);
}
__device__ __forceinline__ float swap_max(float m) {
  auto rr = __builtin_amdgcn_permlane32_swap(__float_as_uint(m), __float_as_uint(m), false, false);
  return fmaxf(__uint_as_float(rr[0]), __uint_as_float(rr[1]));
}
__device__ __forceinline__ float swap_add(float m) {
  auto rr = __builtin_amdgcn_permlane32_swap(__float_as_uint(m), __float_as_uint(m), false, false);
  return __uint_as_float(rr[0]) + __uint_as_float(rr[1]);
}
__device__ __forceinline__ f32x16 splat16(float v) { f32x16 r; for (int i = 0; i < 16; ++i) r[i] = v; return r; }

__global__ __launch_bounds__(512, 2) void fused_kernel(const char* __restrict__ x, const char* __restrict__ wA, const char* __restrict__ wB,
                                                       const char* __restrict__ woA, const char* __restrict__ woB,
                                                       const char* __restrict__ gw, char* ws, char* __restrict__ out) {
  extern __shared__ __attribute__((aligned(16))) char lds[];
  const int tid = threadIdx.x, lane = tid & 63;
  const int w = __builtin_amdgcn_readfirstlane(tid >> 6);
  const int xcd = blockIdx.x & 7, slot = blockIdx.x >> 3;
  const unsigned lds0 = (unsigned)(uintptr_t)lds;
  gu64* flg = (gu64*)ws;
  const rsrc_t rs = __builtin_amdgcn_make_buffer_rsrc(ws, 0, WS_END, 0x00020000);
  DBG_DECL
  STAMP(0, 0);
  if (w == 6) {
    u64 pc; unsigned len;
    asm volatile("s_getpc_b64 %0\nGM_CODE_BEG:\n\ts_mov_b32 %1, GM_CODE_END-GM_CODE_BEG" : "=s"(pc), "=s"(len));
    asm volatile("s_add_u32 m0, %2, %3\n\ts_nop 0\n\tglobal_load_lds_dword %0, %1"
                 :: "v"(min((unsigned)((slot & 7) * 64 + lane) * 128, len)), "s"(pc), "s"(lds0), "i"(L_EP + 1024) : "memory", "scc");
  }
  const int ti = 8 * (xcd & 1) + (slot & 7), cj = 4 * (xcd >> 1) + (slot >> 3);
  const int rot = (ti + 2 * cj) % 10;
  unsigned xlo[2], wlo[2];
#pragma unroll
  for (int i = 0; i < 2; ++i) {
    const int row = 16 * w + 8 * i + (lane >> 3);
    xlo[i] = (unsigned)(row * (D_ * 2) + (((lane & 7) ^ ((row >> 1) & 7)) * 16));
    wlo[i] = (unsigned)((8 * w + 4 * i + (lane >> 4)) * (N3_ * 2) + (((lane & 15) ^ (4 * (lane >> 4))) * 16));
  }
  const char* xb = x + (size_t)(128 * ti) * (D_ * 2);
  const char* wBb = wB + cj * 256;
  const char* wAb = wA + cj * 256;
  const unsigned ldw = lds0 + w * 2048;
  auto piece = [&](auto stc, auto pc) AINL {
    constexpr int st = decltype(stc)::value, p = decltype(pc)::value, base = g1_buf(st) + (p & 1) * 1024;
    int kb = (st < 10 ? st : 2 * (st - 10) + (p >> 1)) + rot; if (kb >= 10) kb -= 10;
    if constexpr (st < 10 && p < 2) dma<base + 16384>(wBb + (size_t)kb * (64 * N3_ * 2), wlo[p & 1], ldw);
    else if constexpr (st < 10) dma<base>(xb + kb * 128, xlo[p & 1], ldw);
    else dma<base + (p >> 1) * 16384>(wAb + (size_t)kb * (64 * N3_ * 2), wlo[p & 1], ldw);
  };
  if (cj < 15) {
    sfor<0, 8>([&](auto pc) AINL { constexpr int p = decltype(pc)::value; piece(IC<(p >> 2)>{}, IC<(p & 3)>{}); });
    if (w == 7) dma_sc1<L_EP>(ws, lane * 16, lds0);
  }
  STAMP(0, 14);
  SBAR();
  u64 tag;
  const int h = 4 * xcd + (slot & 3), c = slot >> 2;
  const int e = h & 1, ua = 5 * (h >> 1) + 3 * e, us = 5 * (h >> 1) + 2;
  const int jlo = (5 * (h >> 1) + 2 * e) >> 4, jhi = (5 * (h >> 1) + 2 * e + 2) >> 4;
  const int kh = w >> 2, tq = w & 3;
  const char* kbase = ws + WS_QKB + (size_t)(80 + (tq >= 2 ? us : ua + tq)) * UNIT + 1024 * kh * 16;
  const char* vbase = ws + WS_VB + (size_t)(h * 64 + 32 * kh + tq) * 2048;
  const unsigned ldr = lds0 + kh * AT_HALF + tq * 2048;
  auto kpiece = [&](auto stc) AINL {
    constexpr int st = decltype(stc)::value;
    if (tq < 3) dma2_sc1<L_RING + (st & 1) * AT_STAGE>(kbase + st * 2048, lane * 16, ldr);
  };
  auto vpiece = [&](auto stc) AINL {
    constexpr int st = decltype(stc)::value;
    dma2_sc1<L_RING + (st & 1) * AT_STAGE + 6144>(vbase + st * 8192, (unsigned)((lane & 32) + min(lane & 31, 21)) * 16, ldr);
  };
  const unsigned fiKV = (5 + 5 * (lane >> 5) + ((lane & 16) ? jhi : jlo)) * 16 + (lane & 15), fiQ = ((lane & 1) ? jhi : jlo) * 16 + 2 * c + ((lane >> 1) & 1);
  auto att_start = [&]() AINL {
    kpiece(IC<0>{});
    if (tq == 3) {
      const char* qb = ws + WS_QKB + (256 * c + 128 * kh) * 16;
      dma2_sc1<L_Q>(qb + (size_t)ua * UNIT, lane * 16, lds0 + kh * 2048);
      dma2_sc1<L_Q + 4096>(qb + (size_t)(ua + 1) * UNIT, lane * 16, lds0 + kh * 2048);
      dma2_sc1<L_Q + 8192>(qb + (size_t)us * UNIT, lane * 16, lds0 + kh * 2048);
    }
    vpiece(IC<0>{});
  };

  bool early = false;
  u32x4 pk[4]; unsigned dstA;
  if (cj < 10) g1_body<false>(ws, rs, lds, ti, cj, w, lane, tag, early, fiKV, fiQ, pk, dstA, piece, att_start DBG_PASS);
  else if (cj < 15) g1_body<true>(ws, rs, lds, ti, cj, w, lane, tag, early, fiKV, fiQ, pk, dstA, piece, att_start DBG_PASS);
  else {
    const char* wo = ((ti & 8) ? woB : woA) + (size_t)(80 * w) * (D_ * 2) + (ti & 7) * 160;
#pragma unroll 1
    for (int p = 0; p < 13; ++p) {
      const int s0 = min(64 * p, 736), sl = s0 + lane;
      dma<0>(wo, (unsigned)((sl / 10) * (D_ * 2) + (sl % 10) * 16), lds0 + w * 12800 + s0 * 16);
    }
    const u64 ep = __hip_atomic_load(flg, RLX_AGENT);
    tag = uni64((unsigned)ep, (unsigned)(ep >> 32)) + TAG_STEP;
    wait_bar<0>();
    const int g4 = lane >> 4, rl = 320 * kh + 4 * (g4 & 1) + ((lane & 15) >> 2);
#pragma unroll
    for (int ff = 0; ff < 13; ++ff) {
      const int f = min(tq + 4 * ff, 49), j = f / 5, n = f - 5 * j;
      const int row = rl + (j < 8 ? 40 * j + 24 * (g4 >> 1) : 160 * (j - 8) + 80 * (g4 >> 1) + 16);
      const lds_cptr p = (lds_cptr)lds + row * 160 + n * 32 + 8 * (lane & 3);
      st128(rs, WS_WO + (unsigned)(ti * 100 + kh * 50 + f) * 1024 + lane * 16, __builtin_bit_cast(u32x4, cat8(trd(p), trd(p + (j < 8 ? 8 : 40) * 160))));
    }
    wait_bar<0>();
    if (tid == 0) __hip_atomic_store(flg + (FG + ti) * 16, tag, RLX_AGENT);
    dstA = OOB;
#pragma unroll
    for (int i = 0; i < 4; ++i) pk[i] = u32x4{0u, 0u, 0u, 0u};
  }
  const unsigned flag1 = F1A + cj * 16 + ti;

  STAMP(1, 0);
  const int hh = lane >> 5, r = lane & 31;
  if (!early) {
    wait_flags(flg, F1B + fiKV, tag);
    wait_flags(flg, F1B + fiQ, tag);
    att_start();
  }
  STAMP(1, 1);
  wait_bar<0>();
  STAMP(1, 2);
  kpiece(IC<1>{}); vpiece(IC<1>{});
  bf16x8 q0[2]; s16x4 q1[2];
#pragma unroll
  for (int t = 0; t < 2; ++t) {
    const lds_cptr qp = (lds_cptr)lds + L_Q + (64 * tq + 32 * t + r) * 16;
    q0[t] = ldsr128(qp + hh * 4096);
    const s16x4 tl = ldsr64(qp + 8192 + 8 * e);
    q1[t] = (hh == e) ? tl : s16x4{0, 0, 0, 0};
  }

  const int rt = 4 * xcd + (slot & 3), ct = slot >> 2;
  const char* wob = ws + WS_WO + (size_t)ct * 102400 + w * 3072;
  auto woquarter = [&](auto brc, auto qc, auto dc) AINL {
    constexpr int off = decltype(brc)::value * 819200 + decltype(qc)::value * WQ, d = decltype(dc)::value;
    dma3_sc1<d>(wob + off, lane * 16, lds0 + w * 3072);
    if (w == 0) dma_sc1<d + 24576>(wob + off + 24576, lane * 16, lds0);
  };
  const unsigned osrc = (unsigned)((rt * 4 + tq) * 20 + 10 * kh) * 1024 + lane * 16;
  float gate;
  auto fiA = [&]() AINL {
    const int tt = 2 * c - 1 + lane % 3;
    return lane < 18 ? F1A + (5 * (lane / 6) + (((lane / 3) & 1) ? jhi : jlo)) * 16 + (tt < 0 ? 0 : tt) : FG + (lane == 18 ? ct : 8 + ct);
  };
  auto fiOA = [&]() AINL { return FOA + (rt >> 2) * 32 + r; };
  const unsigned obm = WS_OB + o_main(256 * c + 64 * tq + 32 * kh + r, h, hh), obt = hh ? OOB : WS_OB + o_tail(256 * c + 64 * tq + 32 * kh + r, h);

  int idx = 0xDDDD; PIN(idx);
  const lds_cptr ring = (lds_cptr)lds + L_RING + kh * AT_HALF;
  const lds_cptr kp0 = ring + hh * 2048 + r * 16;
  const lds_cptr kp1 = (hh == e) ? ring + 2 * 2048 + r * 16 + 8 * hh : ring + 6144 + 21 * 16;
  const lds_cptr vp = ring + 6144 + hh * 512 + r * 16;
  f32x16 ca, cb;
  {
    const bf16x8 k0 = ldsr128(kp0 - kh * AT_HALF); const s16x4 k1 = ldsr64(kp1 - kh * AT_HALF);
    f32x16 s = MFMA32(k0, q0[0], f32x16{}); s = MFMA8(k1, q1[0], s);
    f32x16 t = MFMA32(k0, q0[1], f32x16{}); t = MFMA8(k1, q1[1], t);
    ca = splat16(-swap_max(max16(s)));
    cb = splat16(-swap_max(max16(t)));
  }
  PIN(ca); PIN(cb);
  f32x16 oa = f32x16{}, ob = f32x16{}, sa, sb;
  bf16x8 k0 = ldsr128(kp0); s16x4 k1 = ldsr64(kp1);
  bf16x8 v0 = ldsr128(vp), v1 = ldsr128(vp + 1024);
  bf16x8 v1p = v1;
  sa = MFMA32(k0, q0[0], ca); sa = MFMA8(k1, q1[0], sa);
  sb = f32x16{};
  STAMP(1, 3);
  u32x4 lq; unsigned lqt;
  const char* labase = ws + WS_QKVA + (size_t)(80 * (1 + w / 3) + (w % 3 == 2 ? us : ua + w % 3)) * UNIT + (256 * c - 16) * 16;
  u32x4 of[10];
  unsigned gx[5], gg[5];

  auto local_attention = [&]() AINL {
    const int tok = 256 * c + 32 * w + r;
    const lds_cptr lm = (lds_cptr)lds + L_LA + hh * LA_UNIT + (32 * w + r + 2) * 16;
    const lds_cptr lt = (lds_cptr)lds + L_LA + 2 * LA_UNIT + (32 * w + r + 2) * 16 + 8 * e + 4 * hh;
    u32x4 km[8], vm[8]; unsigned kt[8], vt[8];
#pragma unroll
    for (int j = 0; j < 8; ++j) { km[j] = *(const LAS u32x4*)(lm + 32 * (7 - j)); kt[j] = *(const LAS unsigned*)(lt + 32 * (7 - j)); }
    SBAR();
    float sp[8];
#pragma unroll
    for (int j = 0; j < 8; ++j) sp[j] = dot2(lq[0], km[j][0], 0.f);
#pragma unroll
    for (int u = 1; u < 5; ++u)
#pragma unroll
      for (int j = 0; j < 8; ++j) sp[j] = dot2(u < 4 ? lq[u & 3] : lqt, u < 4 ? km[j][u & 3] : kt[j], sp[j]);
    SBAR();
#pragma unroll
    for (int j = 0; j < 8; ++j) { vm[j] = *(const LAS u32x4*)(lm + 3 * LA_UNIT + 32 * (7 - j)); vt[j] = *(const LAS unsigned*)(lt + 3 * LA_UNIT + 32 * (7 - j)); }
    SBAR();
    float lg[4];
#pragma unroll
    for (int j = 0; j < 4; ++j) {
      auto rr = __builtin_amdgcn_permlane32_swap(__float_as_uint(sp[2 * j]), __float_as_uint(sp[2 * j + 1]), false, false);
      lg[j] = (__uint_as_float(rr[0]) + __uint_as_float(rr[1])) * QSCALE;
    }
    if (__builtin_expect(c == 0 && w == 0, 0)) {
      asm volatile("" ::: "memory");
#pragma unroll
      for (int j = 0; j < 4; ++j) if (tok - 2 * (2 * j + hh) < 0) lg[j] = -1e9f * 1.4426950408889634f;
    }
    const float m = swap_max(fmaxf(fmaxf(lg[0], lg[1]), fmaxf(lg[2], lg[3])));
#pragma unroll
    for (int j = 0; j < 4; ++j) lg[j] = __builtin_amdgcn_exp2f(lg[j] - m);
    const float rl = __builtin_amdgcn_rcpf(swap_add((lg[0] + lg[1]) + (lg[2] + lg[3])));
    unsigned pp[4];
#pragma unroll
    for (int j = 0; j < 4; ++j) {
      const unsigned pj = __float_as_uint(lg[j] * rl);
      auto rr = __builtin_amdgcn_permlane32_swap(pj, pj, false, false);
      pp[j] = cvtpk(__uint_as_float(rr[0]), __uint_as_float(rr[1]));
    }
    float o[10];
#pragma unroll
    for (int j = 0; j < 4; ++j) {
      unsigned t[10];
#pragma unroll
      for (int u = 0; u < 5; ++u) {
        const unsigned a = u < 4 ? vm[2 * j][u & 3] : vt[2 * j], b = u < 4 ? vm[2 * j + 1][u & 3] : vt[2 * j + 1];
        t[2 * u] = __builtin_amdgcn_perm(b, a, 0x05040100u); t[2 * u + 1] = __builtin_amdgcn_perm(b, a, 0x07060302u);
      }
#pragma unroll
      for (int u = 0; u < 10; ++u) o[u] = dot2(pp[j], t[u], j ? o[u] : 0.f);
    }
    auto s0 = __builtin_amdgcn_permlane32_swap(cvtpk(o[0], o[1]), cvtpk(o[4], o[5]), false, false);
    auto s1 = __builtin_amdgcn_permlane32_swap(cvtpk(o[2], o[3]), cvtpk(o[6], o[7]), false, false);
    const unsigned tl = cvtpk(o[8], o[9]);
    auto s2 = __builtin_amdgcn_permlane32_swap(tl, tl, false, false);
    st128(rs, WS_OA + o_main(tok, h, hh), u32x4{s0[0], s1[0], s0[1], s1[1]});
    st64(rs, hh ? OOB : WS_OA + o_tail(tok, h), u32x2{s2[0], s2[1]});
    float gs = 0.f;
#pragma unroll
    for (int i = 0; i < 5; ++i) gs = dot2(gx[i], gg[i], gs);
    gs += __builtin_bit_cast(float, __builtin_amdgcn_update_dpp(0, __builtin_bit_cast(int, gs), 0xB1, 0xf, 0xf, true));
    gs += __builtin_bit_cast(float, __builtin_amdgcn_update_dpp(0, __builtin_bit_cast(int, gs), 0x4E, 0xf, 0xf, true));
    gs += __builtin_bit_cast(float, __builtin_amdgcn_update_dpp(0, __builtin_bit_cast(int, gs), 0x141, 0xf, 0xf, true));
    gs += __builtin_bit_cast(float, __builtin_amdgcn_update_dpp(0, __builtin_bit_cast(int, gs), 0x140, 0xf, 0xf, true));
    { auto g2 = __builtin_amdgcn_permlane16_swap(__float_as_uint(gs), __float_as_uint(gs), false, false); gs = __uint_as_float(g2[0]) + __uint_as_float(g2[1]); }
    gs = swap_add(gs);
    const float gv = __builtin_amdgcn_rcpf(1.f + __builtin_amdgcn_exp2f(-1.4426950408889634f * gs));
    __builtin_amdgcn_raw_buffer_store_b32(__float_as_uint(gv), rs, lane == 0 ? WS_G + (256 * c + 8 * h + w) * 4 : OOB, 0, SC1);
    SBAR();
  };

#define EX(S, j) S[j] = __builtin_amdgcn_exp2f(S[j])
#define EX2(S, j) do { EX(S, j); EX(S, j + 1); } while (0)
#define EX4(S, j) do { EX(S, j); EX(S, j + 1); EX(S, j + 2); EX(S, j + 3); } while (0)
  sfor<0, 8>([&](auto stc) AINL {
    constexpr int st = decltype(stc)::value;
    constexpr int cur = (st & 1) * AT_STAGE, nxt = ((st + 1) & 1) * AT_STAGE;
    sfor<0, 4>([&](auto ic) AINL {
      constexpr int i = decltype(ic)::value;
      constexpr int nk = (i < 3) ? cur + (i + 1) * 512 : nxt, nv = (i < 3) ? cur + (i + 1) * 2048 : nxt;
      constexpr int sl = (i + 1) & 3, sst = (i == 3) ? st + 1 : st;
      SBAR();
      EX2(sa, 0); PIN(sa); SBAR();
      ob = smf(v1p, sb, 1, ob, idx); EX4(sa, 2); PIN(sa); SBAR();
      sb = MFMA32(k0, q0[1], cb); EX4(sa, 6); PIN(sa); SBAR();
      sb = MFMA8(k1, q1[1], sb);
      if constexpr (i == 3) {
        wait_bar<(st == 3 || st == 4) ? 6 : st == 7 ? 63 : 0>();
        if constexpr (st == 0) { if (tid == 0) __hip_atomic_store(flg + flag1 * 16, tag, RLX_AGENT); }
        if constexpr (st == 2) {
          PIN(lq); PIN(lqt);
#pragma unroll
          for (int i = 0; i < 5; ++i) { PIN(gx[i]); PIN(gg[i]); }
        }
        if constexpr (st == 3) { if (tid == 0) __hip_atomic_store(flg + (FOA + c * 32 + h) * 16, tag, RLX_AGENT); }
        if constexpr (st == 1 || st == 5) {
          if (!__all(*(const LAS u64*)((lds_cptr)lds + L_FL + lane * 16) == tag)) wait_flags(flg, st == 1 ? fiA() : fiOA(), tag);
        }
      }
      __builtin_amdgcn_s_setprio(3 - sl);
      k0 = ldsr128(kp0 + nk); k1 = ldsr64(kp1 + nk);
      if constexpr (sst >= 1 && sst + 1 < 8 && sl == 0) kpiece(IC<sst + 1>{});
      if constexpr (sst == 0 && sl < 3) st128(rs, dstA + (4 * sl - 4) * UNIT, pk[2 * sl - 2]);
      if constexpr ((sst == 1 && sl == 2) || (sst == 5 && sl == 1)) { if (w == 7) dma_sc1<L_FL>(ws, (sst == 1 ? fiA() : fiOA()) * 128, lds0); }
      if constexpr (sst == 2 && sl == 1) { if (w < 6) dma_sc1<L_LA>(labase, (c == 0 ? max(lane, 16) : lane) * 16, lds0 + w * LA_UNIT); }
      if constexpr (sst == 2 && sl == 2) { if (w < 6) dma2_sc1<L_LA + 2304>(labase + 2304, lane * 16, lds0 + w * LA_UNIT); }
      if constexpr ((sst == 3 || sst == 4) && (sl == 1 || sl == 2)) woquarter(IC<0>{}, IC<2 * (sst - 3) + sl - 1>{}, IC<L_R + (2 * (sst - 3) + sl - 1) * WQ>{});
      if constexpr (sst == 7 && sl == 0) woquarter(IC<1>{}, IC<1>{}, IC<L_RING>{});
      EX4(sa, 10); PIN(sa); SBAR();
      oa = smf(v0, sa, 0, oa, idx); EX2(sa, 14); PIN(sa); SBAR();
      EX2(sb, 0); PIN(sb); SBAR();
      oa = smf(v1, sa, 1, oa, idx); EX4(sb, 2); PIN(sb); SBAR();
      sa = MFMA32(k0, q0[0], ca); EX4(sb, 6); PIN(sb); SBAR();
      sa = MFMA8(k1, q1[0], sa);
      if constexpr (sst >= 1 && sst + 1 < 8 && sl == 0) vpiece(IC<sst + 1>{});
      if constexpr (sst == 0 && sl < 3) st128(rs, dstA + (4 * sl - 2) * UNIT, pk[2 * sl - 1]);
      if constexpr (sst == 1 && sl == 1) {
        st128(rs, obm, u32x4{~0u, ~0u, ~0u, ~0u}); st64(rs, obt, u32x2{~0u, ~0u});
      }
      if constexpr (sst == 2 && sl == 1) {
        if (w < 6) dma2_sc1<L_LA + 1024>(labase + 1024, lane * 16, lds0 + w * LA_UNIT);
        const unsigned to = (unsigned)(256 * c + 32 * w + r) * 16;
        lq = ld128(rs, WS_QKVA + (ua + hh) * UNIT + to);
        lqt = ld32(rs, WS_QKVA + us * UNIT + to + 8 * e + 4 * hh);
      }
      if constexpr (sst == 2 && sl == 0) {
#pragma unroll
        for (int i = 0; i < 5; ++i) { gx[i] = ((const unsigned*)(x + (size_t)(256 * c + 8 * h + w) * (D_ * 2)))[lane * 5 + i]; gg[i] = ((const unsigned*)gw)[lane * 5 + i]; }
      }
      if constexpr (sst == 7 && sl > 0) {
#pragma unroll
        for (int j = 4 * sl - 4; j < 4 * sl && j < 10; ++j) of[j] = ld128(rs, WS_OA + osrc + j * 1024);
        if constexpr (sl == 3) gate = __uint_as_float(ld32(rs, WS_G + (rt * 64 + tq * 16 + (lane & 15)) * 4));
      }
      v1p = v1;
      const bf16x8 v0n = ldsr128(vp + nv); v1 = ldsr128(vp + nv + 1024);
      EX4(sb, 10); PIN(sb); SBAR();
      ob = smf(v0, sb, 0, ob, idx);
      v0 = v0n;
      EX2(sb, 14); PIN(sb); SBAR();
    });
    if constexpr (st == 2) local_attention();
    STAMP(1, 4 + st);
  });
  ob = smf(v1p, sb, 1, ob, idx);
  __builtin_amdgcn_s_setprio(0);
  const lds_cptr wl = (lds_cptr)lds + L_R + kh * (2 * WQ) + lane * 16;
  bf16x8 wf[25];
  float ofin[12];
  {
    float* xw = (float*)(lds + L_RING + AT_STAGE + (tq * 2 + kh) * 3072) + lane;
    const float* xr = (const float*)(lds + L_RING + AT_STAGE + (tq * 2 + (kh ^ 1)) * 3072) + lane;
#pragma unroll
    for (int i = 0; i < 12; ++i) xw[i * 64] = kh ? oa[i] : ob[i];
    wait_bar<0>();
#pragma unroll
    for (int j = 0; j < 10; ++j) PIN(of[j]);
    PIN(gate);
#pragma unroll
    for (int i = 0; i < 12; ++i) ofin[i] = (kh ? ob[i] : oa[i]) + xr[i * 64];
  }
  {
    auto rr = __builtin_amdgcn_permlane32_swap(__float_as_uint(ofin[8]), __float_as_uint(ofin[8]), false, false);
    float l = __uint_as_float(rr[1]);
    if (__builtin_expect(__any(!(l > 8.67e-19f && l < 1.15e18f)), 0)) {
      const bf16x8 qa = kh ? q0[1] : q0[0]; const s16x4 qb = kh ? q1[1] : q1[0];
      float m = -INFINITY;
#pragma unroll 1
      for (int pass = 0; pass < 2; ++pass) {
        const f32x16 cc = splat16(pass ? -m : 0.f);
        f32x16 o = f32x16{};
#pragma unroll 1
        for (int kt = 0; kt < 64; ++kt) {
          const unsigned to = (unsigned)(32 * kt + r) * 16;
          const bf16x8 a0 = __builtin_bit_cast(bf16x8, ld128(rs, WS_QKB + (80 + ua + hh) * UNIT + to));
          const s16x4 a1 = __builtin_bit_cast(s16x4, ld64(rs, WS_QKB + (80 + us) * UNIT + to + 8 * e));
          f32x16 s = MFMA32(a0, qa, cc); s = MFMA8(a1, qb, s);
          if (pass == 0) { m = fmaxf(m, max16(s)); continue; }
#pragma unroll
          for (int j = 0; j < 16; ++j) EX(s, j);
#pragma unroll
          for (int s2 = 0; s2 < 2; ++s2) {
            const u32x4 vv = ld128(rs, WS_VB + (h * 64 + kt) * 2048 + (2 * s2 + hh) * 512 + min(r, 21) * 16);
            o = smf(__builtin_bit_cast(bf16x8, vv), s, s2, o, idx);
          }
        }
        if (pass == 0) m = swap_max(m);
        else {
#pragma unroll
          for (int i = 0; i < 12; ++i) ofin[i] = o[i];
        }
      }
      auto r2 = __builtin_amdgcn_permlane32_swap(__float_as_uint(ofin[8]), __float_as_uint(ofin[8]), false, false);
      l = __uint_as_float(r2[1]);
    }
    const float rl = __builtin_amdgcn_rcpf(l);
    unsigned ow[6];
#pragma unroll
    for (int i = 0; i < 6; ++i) ow[i] = min(cvtpk(ofin[2 * i] * rl, ofin[2 * i + 1] * rl), 0xfffffffeu);
    st128(rs, obm, u32x4{ow[0], ow[1], ow[2], ow[3]});
    st64(rs, obt, u32x2{ow[4], ow[5]});
  }
  SBAR();
  STAMP(1, 12);
#pragma unroll
  for (int f = 0; f < 25; ++f) wf[f] = ldsr128(wl + f * 1024);
  SBAR();

  f32x4 accA[5], accB[5];
  u32x4 og[10];
#define OBLD(k, a, o) "s_bitcmp1_b32 %14, " #k "\n\ts_cbranch_scc0 1f\n\tglobal_load_dwordx4 %" #k ", %" #a ", %13 offset:" #o " sc1\n1:\n\t"
#define OB_ALL OBLD(0, 10, 0) OBLD(1, 10, 1024) OBLD(2, 10, 2048) OBLD(3, 10, 3072) OBLD(4, 11, 0) OBLD(5, 11, 1024) OBLD(6, 11, 2048) OBLD(7, 11, 3072) OBLD(8, 12, 0) OBLD(9, 12, 1024)
#define OG(c) c(og[0]), c(og[1]), c(og[2]), c(og[3]), c(og[4]), c(og[5]), c(og[6]), c(og[7]), c(og[8]), c(og[9])
#define OBL1(k, a, o) asm volatile("global_load_dwordx4 %0, %1, %2 offset:" #o " sc1" : "=&v"(og[k]) : "v"(osrc + a), "s"(ws + WS_OB) : "memory")
#pragma unroll
  for (int j = 0; j < 10; ++j) {
    if (j == 2) wait_bar<63>();
#pragma unroll
    for (int n = 0; n < 5; ++n) accA[n] = MFMA16(wf[(j % 5) * 5 + n], of[j], j == 0 ? f32x4{} : accA[n]);
    SBAR();
    if (j >= 2 && j < 5) {
#pragma unroll
      for (int f = (j == 2 ? 0 : 5 * j); f < 5 * j + 5; ++f) wf[f] = ldsr128(wl + WQ + f * 1024);
    }
    if (j == 2) woquarter(IC<1>{}, IC<0>{}, IC<L_R>{});
    if (j == 3) woquarter(IC<1>{}, IC<2>{}, IC<L_R + 2 * WQ>{});
    if (j == 4) woquarter(IC<1>{}, IC<3>{}, IC<L_RING + AT_STAGE>{});
    if (j == 5) { OBL1(0, 0, 0); OBL1(1, 0, 1024); OBL1(2, 0, 2048); }
    if (j == 6) { OBL1(3, 0, 3072); OBL1(4, 4096, 0); OBL1(5, 4096, 1024); }
    if (j == 7) { OBL1(6, 4096, 2048); OBL1(7, 4096, 3072); }
    if (j == 8) { OBL1(8, 8192, 0); OBL1(9, 8192, 1024); }
    SBAR();
  }
  STAMP(2, 0);
  waitv_bar<10>();
#pragma unroll
  for (int f = 0; f < 25; ++f) wf[f] = ldsr128(wl + f * 1024);
  SBAR();
  asm volatile("s_waitcnt vmcnt(0)" : OG("+v") :: "memory");
#pragma unroll 1
  for (unsigned spins = 0; spins < (1u << 16); ++spins) {
    unsigned pend = 0;
#pragma unroll
    for (int j = 0; j < 10; ++j) pend |= __any(max(max(og[j][0], og[j][1]), max(og[j][2], og[j][3])) == ~0u) ? (1u << j) : 0u;
    if (!pend) break;
    asm volatile(OB_ALL "s_waitcnt vmcnt(0)" : OG("+v") : "v"(osrc), "v"(osrc + 4096), "v"(osrc + 8192), "s"(ws + WS_OB), "s"(pend) : "memory", "scc");
  }
  STAMP(2, 1);
  const lds_cptr wh = (lds_cptr)lds + L_RING + kh * AT_STAGE + lane * 16;
#pragma unroll
  for (int j = 0; j < 10; ++j) {
#pragma unroll
    for (int n = 0; n < 5; ++n) accB[n] = MFMA16(wf[(j % 5) * 5 + n], og[j], j == 0 ? f32x4{} : accB[n]);
    if (j < 5) {
#pragma unroll
      for (int n = 0; n < 5; ++n) wf[j * 5 + n] = ldsr128(wh + (j * 5 + n) * 1024);
    }
    SBAR();
  }
  STAMP(2, 2);
  f32x4* xch = (f32x4*)(lds + L_R + WQ) + tq * 5 * 64 + lane;
#pragma unroll
  for (int n = 0; n < 5; ++n) accB[n] = accA[n] * gate + accB[n] * (1.f - gate);
  if (kh) {
#pragma unroll
    for (int n = 0; n < 5; ++n) xch[n * 64] = accB[n];
  }
  wait_bar<63>();
  if (!kh) {
    char* op = out + (size_t)(rt * 64 + tq * 16 + (lane & 15)) * (D_ * 2) + ct * 160 + (lane >> 4) * 8;
#pragma unroll
    for (int n = 0; n < 5; ++n) {
      const f32x4 v = accB[n] + xch[n * 64];
      *(u32x2*)(op + n * 32) = u32x2{cvtpk(v[0], v[1]), cvtpk(v[2], v[3])};
    }
  }
  STAMP(2, 3);
  DBG_OUT();
  asm volatile("GM_CODE_END:");
  if (blockIdx.x == 0 && w == 7) {
#pragma unroll 1
    for (int i = 0; i < 4; ++i) wait_flags(flg, FOA + i * 64 + lane, tag);
    if (lane == 0) __hip_atomic_store(flg, tag, RLX_AGENT);
  }
}

extern "C" void kernel_launch(void* const* d_in, const int* in_sizes, int n_in, void* d_out, int out_size, void* d_ws,
                              size_t ws_size, hipStream_t stream) {
  (void)hipFuncSetAttribute((const void*)fused_kernel, hipFuncAttributeMaxDynamicSharedMemorySize, LDS_BYTES);
  fused_kernel<<<256, 512, LDS_BYTES, stream>>>((const char*)d_in[0], (const char*)d_in[1], (const char*)d_in[4], (const char*)d_in[2],
                                                (const char*)d_in[5], (const char*)d_in[6], (char*)d_ws, (char*)d_out);
}
```

```cpp
#include <hip/hip_runtime.h>
#include <hip/hip_bf16.h>
#include <stdint.h>

typedef short bf16x8 __attribute__((ext_vector_type(8)));
typedef short s16x4 __attribute__((ext_vector_type(4)));
typedef __bf16 bfv8 __attribute__((ext_vector_type(8)));
typedef __bf16 bfv2 __attribute__((ext_vector_type(2)));
typedef __bf16 bfv16 __attribute__((ext_vector_type(16)));
typedef float f32x16 __attribute__((ext_vector_type(16)));
typedef float f32x8 __attribute__((ext_vector_type(8)));
typedef float f32x4 __attribute__((ext_vector_type(4)));
typedef unsigned u32x4 __attribute__((ext_vector_type(4)));
typedef unsigned u32x2 __attribute__((ext_vector_type(2)));
typedef unsigned long long u64;
#define LAS __attribute__((address_space(3)))
#define GAS __attribute__((address_space(1)))
#define AINL __attribute__((always_inline))
typedef LAS const char* lds_cptr;
typedef LAS char* lds_ptr;
typedef GAS u64 gu64;

constexpr int D_ = 640, N3_ = 1920;
constexpr u64 TAG_STEP = 0x9E3779B97F4A7C15ull;
constexpr float QSCALE = 0.22360679774997896f * 1.4426950408889634f;
constexpr unsigned OOB = 0x70000000u;

constexpr unsigned UNIT = 2048 * 16;
constexpr unsigned F1A = 512, FG = 768, FOA = 1024, F1B = 2048, NREP = 16;
#define REPSTEP 256
constexpr unsigned WS_QKB = (2048 + NREP * 256) * 128;
constexpr unsigned WS_VB = WS_QKB + 160 * UNIT;
constexpr unsigned BR_A = 160 * UNIT + 32 * 64 * 2048;
constexpr unsigned WS_OA = WS_QKB + 2 * BR_A;
constexpr unsigned WS_OB = WS_OA + 2048 * 640 * 2;
constexpr unsigned WS_WO = WS_OB + 2048 * 640 * 2;
constexpr unsigned WS_G = WS_WO + 2 * 640 * 640 * 2;
constexpr unsigned WS_C = WS_G + 2048 * 4;
constexpr unsigned WS_END = WS_C + 256 * 4096;

constexpr int L_R = 0, WQ = 25600;
constexpr int L_RING = 106496, AT_HALF = 14336, AT_STAGE = 2 * AT_HALF;
constexpr int L_FL = 104448, L_EP = 98304, L_PF = 81920, L_Q = 0;
constexpr int L_LK = 51200, LA_UNIT = 4608, L_LV = L_LK + 3 * LA_UNIT;
constexpr int LDS_BYTES = 163840;

#define STAMP(kid, i) do {} while (0)
#define DBG_DECL
#define DBG_ARG
#define DBG_PASS
#define DBG_OUT() do {} while (0)
#define BST 2
#define OB0 1
#define OB1 2
#define OB2 3
#define OB3 4
#define TPRIO(x) __builtin_amdgcn_s_setprio(x)
#define SBAR() __builtin_amdgcn_sched_barrier(0)
#define PIN(x) asm volatile("" : "+v"(x))
#define MFMA32(a, b, c) __builtin_amdgcn_mfma_f32_32x32x16_bf16(__builtin_bit_cast(bfv8, a), __builtin_bit_cast(bfv8, b), c, 0, 0, 0)
#define MFMA8(a, b, c) __builtin_amdgcn_mfma_f32_32x32x8bf16_1k(a, b, c, 0, 0, 0)
#define MFMA16(a, b, c) __builtin_amdgcn_mfma_f32_16x16x32_bf16(__builtin_bit_cast(bfv8, a), __builtin_bit_cast(bfv8, b), c, 0, 0, 0)
#define RLX_AGENT __ATOMIC_RELAXED, __HIP_MEMORY_SCOPE_AGENT
#define SC1 16

template <int V> struct IC { static constexpr int value = V; };
template <int I, int N, class F> __device__ __forceinline__ void sfor(F&& f) { if constexpr (I < N) { f(IC<I>{}); sfor<I + 1, N>(f); } }
template <int N> __device__ __forceinline__ void wait_bar() { asm volatile("s_waitcnt vmcnt(%0) lgkmcnt(0)\n\ts_barrier" :: "i"(N) : "memory"); }
template <int N> __device__ __forceinline__ void waitv_bar() { asm volatile("s_waitcnt vmcnt(%0)\n\ts_barrier" :: "i"(N) : "memory"); }

__device__ __forceinline__ unsigned cvtpk(float lo, float hi) {
  bfv2 r; r[0] = (__bf16)lo; r[1] = (__bf16)hi;
  return __builtin_bit_cast(unsigned, r);
}
__device__ __forceinline__ float dot2(unsigned a, unsigned b, float c) {
  return __builtin_amdgcn_fdot2_f32_bf16(__builtin_bit_cast(bfv2, a), __builtin_bit_cast(bfv2, b), c, false);
}
template <int OFF> __device__ __forceinline__ void dma(const char* base, unsigned voff, unsigned lds_base) {
  asm volatile("s_add_u32 m0, %2, %3\n\ts_nop 0\n\tglobal_load_lds_dwordx4 %0, %1" :: "v"(voff), "s"(base), "s"(lds_base), "i"(OFF) : "memory", "scc");
}
template <int OFF> __device__ __forceinline__ void dma_sc1(const char* base, unsigned voff, unsigned lds_base) {
  asm volatile("s_add_u32 m0, %2, %3\n\ts_nop 0\n\tglobal_load_lds_dwordx4 %0, %1 sc1" :: "v"(voff), "s"(base), "s"(lds_base), "i"(OFF) : "memory", "scc");
}
template <int OFF> __device__ __forceinline__ void dma2_sc1(const char* base, unsigned voff, unsigned lds_base) {
  asm volatile("s_add_u32 m0, %2, %3\n\ts_nop 0\n\tglobal_load_lds_dwordx4 %0, %1 sc1\n\tglobal_load_lds_dwordx4 %0, %1 offset:1024 sc1"
               :: "v"(voff), "s"(base), "s"(lds_base), "i"(OFF) : "memory", "scc");
}
template <int OFF> __device__ __forceinline__ void dma3_sc1(const char* base, unsigned voff, unsigned lds_base) {
  asm volatile("s_add_u32 m0, %2, %3\n\ts_nop 0\n\tglobal_load_lds_dwordx4 %0, %1 sc1\n\tglobal_load_lds_dwordx4 %0, %1 offset:1024 sc1\n\t"
               "global_load_lds_dwordx4 %0, %1 offset:2048 sc1" :: "v"(voff), "s"(base), "s"(lds_base), "i"(OFF) : "memory", "scc");
}
typedef short v4i16_t __attribute__((ext_vector_type(4)));
__device__ __forceinline__ s16x4 trd(lds_cptr p) {
  return __builtin_bit_cast(s16x4, __builtin_amdgcn_ds_read_tr16_b64_v4i16((LAS v4i16_t*)p));
}
__device__ __forceinline__ bf16x8 cat8(s16x4 a, s16x4 b) { return __builtin_shufflevector(a, b, 0, 1, 2, 3, 4, 5, 6, 7); }
__device__ __forceinline__ bf16x8 ldsr128(lds_cptr p) { return *(const LAS bf16x8*)p; }
__device__ __forceinline__ s16x4 ldsr64(lds_cptr p) { return *(const LAS s16x4*)p; }
__device__ __forceinline__ u32x4 pack8(const f32x16& a, int s, float sc) {
  return u32x4{cvtpk(a[8 * s] * sc, a[8 * s + 1] * sc), cvtpk(a[8 * s + 2] * sc, a[8 * s + 3] * sc),
               cvtpk(a[8 * s + 4] * sc, a[8 * s + 5] * sc), cvtpk(a[8 * s + 6] * sc, a[8 * s + 7] * sc)};
}
__device__ __forceinline__ int swap23(int r) { return (r & 0x13) | ((r & 4) << 1) | ((r & 8) >> 1); }
__device__ __forceinline__ u64 uni64(unsigned lo, unsigned hi) {
  return ((u64)(unsigned)__builtin_amdgcn_readfirstlane(hi) << 32) | (unsigned)__builtin_amdgcn_readfirstlane(lo);
}

typedef __amdgpu_buffer_rsrc_t rsrc_t;
__device__ __forceinline__ void st128(rsrc_t rs, unsigned off, u32x4 v) { __builtin_amdgcn_raw_buffer_store_b128(v, rs, off, 0, SC1); }
__device__ __forceinline__ void st64(rsrc_t rs, unsigned off, u32x2 v) { __builtin_amdgcn_raw_buffer_store_b64(v, rs, off, 0, SC1); }
__device__ __forceinline__ u32x4 ld128(rsrc_t rs, unsigned off) { return __builtin_amdgcn_raw_buffer_load_b128(rs, off, 0, SC1); }
__device__ __forceinline__ u32x2 ld64(rsrc_t rs, unsigned off) { return __builtin_amdgcn_raw_buffer_load_b64(rs, off, 0, SC1); }
__device__ __forceinline__ unsigned ld32(rsrc_t rs, unsigned off) { return __builtin_amdgcn_raw_buffer_load_b32(rs, off, 0, SC1); }

template <int SLEEP = 6, int ROT = 0>
__device__ __forceinline__ void wait_flags(gu64* flg, unsigned idx, u64 tag) {
  bool ok = false;
#pragma unroll 1
  for (unsigned spins = 0; spins < (1u << 18); ++spins) {
    if (!ok) ok = __hip_atomic_load(flg + (idx + min(spins, (unsigned)ROT) * 256) * 16, RLX_AGENT) == tag;
    if (__all(ok)) break;
    __builtin_amdgcn_s_sleep(SLEEP);
  }
  asm volatile("" ::: "memory");
}
__device__ __forceinline__ void sample_flags(u64& fa, u64& fb, unsigned ia, unsigned ib, const char* base, u64 miss) {
  asm volatile("s_cmp_eq_u64 %5, 0\n\ts_cbranch_scc1 1f\n\tglobal_load_dwordx2 %0, %2, %4 sc1\n\tglobal_load_dwordx2 %1, %3, %4 sc1\n1:"
               : "+v"(fa), "+v"(fb) : "v"(ia * 128), "v"(ib * 128), "s"(base), "s"(miss) : "memory", "scc");
}

__device__ __forceinline__ unsigned o_main(int tok, int h, int hh) {
  const int m = h >> 1;
  return (unsigned)((((tok >> 4) * 20 + 10 * (m >> 3) + (m & 7)) * 64 + (2 * (h & 1) + hh) * 16 + (tok & 15)) * 16);
}
__device__ __forceinline__ unsigned o_tail(int tok, int h) {
  const int m = h >> 1;
  return (unsigned)(((tok >> 4) * 20 + 10 * (m >> 3) + 8 + ((m >> 2) & 1)) * 1024 + 512 * (m & 1) + ((2 * ((m >> 1) & 1) + (h & 1)) * 16 + (tok & 15)) * 8);
}

#define G1_ORDER "BBBBBBBBBBAAAAAAAAAA"
#define G1_PUB 14
#define G1_EP 12
#define G1_SAMP 72
#define G1_LOOK 76
#define G1_AGAIN true
struct G1 {
  int isB[24] = {}, kb[24] = {}, uf[25] = {};
  int lastB = 0;
  int iss_lo[20] = {}, iss_hi[20] = {};
  int wait[20] = {};
  constexpr G1() {
    const char* ord = G1_ORDER;
    int nb = 0, na = 0;
    for (int o = 0; o < 24; ++o) {
      isB[o] = o < 20 && ord[o] == 'B'; kb[o] = o < 20 ? (isB[o] ? nb++ : na++) : 0;
      uf[o + 1] = uf[o] + (o < 20 ? 1 + isB[o] : 0);
      if (isB[o]) lastB = o;
    }
    int pos[60] = {}, cnt = 0, lastst = 0, done = 2 * uf[2];
    for (int d = 0; d < done; ++d) pos[d] = cnt++;
    for (int o = 0; o < 20; ++o) {
      int need = pos[2 * uf[o + 1] - 1];
      if (o == G1_PUB && lastst > need) need = lastst;
      wait[o] = o >= 18 ? 0 : cnt - 1 - need;
      if (o == G1_EP) lastst = cnt++;
      iss_lo[o] = done; iss_hi[o] = 2 * uf[o + 4];
      for (int ks = 0; ks < 4; ++ks) {
        const int per = (iss_hi[o] - iss_lo[o] + 3) / 4;
        for (int z = 0; z < per && done < iss_hi[o]; ++z) pos[done++] = cnt++;
        const int n = 4 * o + ks - 2 - (4 * lastB + 3);
        if (n >= 0 && n < 4 * BST && n % BST == 0) lastst = cnt++;
      }
    }
  }
};
constexpr int g1_slot(int u) { return ((u + 6) % 10) * 16384; }
template <bool VT, class FPiece, class FStart>
__device__ __forceinline__ void g1_body(char* ws, rsrc_t rs, char* lds, int ti, int cj, int w, int lane, u64& tag, bool& early, unsigned fiKV, unsigned fiQ,
                                        u32x4 (&pk)[4], unsigned (&dstB)[2], FPiece&& piece, FStart&& att_start DBG_ARG) {
  constexpr G1 P;
  static_assert(P.uf[G1_EP - 1] > 20 && G1_EP < G1_PUB && P.uf[4] <= 9, "epoch / prefetch scratch slots");
  const int tg = w & 3, cg = w >> 2;
  const int r = lane & 31, hh = lane >> 5, g16 = (lane >> 4) & 1, q4 = (lane & 15) >> 2, p4 = lane & 3;
  const int tr = VT ? swap23(r) : r;
#pragma unroll
  for (int t2 = 0; t2 < 2; ++t2) {
    if (VT) {
      const int F = (cj - 10) * 128 + 64 * cg + 32 * t2 + r, hd = F / 20;
      int d = F - 20 * hd - 4 * (hd & 1); if (d < 0) d += 20;
      dstB[t2] = WS_VB + (unsigned)((hd * 64 + 4 * ti + tg) * 2048 + hh * 512 + d * 16);
    } else {
      dstB[t2] = WS_QKB + (unsigned)(cj * 16 + 8 * cg + 4 * t2 + hh) * UNIT + (unsigned)(128 * ti + 32 * tg + r) * 16;
    }
  }
  unsigned xoff[4];
#pragma unroll
  for (int ks = 0; ks < 4; ++ks) xoff[ks] = (unsigned)((32 * tg + tr) * 128 + (((2 * ks + hh) ^ ((tr >> 1) & 7)) * 16));
  const int psw = ((p4 & 1) << 1) | (p4 >> 1);
  unsigned woff[2];
#pragma unroll
  for (int t2 = 0; t2 < 2; ++t2) woff[t2] = (unsigned)((8 * hh + q4) * 256 + (((2 * cg + t2) ^ q4) * 64) + 32 * g16 + 8 * (VT ? p4 : psw));
  const float sc = (!VT && cj < 5) ? QSCALE : 1.f;
  bf16x8 xf[40], wf[3][2];
  u64 fa, fb;
  f32x16 accB[2], accA[2];
  u64 miss = 1;
  auto look = [&](bool again) AINL {
    asm volatile("s_cmp_eq_u64 %2, 0\n\ts_cbranch_scc1 1f\n\ts_waitcnt vmcnt(0)\n1:" : "+v"(fa), "+v"(fb) : "s"(miss) : "memory", "scc");
    const u64 m = __ballot(fa != tag || fb != tag);
    if (m == 0 && miss != 0) att_start();
    miss = m;
    if (again) sample_flags(fa, fb, fiKV, fiQ, ws + (F1B + REPSTEP) * 128, miss);
  };
  sfor<0, 82>([&](auto ic) AINL {
    constexpr int nn = decltype(ic)::value, n = nn - 2;
    constexpr int o = nn >> 2, ks = nn & 3;
    if constexpr (nn < 80) {
      if constexpr (ks == 0) {
        if constexpr (o < 19) waitv_bar<P.wait[o]>();
        if constexpr (o == G1_EP) dma_sc1<L_EP>(ws, 0, (unsigned)(uintptr_t)lds + w * 1024);
        if constexpr (o == G1_PUB) { const u32x2 ep = *(const LAS u32x2*)((lds_cptr)lds + L_EP + w * 1024); tag = uni64(ep[0], ep[1]) + TAG_STEP; }
        if constexpr (o == G1_PUB) { if (w == 0 && lane < NREP) __hip_atomic_store((gu64*)ws + (F1B + lane * 256 + cj * 16 + ti) * 16, tag, RLX_AGENT); }
        if constexpr (o % 2 == 0) STAMP(0, 1 + o / 2);
      }
      if constexpr (nn == G1_SAMP) { fa = 0; fb = 0; sample_flags(fa, fb, fiKV, fiQ, ws + F1B * 128, miss); }
      if constexpr (nn == G1_LOOK) look(G1_AGAIN);
      if constexpr (P.isB[o]) xf[4 * P.kb[o] + ks] = ldsr128((lds_cptr)lds + g1_slot(P.uf[o] + 1) + xoff[ks]);
#pragma unroll
      for (int t2 = 0; t2 < 2; ++t2) {
        const lds_cptr wp = (lds_cptr)lds + g1_slot(P.uf[o]) + woff[t2] + ks * 4096;
        wf[nn % 3][t2] = cat8(trd(wp), trd(wp + 1024));
      }
      constexpr int per = (P.iss_hi[o] - P.iss_lo[o] + 3) / 4, d0 = P.iss_lo[o] + per * ks;
      sfor<d0, (d0 + per < P.iss_hi[o] ? d0 + per : P.iss_hi[o])>(piece);
    }
    if constexpr (n >= 0) {
      constexpr int om = n >> 2, xi = 4 * P.kb[om] + (n & 3);
#pragma unroll
      for (int t2 = 0; t2 < 2; ++t2) {
        f32x16& ac = P.isB[om] ? accB[t2] : accA[t2];
        const f32x16 cin = xi == 0 ? f32x16{} : ac;
        ac = VT ? MFMA32(xf[xi], wf[n % 3][t2], cin) : MFMA32(wf[n % 3][t2], xf[xi], cin);
      }
    }
    SBAR();
    constexpr int nb = n - (4 * P.lastB + 3);
    if constexpr (nb == 0) {
#pragma unroll
      for (int i = 0; i < 4; ++i) { pk[i] = VT ? pack8(accB[i >> 1], i & 1, 1.f) : pack8(accB[i >> 1], i & 1, sc); PIN(pk[i]); }
    }
    if constexpr (nb >= 0 && nb < 4 * BST && nb % BST == 0) {
      constexpr int i = nb / BST;
      st128(rs, dstB[i >> 1] + (i & 1) * (VT ? 1024 : 2 * UNIT), pk[i]);
      SBAR();
    }
  });
  STAMP(0, 13);
  look(false);
  early = miss == 0;
#pragma unroll
  for (int i = 0; i < 4; ++i) pk[i] = VT ? pack8(accA[i >> 1], i & 1, 1.f) : pack8(accA[i >> 1], i & 1, sc);
}

__device__ __forceinline__ f32x16 smf(bf16x8 a, const f32x16& p, int s, f32x16 o, int idx) {
  f32x8 b = s ? __builtin_shufflevector(p, p, 8, 9, 10, 11, 12, 13, 14, 15) : __builtin_shufflevector(p, p, 0, 1, 2, 3, 4, 5, 6, 7);
  return __builtin_amdgcn_smfmac_f32_32x32x32_bf16(__builtin_bit_cast(bfv8, a), __builtin_bit_cast(bfv16, b), o, idx, 0, 0);
}
__device__ __forceinline__ float max16(const f32x16& s) {
  float a = fmaxf(fmaxf(s[0], s[1]), s[2]), b = fmaxf(fmaxf(s[3], s[4]), s[5]);
  a = fmaxf(fmaxf(a, s[6]), s[7]); b = fmaxf(fmaxf(b, s[8]), s[9]);
  a = fmaxf(fmaxf(a, s[10]), s[11]); b = fmaxf(fmaxf(b, s[12]), s[13]);
  a = fmaxf(fmaxf(a, s[14]), s[15]);
  return fmaxf(a, b);
}
__device__ __forceinline__ float swap_max(float m) {
  auto rr = __builtin_amdgcn_permlane32_swap(__float_as_uint(m), __float_as_uint(m), false, false);
  return fmaxf(__uint_as_float(rr[0]), __uint_as_float(rr[1]));
}
__device__ __forceinline__ float swap_add(float m) {
  auto rr = __builtin_amdgcn_permlane32_swap(__float_as_uint(m), __float_as_uint(m), false, false);
  return __uint_as_float(rr[0]) + __uint_as_float(rr[1]);
}
__device__ __forceinline__ void set_ref(s16x4& q1, float m, bool spare) {
  const unsigned a = __float_as_uint(-m) & 0xffff0000u;
  const float r1 = -m - __uint_as_float(a);
  const unsigned b = __float_as_uint(r1) & 0xffff0000u;
  const unsigned c = __float_as_uint(r1 - __uint_as_float(b));
  if (spare) q1 = __builtin_bit_cast(s16x4, u32x2{(a >> 16) | b, c >> 16});
}

__global__ __launch_bounds__(512, 2) void fused_kernel(const char* __restrict__ x, const char* __restrict__ wA, const char* __restrict__ wB,
                                                       const char* __restrict__ woA, const char* __restrict__ woB,
                                                       const char* __restrict__ gw, char* ws, char* __restrict__ out) {
  extern __shared__ __attribute__((aligned(16))) char lds[];
  const int tid = threadIdx.x, lane = tid & 63;
  const int w = __builtin_amdgcn_readfirstlane(tid >> 6);
  const int xcd = blockIdx.x & 7, slot = blockIdx.x >> 3;
  const unsigned lds0 = (unsigned)(uintptr_t)lds;
  gu64* flg = (gu64*)ws;
  const rsrc_t rs = __builtin_amdgcn_make_buffer_rsrc(ws, 0, WS_END, 0x00020000);
  DBG_DECL
  STAMP(0, 0);
  const int ti = 8 * (xcd & 1) + (slot & 7), cj = 4 * (xcd >> 1) + (slot >> 3);
  const unsigned ldpf = lds0 + (cj < 15 ? L_PF : L_RING);
  if (w == 6) {
    u64 pc; unsigned len;
    asm volatile("s_getpc_b64 %0\nGM_CODE_BEG:\n\ts_mov_b32 %1, GM_CODE_END-GM_CODE_BEG" : "=s"(pc), "=s"(len));
    asm volatile("s_add_u32 m0, %2, %3\n\ts_nop 0\n\tglobal_load_lds_dword %0, %1"
                 :: "v"(min((unsigned)((slot & 7) * 64 + lane) * 128, len)), "s"(pc), "s"(ldpf), "i"(0) : "memory", "scc");
  }
  const int rot = (ti + 2 * cj) % 10;
  unsigned xlo[2], wlo[2];
#pragma unroll
  for (int i = 0; i < 2; ++i) {
    const int row = 16 * w + 8 * i + (lane >> 3);
    xlo[i] = (unsigned)(row * (D_ * 2) + (((lane & 7) ^ ((row >> 1) & 7)) * 16));
    wlo[i] = (unsigned)((8 * w + 4 * i + (lane >> 4)) * (N3_ * 2) + (((lane & 15) ^ (4 * (lane >> 4))) * 16));
  }
  const char* xb = x + (size_t)(128 * ti) * (D_ * 2);
  const char* wBb = wB + cj * 256;
  const char* wAb = wA + cj * 256;
  const unsigned ldw = lds0 + w * 2048;
  auto piece = [&](auto dc) AINL {
    constexpr G1 P;
    constexpr int d = decltype(dc)::value, u = d >> 1, base = g1_slot(u) + (d & 1) * 1024;
    constexpr int o = [&]() { int q = 0; while (P.uf[q + 1] <= u) ++q; return q; }();
    int kb = P.kb[o] + rot; if (kb >= 10) kb -= 10;
    if constexpr (u != P.uf[o]) dma<base>(xb + kb * 128, xlo[d & 1], ldw);
    else dma<base>((P.isB[o] ? wBb : wAb) + (size_t)kb * (64 * N3_ * 2), wlo[d & 1], ldw);
  };
  if (cj < 15) {
    sfor<0, 4>(piece);
    sfor<4, 8>(piece);
  }
#define TLB_STRIDE 0
  STAMP(0, 14);
  SBAR();
  u64 tag;
  const int h = 4 * xcd + (slot & 3), c = slot >> 2;
  const int e = h & 1, ua = 5 * (h >> 1) + 3 * e, us = 5 * (h >> 1) + 2;
  const int jlo = (5 * (h >> 1) + 2 * e) >> 4, jhi = (5 * (h >> 1) + 2 * e + 2) >> 4;
  const int kh = w >> 2, tq = w & 3;
  const char* kbase = ws + WS_QKB + (size_t)(80 + (tq >= 2 ? us : ua + tq)) * UNIT + 1024 * kh * 16;
  const unsigned crow = WS_C + blockIdx.x * 4096 + ((lane & 31) == 20 ? 0 : 16);
  if (w == 5) { const unsigned cv = (lane & 1) ? 0u : 0x3f803f80u; st128(rs, lane < 8 ? WS_C + blockIdx.x * 4096 + (lane >> 1) * 1024 + (lane & 1) * 16 : OOB, u32x4{cv, cv, cv, cv}); }
  const unsigned ldr = lds0 + kh * AT_HALF + tq * 2048;
  unsigned vlo = (lane & 31) < 20 ? WS_VB + (h * 64 + 32 * kh + tq) * 2048 + lane * 16 : crow, vstr = (lane & 31) < 20 ? 8192u : 0u;
  auto kpiece = [&](auto stc) AINL {
    constexpr int st = decltype(stc)::value;
    if (tq < 3) dma2_sc1<L_RING + (st & 1) * AT_STAGE>(kbase + st * 2048, lane * 16, ldr);
  };
  auto vpiece = [&](auto stc) AINL {
    constexpr int st = decltype(stc)::value;
    dma2_sc1<L_RING + (st & 1) * AT_STAGE + 6144>(ws, vlo + st * vstr, ldr);
  };
  const unsigned fiKV = (5 + 5 * (lane >> 5) + ((lane & 16) ? jhi : jlo)) * 16 + (lane & 15), fiQ = ((lane & 1) ? jhi : jlo) * 16 + 2 * c + ((lane >> 1) & 1);
  auto att_start = [&]() AINL {
    kpiece(IC<0>{});
    if (tq == 3) {
      const char* qb = ws + WS_QKB + (256 * c + 128 * kh) * 16;
      dma2_sc1<L_Q>(qb + (size_t)ua * UNIT, lane * 16, lds0 + kh * 2048);
      dma2_sc1<L_Q + 4096>(qb + (size_t)(ua + 1) * UNIT, lane * 16, lds0 + kh * 2048);
      dma2_sc1<L_Q + 8192>(qb + (size_t)us * UNIT, lane * 16, lds0 + kh * 2048);
    }
    vpiece(IC<0>{});
  };

  bool early = false;
  u32x4 pk[4]; unsigned dstA[2];
  if (cj < 10) g1_body<false>(ws, rs, lds, ti, cj, w, lane, tag, early, fiKV, fiQ, pk, dstA, piece, att_start DBG_PASS);
  else if (cj < 15) g1_body<true>(ws, rs, lds, ti, cj, w, lane, tag, early, fiKV, fiQ, pk, dstA, piece, att_start DBG_PASS);
  else {
    const char* wo = ((ti & 8) ? woB : woA) + (size_t)(80 * w) * (D_ * 2) + (ti & 7) * 160;
#pragma unroll 1
    for (int p = 0; p < 13; ++p) {
      const int s0 = min(64 * p, 736), sl = s0 + lane;
      dma<0>(wo, (unsigned)((sl / 10) * (D_ * 2) + (sl % 10) * 16), lds0 + w * 12800 + s0 * 16);
    }
    const u64 ep = __hip_atomic_load(flg, RLX_AGENT);
    tag = uni64((unsigned)ep, (unsigned)(ep >> 32)) + TAG_STEP;
    wait_bar<0>();
    const int g4 = lane >> 4, rl = 320 * kh + 4 * (g4 & 1) + ((lane & 15) >> 2);
#pragma unroll
    for (int ff = 0; ff < 13; ++ff) {
      const int f = min(tq + 4 * ff, 49), j = f / 5, n = f - 5 * j;
      const int row = rl + (j < 8 ? 40 * j + 24 * (g4 >> 1) : 160 * (j - 8) + 80 * (g4 >> 1) + 16);
      const lds_cptr p = (lds_cptr)lds + row * 160 + n * 32 + 8 * (lane & 3);
      st128(rs, WS_WO + (unsigned)(ti * 100 + kh * 50 + f) * 1024 + lane * 16, __builtin_bit_cast(u32x4, cat8(trd(p), trd(p + (j < 8 ? 8 : 40) * 160))));
    }
    wait_bar<0>();
    if (tid == 0) __hip_atomic_store(flg + (FG + ti) * 16, tag, RLX_AGENT);
    if (w == 0) { wait_flags<16>(flg, F1B + (NREP - 1) * 256 + jlo * 16 + 2 * c, tag); }
    wait_bar<63>();
    dstA[0] = OOB; dstA[1] = OOB;
#pragma unroll
    for (int i = 0; i < 4; ++i) pk[i] = u32x4{0u, 0u, 0u, 0u};
  }
  const unsigned flag1 = F1A + cj * 16 + ti;

  STAMP(1, 0);
  const int hh = lane >> 5, r = lane & 31;
  if (!early) {
    bool oka = false, okb = false;
#pragma unroll 1
    for (unsigned spins = 0; spins < (1u << 18); ++spins) {
      const unsigned rp = F1B + (2 + min(spins, NREP - 4)) * REPSTEP;
      const u64 a = __hip_atomic_load(flg + (rp + (oka ? fiQ : fiKV)) * 16, RLX_AGENT), b = __hip_atomic_load(flg + (rp + fiQ) * 16, RLX_AGENT);
      oka = oka || a == tag; okb = okb || b == tag;
      if (__all(oka && okb)) break;
      __builtin_amdgcn_s_sleep(2);
    }
    asm volatile("" ::: "memory");
    att_start();
  }
  STAMP(1, 1);
  const int rt = 4 * xcd + (slot & 3), ct = slot >> 2;
  const char* wob = ws + WS_WO + (size_t)ct * 102400 + w * 3072;
  auto woquarter = [&](auto brc, auto qc, auto dc) AINL {
    constexpr int off = decltype(brc)::value * 819200 + decltype(qc)::value * WQ, d = decltype(dc)::value;
    dma3_sc1<d>(wob + off, lane * 16, lds0 + w * 3072);
    if (w == 0) dma_sc1<d + 24576>(wob + off + 24576, lane * 16, lds0);
  };
  unsigned osrc = (unsigned)((rt * 4 + tq) * 20 + 10 * kh) * 1024 + lane * 16;
  float gate;
  unsigned fiA = lane < 18 ? F1A + (5 * (lane / 6) + (((lane / 3) & 1) ? jhi : jlo)) * 16 + max(2 * c - 1 + lane % 3, 0) : FG + (lane == 18 ? ct : 8 + ct);
  const unsigned fiOA = FOA + (rt >> 2) * 32 + r;
  unsigned obm = WS_OB + o_main(256 * c + 64 * tq + 32 * kh + r, h, hh), obt = hh ? OOB : WS_OB + o_tail(256 * c + 64 * tq + 32 * kh + r, h);
  int idx = 0xDDDD;
  const lds_cptr ring = (lds_cptr)lds + L_RING + kh * AT_HALF;
  lds_cptr kp0 = ring + hh * 2048 + r * 16;
  lds_cptr kp1 = (hh == e) ? ring + 2 * 2048 + r * 16 + 8 * hh : ring + 6144 + 20 * 16;
  lds_cptr vp = ring + 6144 + hh * 512 + r * 16;
  lds_cptr qp = (lds_cptr)lds + L_Q + (64 * tq + r) * 16;
  u32x4 lq; u32x2 lqt;
  unsigned lalo = w < 3 ? WS_QKB + BR_A + (80 + (w == 2 ? us : ua + w)) * UNIT + (256 * c - 16 + lane) * 16
                        : r < 20 ? WS_VB + BR_A + (h * 64 + 8 * c) * 2048 - 1024 + min(4 * (w - 3), 13) * 1024 + lane * 16 : crow;
  const unsigned ladst = lds0 + (w < 3 ? L_LK + w * LA_UNIT : L_LV + min(4 * (w - 3), 13) * 1024);
  f32x16 oa = f32x16{}, ob = f32x16{}, sa, sb;
  PIN(osrc); PIN(fiA); PIN(obm); PIN(obt); PIN(idx); PIN(kp0); PIN(kp1); PIN(vp); PIN(qp); PIN(lalo); PIN(vlo); PIN(vstr); PIN(oa); PIN(ob);
  wait_bar<0>();
  STAMP(1, 2);
  bf16x8 q0[2]; s16x4 q1[2];
#pragma unroll
  for (int t = 0; t < 2; ++t) {
    q0[t] = ldsr128(qp + hh * 4096 + ((t ^ kh ^ 1) * 512));
    q1[t] = ldsr64(qp + 8192 + 8 * e + ((t ^ kh ^ 1) * 512));
  }
  const bf16x8 kr0 = ldsr128(kp0 - kh * AT_HALF); const s16x4 kr1 = ldsr64(kp1 - kh * AT_HALF);
  bf16x8 k0 = ldsr128(kp0); s16x4 k1 = ldsr64(kp1);
  bf16x8 v0 = ldsr128(vp), v1 = ldsr128(vp + 1024);
  bf16x8 v1p = v1;
  SBAR();
  {
    set_ref(q1[0], 0.f, hh != e); set_ref(q1[1], 0.f, hh != e);
    f32x16 s = MFMA32(kr0, q0[0], f32x16{}), t = MFMA32(kr0, q0[1], f32x16{});
    s = MFMA8(kr1, q1[0], s); t = MFMA8(kr1, q1[1], t);
    sa = MFMA32(k0, q0[0], f32x16{});
    SBAR();
    kpiece(IC<1>{}); vpiece(IC<1>{});
    SBAR();
    set_ref(q1[0], swap_max(max16(s)), hh != e);
    sa = MFMA8(k1, q1[0], sa);
    SBAR();
    set_ref(q1[1], swap_max(max16(t)), hh != e);
  }
  STAMP(1, 3);
  u32x4 of[10];
  unsigned gx[5], gg[5]; float gs;

  auto local_attention = [&]() AINL {
    const int tok = 256 * c + 32 * w + r;
    const lds_cptr lk = (lds_cptr)lds + L_LK + (32 * w + r) * 16, lv = (lds_cptr)lds + L_LV + (4 * w + hh) * 512 + r * 16;
    const bf16x8 qa = __builtin_bit_cast(bf16x8, lq);
    const s16x4 qb = __builtin_bit_cast(s16x4, (hh == e) ? lqt : u32x2{0u, 0u});
    f32x16 sx = MFMA32(ldsr128(lk + hh * LA_UNIT), qa, f32x16{}), sy = MFMA32(ldsr128(lk + hh * LA_UNIT + 512), qa, f32x16{});
    sx = MFMA8(ldsr64(lk + 2 * LA_UNIT + 8 * e), qb, sx); sy = MFMA8(ldsr64(lk + 2 * LA_UNIT + 8 * e + 512), qb, sy);
    const bf16x8 va = ldsr128(lv), vb = ldsr128(lv + 1024), vc = ldsr128(lv + 2048);
    const u64 edge = (c == 0 && w == 0) ? 0ull : ~0ull;
    const float ninf = -1e9f * 1.4426950408889634f;
    sfor<0, 24>([&](auto jc) AINL {
      constexpr int j = decltype(jc)::value;
      constexpr u64 mk = []() { u64 m = 0; for (int l = 0; l < 64; ++l) { const int d = (l & 31) + 16 - ((j & 3) + 8 * (j >> 2) + 4 * (l >> 5)); if (d >= 0 && d <= 14 && !(d & 1)) m |= 1ull << l; } return m; }();
      const bool ok = __builtin_amdgcn_inverse_ballot_w64(j < 8 ? mk & edge : mk);
      if constexpr (j < 16) sx[j] = ok ? sx[j] : ninf; else sy[j - 16] = ok ? sy[j - 16] : ninf;
    });
    float m = fmaxf(fmaxf(max16(sx), sy[0]), sy[1]);
    m = fmaxf(fmaxf(m, sy[2]), sy[3]); m = fmaxf(fmaxf(m, sy[4]), sy[5]); m = swap_max(fmaxf(fmaxf(m, sy[6]), sy[7]));
#pragma unroll
    for (int j = 0; j < 16; ++j) sx[j] = __builtin_amdgcn_exp2f(sx[j] - m);
#pragma unroll
    for (int j = 0; j < 8; ++j) sy[j] = __builtin_amdgcn_exp2f(sy[j] - m);
    f32x16 o = smf(va, sx, 0, f32x16{}, idx); o = smf(vb, sx, 1, o, idx); o = smf(vc, sy, 0, o, idx);
    auto lr = __builtin_amdgcn_permlane32_swap(__float_as_uint(o[8]), __float_as_uint(o[8]), false, false);
    const float rl = __builtin_amdgcn_rcpf(__uint_as_float(lr[1]));
    st128(rs, WS_OA + o_main(tok, h, hh), pack8(o, 0, rl));
    st64(rs, hh ? OOB : WS_OA + o_tail(tok, h), u32x2{cvtpk(o[8] * rl, o[9] * rl), cvtpk(o[10] * rl, o[11] * rl)});
    gs += __builtin_bit_cast(float, __builtin_amdgcn_update_dpp(0, __builtin_bit_cast(int, gs), 0xB1, 0xf, 0xf, true));
    gs += __builtin_bit_cast(float, __builtin_amdgcn_update_dpp(0, __builtin_bit_cast(int, gs), 0x4E, 0xf, 0xf, true));
    gs += __builtin_bit_cast(float, __builtin_amdgcn_update_dpp(0, __builtin_bit_cast(int, gs), 0x141, 0xf, 0xf, true));
    gs += __builtin_bit_cast(float, __builtin_amdgcn_update_dpp(0, __builtin_bit_cast(int, gs), 0x140, 0xf, 0xf, true));
    { auto g2 = __builtin_amdgcn_permlane16_swap(__float_as_uint(gs), __float_as_uint(gs), false, false); gs = __uint_as_float(g2[0]) + __uint_as_float(g2[1]); }
    gs = swap_add(gs);
    const float gv = __builtin_amdgcn_rcpf(1.f + __builtin_amdgcn_exp2f(-1.4426950408889634f * gs));
    __builtin_amdgcn_raw_buffer_store_b32(__float_as_uint(gv), rs, lane == 0 ? WS_G + (256 * c + 8 * h + w) * 4 : OOB, 0, SC1);
    SBAR();
  };

  const lds_cptr wl = (lds_cptr)lds + L_R + kh * (2 * WQ) + lane * 16;
  f32x4 accA[5];
  bf16x8 wq[2][5];
  auto opa = [&](auto hc, auto gc) AINL {
    constexpr int hs = decltype(hc)::value, g = decltype(gc)::value;
    if constexpr (hs >= 0 && hs <= 5) SBAR();
    if constexpr (hs >= 1 && hs <= 5) accA[g] = MFMA16(wq[(hs - 1) & 1][g], of[hs - 1], hs == 1 ? f32x4{} : accA[g]);
    if constexpr (hs >= 0 && hs < 5) wq[hs & 1][g] = ldsr128(wl + (hs * 5 + g) * 1024);
    if constexpr (hs >= 0 && hs <= 5) SBAR();
  };
#define EX(S, j) S[j] = __builtin_amdgcn_exp2f(S[j])
#define EX2(S, j) do { EX(S, j); EX(S, j + 1); } while (0)
#define EX4(S, j) do { EX(S, j); EX(S, j + 1); EX(S, j + 2); EX(S, j + 3); } while (0)
  sfor<0, 8>([&](auto stc) AINL {
    constexpr int st = decltype(stc)::value;
    constexpr int cur = (st & 1) * AT_STAGE, nxt = ((st + 1) & 1) * AT_STAGE;
    sfor<0, 4>([&](auto ic) AINL {
      constexpr int i = decltype(ic)::value;
      constexpr int nk = (i < 3) ? cur + (i + 1) * 512 : nxt, nv = (i < 3) ? cur + (i + 1) * 2048 : nxt;
      constexpr int sl = (i + 1) & 3, sst = (i == 3) ? st + 1 : st;
      constexpr int ha = st == 6 ? 2 * i : -9, hb = st == 6 ? 2 * i + 1 : -9;
      SBAR();
      EX2(sa, 0); PIN(sa); SBAR();
      if constexpr (st + i > 0) ob = smf(v1p, sb, 1, ob, idx);
      SBAR(); EX2(sa, 2); opa(IC<ha>{}, IC<0>{}); EX2(sa, 4); PIN(sa); SBAR();
      sb = MFMA32(k0, q0[1], f32x16{}); SBAR(); EX2(sa, 6); opa(IC<ha>{}, IC<1>{}); EX2(sa, 8); PIN(sa); SBAR();
      sb = MFMA8(k1, q1[1], sb); SBAR();
      if constexpr (i == 3) {
        wait_bar<(st == 3 || st == 4) ? 3 : st == 7 ? 63 : 0>();
        if constexpr (st == 0) { if (tid == 0) __hip_atomic_store(flg + flag1 * 16, tag, RLX_AGENT); }
        if constexpr (st == 2) {
          PIN(lq); PIN(lqt);
        }
        if constexpr (st == 1) {
          gs = 0.f;
#pragma unroll
          for (int i = 0; i < 5; ++i) gs = dot2(gx[i], gg[i], gs);
          PIN(gs);
        }
        if constexpr (st == 3) { if (tid == 0) __hip_atomic_store(flg + (FOA + c * 32 + h) * 16, tag, RLX_AGENT); }
        if constexpr (st == 5) {
#pragma unroll
          for (int j = 0; j < 5; ++j) PIN(of[j]);
        }
        if constexpr (st == 1 || st == 4) {
          if (!__all(*(const LAS u64*)((lds_cptr)lds + L_FL + lane * 16) == tag)) wait_flags(flg, st == 1 ? fiA : fiOA, tag);
        }
      }
      __builtin_amdgcn_s_setprio(3 - sl);
      k0 = ldsr128(kp0 + nk); k1 = ldsr64(kp1 + nk);
      if constexpr (sst >= 1 && sst + 1 < 8 && sl == 0) kpiece(IC<sst + 1>{});
      if constexpr (sst == 0 && sl < 3) st128(rs, dstA[sl - 1] + BR_A, pk[2 * sl - 2]);
      if constexpr ((sst == 1 || sst == 4) && sl == 2) { if (w == 7) dma_sc1<L_FL>(ws, (sst == 1 ? fiA : fiOA) * 128, lds0); }
      if constexpr (sst == 2 && sl == 1) dma_sc1<0>(ws, lalo + ((c == 0 && (w == 3 || (w < 3 && lane < 16))) ? 1024 : 0), ladst);
      if constexpr (sst == 2 && sl == 2) { if (w < 3) dma_sc1<3328>(ws + 3328, lalo, ladst); }
      if constexpr ((sst == 3 || sst == 4) && (sl == 1 || sl == 2)) woquarter(IC<0>{}, IC<2 * (sst - 3) + sl - 1>{}, IC<L_R + (2 * (sst - 3) + sl - 1) * WQ>{});
      if constexpr (sst == 7 && sl == 0) woquarter(IC<1>{}, IC<1>{}, IC<L_RING>{});
      if constexpr (sst == 7 && (sl == 1 || sl == 2)) woquarter(IC<1>{}, IC<2 * sl - 2>{}, IC<L_R + (2 * sl - 2) * WQ>{});
      EX2(sa, 10); opa(IC<ha>{}, IC<2>{}); EX2(sa, 12); PIN(sa); SBAR();
      oa = smf(v0, sa, 0, oa, idx); SBAR(); EX2(sa, 14); opa(IC<ha>{}, IC<3>{}); PIN(sa); SBAR();
      EX2(sb, 0); opa(IC<ha>{}, IC<4>{}); PIN(sb); SBAR();
      oa = smf(v1, sa, 1, oa, idx); SBAR(); EX2(sb, 2); opa(IC<hb>{}, IC<0>{}); EX2(sb, 4); PIN(sb); SBAR();
      sa = MFMA32(k0, q0[0], f32x16{}); SBAR(); EX2(sb, 6); opa(IC<hb>{}, IC<1>{}); EX2(sb, 8); PIN(sb); SBAR();
      sa = MFMA8(k1, q1[0], sa); SBAR();
      if constexpr (sst >= 1 && sst + 1 < 8 && sl == 0) vpiece(IC<sst + 1>{});
      if constexpr (sst == 0 && sl < 3) st128(rs, dstA[sl - 1] + (BR_A + (cj >= 10 ? 1024 : 2 * UNIT)), pk[2 * sl - 1]);
      if constexpr (sst == 1 && sl == 1) {
        st128(rs, obm, u32x4{~0u, ~0u, ~0u, ~0u}); st64(rs, obt, u32x2{~0u, ~0u});
      }
      if constexpr (sst == 2 && sl == 1) {
        dma3_sc1<1024>(ws + 1024, lalo, ladst);
        const unsigned to = WS_QKB + BR_A + (unsigned)(256 * c + 32 * w + r) * 16;
        lq = ld128(rs, (ua + hh) * UNIT + to);
        lqt = ld64(rs, us * UNIT + to + 8 * e);
      }
      if constexpr (sst == 1 && sl == 0) {
#pragma unroll
        for (int i = 0; i < 5; ++i) { gx[i] = ((const unsigned*)(x + (size_t)(256 * c + 8 * h + w) * (D_ * 2)))[lane * 5 + i]; gg[i] = ((const unsigned*)gw)[lane * 5 + i]; }
      }
      if constexpr ((sst == 5 || sst == 7) && (sl == 1 || sl == 2)) {
#pragma unroll
        for (int j = (sst == 7 ? 5 : 0) + 3 * sl - 3; j < (sst == 7 ? 5 : 0) + (sl == 1 ? 3 : 5); ++j) {
          if (j < 8) of[j] = ld128(rs, WS_OA + osrc + j * 1024);
          else { const u32x2 a = ld64(rs, WS_OA + osrc - lane * 8 + j * 1024), b = ld64(rs, WS_OA + osrc - lane * 8 + j * 1024 + 512); of[j] = u32x4{a[0], a[1], b[0], b[1]}; }
        }
        if constexpr (sst == 7 && sl == 2) gate = __uint_as_float(ld32(rs, WS_G + (rt * 64 + tq * 16 + (lane & 15)) * 4));
      }
      v1p = v1;
      const bf16x8 v0n = ldsr128(vp + nv); v1 = ldsr128(vp + nv + 1024);
      EX2(sb, 10); opa(IC<hb>{}, IC<2>{}); EX2(sb, 12); PIN(sb); SBAR();
      ob = smf(v0, sb, 0, ob, idx); SBAR();
      v0 = v0n;
      EX(sb, 14); opa(IC<hb>{}, IC<3>{}); EX(sb, 15); opa(IC<hb>{}, IC<4>{}); PIN(sb); SBAR();
      if constexpr ((st == 2 && i == 3) || (st == 3 && i == 0)) { if (kh == (st == 3)) local_attention(); }
    });
    STAMP(1, 4 + st);
  });
  ob = smf(v1p, sb, 1, ob, idx);
  __builtin_amdgcn_s_setprio(0);
  bf16x8 wf[25];
  float ofin[12];
  {
    f32x4* xw = (f32x4*)(lds + L_RING + AT_STAGE + w * 3072) + lane;
    const f32x4* xr = (const f32x4*)(lds + L_RING + AT_STAGE + (w ^ 4) * 3072) + lane;
#pragma unroll
    for (int i = 0; i < 3; ++i) xw[i * 64] = f32x4{oa[4 * i], oa[4 * i + 1], oa[4 * i + 2], oa[4 * i + 3]};
    wait_bar<0>();
#pragma unroll
    for (int j = 5; j < 10; ++j) PIN(of[j]);
    PIN(gate);
#pragma unroll
    for (int i = 0; i < 3; ++i) { const f32x4 v = xr[i * 64]; for (int k = 0; k < 4; ++k) ofin[4 * i + k] = ob[4 * i + k] + v[k]; }
  }
  {
    auto rr = __builtin_amdgcn_permlane32_swap(__float_as_uint(ofin[8]), __float_as_uint(ofin[8]), false, false);
    float l = __uint_as_float(rr[1]);
    if (__builtin_expect(__any(!(l > 8.67e-19f && l < 1.15e18f)), 0)) {
      const bf16x8 qa = q0[1]; s16x4 qb = q1[1];
      float m = -INFINITY;
#pragma unroll 1
      for (int pass = 0; pass < 2; ++pass) {
        set_ref(qb, pass ? m : 0.f, hh != e);
        f32x16 o = f32x16{};
#pragma unroll 1
        for (int kt = 0; kt < 64; ++kt) {
          const unsigned to = (unsigned)(32 * kt + r) * 16;
          const bf16x8 a0 = __builtin_bit_cast(bf16x8, ld128(rs, WS_QKB + (80 + ua + hh) * UNIT + to));
          const s16x4 a1 = __builtin_bit_cast(s16x4, ld64(rs, hh == e ? WS_QKB + (80 + us) * UNIT + to + 8 * e : WS_C + blockIdx.x * 4096));
          f32x16 s = MFMA32(a0, qa, f32x16{}); s = MFMA8(a1, qb, s);
          if (pass == 0) { m = fmaxf(m, max16(s)); continue; }
#pragma unroll
          for (int j = 0; j < 16; ++j) EX(s, j);
#pragma unroll
          for (int s2 = 0; s2 < 2; ++s2) {
            const u32x4 vv = ld128(rs, r < 20 ? WS_VB + (h * 64 + kt) * 2048 + (2 * s2 + hh) * 512 + r * 16 : crow);
            o = smf(__builtin_bit_cast(bf16x8, vv), s, s2, o, idx);
          }
        }
        if (pass == 0) m = swap_max(m);
        else {
#pragma unroll
          for (int i = 0; i < 12; ++i) ofin[i] = o[i];
        }
      }
      auto r2 = __builtin_amdgcn_permlane32_swap(__float_as_uint(ofin[8]), __float_as_uint(ofin[8]), false, false);
      l = __uint_as_float(r2[1]);
    }
    const float rl = __builtin_amdgcn_rcpf(l);
    unsigned ow[6];
#pragma unroll
    for (int i = 0; i < 6; ++i) ow[i] = min(cvtpk(ofin[2 * i] * rl, ofin[2 * i + 1] * rl), 0xfffffffeu);
    st128(rs, obm, u32x4{ow[0], ow[1], ow[2], ow[3]});
    st64(rs, obt, u32x2{ow[4], ow[5]});
  }
  SBAR();
  STAMP(1, 12);
  wait_bar<63>();
  woquarter(IC<1>{}, IC<3>{}, IC<L_RING + AT_STAGE>{});
#pragma unroll
  for (int f = 0; f < 25; ++f) wf[f] = ldsr128(wl + WQ + f * 1024);
  SBAR();

  f32x4 accB[5];
  u32x4 og[8]; u32x2 ot[4];
#define OBLD(k, a, o) "s_bitcmp1_b32 %16, " #k "\n\ts_cbranch_scc0 1f\n\tglobal_load_dwordx4 %" #k ", %" #a ", %15 offset:" #o " sc1\n1:\n\t"
#define OBLT(k, r0, r1, o0, o1) "s_bitcmp1_b32 %16, " #k "\n\ts_cbranch_scc0 1f\n\tglobal_load_dwordx2 %" #r0 ", %14, %15 offset:" #o0 " sc1\n\tglobal_load_dwordx2 %" #r1 ", %14, %15 offset:" #o1 " sc1\n1:\n\t"
#define OB_ALL OBLD(0, 12, 0) OBLD(1, 12, 1024) OBLD(2, 12, 2048) OBLD(3, 12, 3072) OBLD(4, 13, 0) OBLD(5, 13, 1024) OBLD(6, 13, 2048) OBLD(7, 13, 3072) OBLT(8, 8, 9, 0, 512) OBLT(9, 10, 11, 1024, 1536)
#define OG(c) c(og[0]), c(og[1]), c(og[2]), c(og[3]), c(og[4]), c(og[5]), c(og[6]), c(og[7]), c(ot[0]), c(ot[1]), c(ot[2]), c(ot[3])
#define OBL1(k, a, o) asm volatile("global_load_dwordx4 %0, %1, %2 offset:" #o " sc1" : "=&v"(og[k]) : "v"(osrc + a), "s"(ws + WS_OB) : "memory")
#define OBL2(k, o) asm volatile("global_load_dwordx2 %0, %1, %2 offset:" #o " sc1" : "=&v"(ot[k]) : "v"(otsrc), "s"(ws + WS_OB) : "memory")
#define OFR(j) ((j) < 8 ? og[(j) & 7] : u32x4{ot[2 * ((j) & 1)][0], ot[2 * ((j) & 1)][1], ot[2 * ((j) & 1) + 1][0], ot[2 * ((j) & 1) + 1][1]})
  const unsigned otsrc = osrc - lane * 8 + 8192;
#pragma unroll
  for (int j = 0; j < 5; ++j) {
    if (j == 0) TPRIO(3); if (j == 2) TPRIO(2); if (j == 3) TPRIO(1); if (j == 4) TPRIO(0);
#pragma unroll
    for (int n = 0; n < 5; ++n) accA[n] = MFMA16(wf[j * 5 + n], of[5 + j], accA[n]);
    SBAR();
#pragma unroll
    for (int n = 0; n < 5; ++n) wf[j * 5 + n] = ldsr128(wl + (j * 5 + n) * 1024);
    if (j == OB0) { OBL1(0, 0, 0); OBL1(1, 0, 1024); OBL1(2, 0, 2048); }
    if (j == OB1) { OBL1(3, 0, 3072); OBL1(4, 4096, 0); OBL1(5, 4096, 1024); }
    if (j == OB2) { OBL1(6, 4096, 2048); OBL1(7, 4096, 3072); }
    if (j == OB3) { OBL2(0, 0); OBL2(1, 512); OBL2(2, 1024); OBL2(3, 1536); }
    SBAR();
  }
  STAMP(2, 0);
  waitv_bar<12>();
  asm volatile("s_waitcnt vmcnt(0)" : OG("+v") :: "memory");
  auto marked = [&]() AINL {
    unsigned pend = 0;
#pragma unroll
    for (int j = 0; j < 10; ++j) { const u32x4 v = OFR(j); pend |= __any(max(max(v[0], v[1]), max(v[2], v[3])) == ~0u) ? (1u << j) : 0u; }
    return pend;
  };
  const lds_cptr wh = (lds_cptr)lds + L_RING + kh * AT_STAGE + lane * 16;
#pragma unroll 1
  for (unsigned spins = 0; spins < (1u << 16); ++spins) {
    const unsigned pend = marked();
    if (!pend) break;
    asm volatile(OB_ALL "s_waitcnt vmcnt(0)" : OG("+v") : "v"(osrc), "v"(osrc + 4096), "v"(otsrc), "s"(ws + WS_OB), "s"(pend) : "memory", "scc");
  }
  STAMP(2, 1);
#pragma unroll
  for (int j = 0; j < 10; ++j) {
    if (j == 0) TPRIO(3); if (j == 3) TPRIO(2); if (j == 6) TPRIO(1); if (j == 8) TPRIO(0);
#pragma unroll
    for (int n = 0; n < 5; ++n) accB[n] = MFMA16(wf[(j % 5) * 5 + n], OFR(j), j == 0 ? f32x4{} : accB[n]);
    if (j < 5) {
#pragma unroll
      for (int n = 0; n < 5; ++n) wf[j * 5 + n] = ldsr128(wh + (j * 5 + n) * 1024);
    }
    SBAR();
  }
  STAMP(2, 2);
  f32x4* xch = (f32x4*)(lds + L_R + WQ) + tq * 5 * 64 + lane;
#pragma unroll
  for (int n = 0; n < 5; ++n) accB[n] = accA[n] * gate + accB[n] * (1.f - gate);
  if (kh) {
#pragma unroll
    for (int n = 0; n < 5; ++n) xch[n * 64] = accB[n];
  }
  wait_bar<63>();
  if (!kh) {
    char* op = out + (size_t)(rt * 64 + tq * 16 + (lane & 15)) * (D_ * 2) + ct * 160 + (lane >> 4) * 8;
#pragma unroll
    for (int n = 0; n < 5; ++n) {
      const f32x4 v = accB[n] + xch[n * 64];
      *(u32x2*)(op + n * 32) = u32x2{cvtpk(v[0], v[1]), cvtpk(v[2], v[3])};
    }
  }
  STAMP(2, 3);
  DBG_OUT();
  asm volatile("GM_CODE_END:");
  if (blockIdx.x == 0 && w == 7) {
#pragma unroll 1
    for (int i = 0; i < 4; ++i) wait_flags(flg, FOA + i * 64 + lane, tag);
    if (lane == 0) __hip_atomic_store(flg, tag, RLX_AGENT);
  }
}

extern "C" void kernel_launch(void* const* d_in, const int* in_sizes, int n_in, void* d_out, int out_size, void* d_ws,
                              size_t ws_size, hipStream_t stream) {
  (void)hipFuncSetAttribute((const void*)fused_kernel, hipFuncAttributeMaxDynamicSharedMemorySize, LDS_BYTES);
  fused_kernel<<<256, 512, LDS_BYTES, stream>>>((const char*)d_in[0], (const char*)d_in[1], (const char*)d_in[4], (const char*)d_in[2],
                                                (const char*)d_in[5], (const char*)d_in[6], (char*)d_ws, (char*)d_out);
}
```

```cpp
#include <hip/hip_runtime.h>
#include <hip/hip_bf16.h>
#include <stdint.h>

typedef short bf16x8 __attribute__((ext_vector_type(8)));
typedef short s16x4 __attribute__((ext_vector_type(4)));
typedef __bf16 bfv8 __attribute__((ext_vector_type(8)));
typedef __bf16 bfv2 __attribute__((ext_vector_type(2)));
typedef __bf16 bfv16 __attribute__((ext_vector_type(16)));
typedef float f32x16 __attribute__((ext_vector_type(16)));
typedef float f32x8 __attribute__((ext_vector_type(8)));
typedef float f32x4 __attribute__((ext_vector_type(4)));
typedef unsigned u32x4 __attribute__((ext_vector_type(4)));
typedef unsigned u32x2 __attribute__((ext_vector_type(2)));
typedef unsigned long long u64;
#define LAS __attribute__((address_space(3)))
#define GAS __attribute__((address_space(1)))
#define AINL __attribute__((always_inline))
typedef LAS const char* lds_cptr;
typedef LAS char* lds_ptr;
typedef GAS u64 gu64;

constexpr int D_ = 640, N3_ = 1920;
constexpr u64 TAG_STEP = 0x9E3779B97F4A7C15ull;
constexpr float QSCALE = 0.22360679774997896f * 1.4426950408889634f;
constexpr unsigned OOB = 0x70000000u;

constexpr unsigned UNIT = 2048 * 16;
constexpr unsigned F1A = 512, FG = 768, FOA = 1024, F1B = 2048, NREP = 16;
constexpr unsigned WS_QKB = (2048 + NREP * 256) * 128;
constexpr unsigned WS_VB = WS_QKB + 160 * UNIT;
constexpr unsigned BR_A = 160 * UNIT + 32 * 64 * 2048;
constexpr unsigned WS_OA = WS_QKB + 2 * BR_A;
constexpr unsigned WS_OB = WS_OA + 2048 * 640 * 2;
constexpr unsigned WS_WO = WS_OB + 2048 * 640 * 2;
constexpr unsigned WS_G = WS_WO + 2 * 640 * 640 * 2;
constexpr unsigned WS_C = WS_G + 2048 * 4;
constexpr unsigned WS_END = WS_C + 256 * 4096;

constexpr int L_R = 0, WQ = 25600;
constexpr int L_RING = 106496, AT_HALF = 14336, AT_STAGE = 2 * AT_HALF;
constexpr int L_FL = 104448, L_EP = 98304, L_PF = 81920, L_Q = 0;
constexpr int L_LK = 51200, LA_UNIT = 4608, L_LV = L_LK + 3 * LA_UNIT;
constexpr int LDS_BYTES = 163840;

constexpr int BST = 2;
#define SBAR() __builtin_amdgcn_sched_barrier(0)
#define PIN(x) asm volatile("" : "+v"(x))
#define MFMA32(a, b, c) __builtin_amdgcn_mfma_f32_32x32x16_bf16(__builtin_bit_cast(bfv8, a), __builtin_bit_cast(bfv8, b), c, 0, 0, 0)
#define MFMA8(a, b, c) __builtin_amdgcn_mfma_f32_32x32x8bf16_1k(a, b, c, 0, 0, 0)
#define MFMA16(a, b, c) __builtin_amdgcn_mfma_f32_16x16x32_bf16(__builtin_bit_cast(bfv8, a), __builtin_bit_cast(bfv8, b), c, 0, 0, 0)
#define RLX_AGENT __ATOMIC_RELAXED, __HIP_MEMORY_SCOPE_AGENT
#define SC1 16

template <int V> struct IC { static constexpr int value = V; };
template <int I, int N, class F> __device__ __forceinline__ void sfor(F&& f) { if constexpr (I < N) { f(IC<I>{}); sfor<I + 1, N>(f); } }
template <int N> __device__ __forceinline__ void wait_bar() { asm volatile("s_waitcnt vmcnt(%0) lgkmcnt(0)\n\ts_barrier" :: "i"(N) : "memory"); }
template <int N> __device__ __forceinline__ void waitv_bar() { asm volatile("s_waitcnt vmcnt(%0)\n\ts_barrier" :: "i"(N) : "memory"); }

__device__ __forceinline__ unsigned cvtpk(float lo, float hi) {
  bfv2 r; r[0] = (__bf16)lo; r[1] = (__bf16)hi;
  return __builtin_bit_cast(unsigned, r);
}
__device__ __forceinline__ float dot2(unsigned a, unsigned b, float c) {
  return __builtin_amdgcn_fdot2_f32_bf16(__builtin_bit_cast(bfv2, a), __builtin_bit_cast(bfv2, b), c, false);
}
template <int OFF> __device__ __forceinline__ void dma(const char* base, unsigned voff, unsigned lds_base) {
  asm volatile("s_add_u32 m0, %2, %3\n\ts_nop 0\n\tglobal_load_lds_dwordx4 %0, %1" :: "v"(voff), "s"(base), "s"(lds_base), "i"(OFF) : "memory", "scc");
}
template <int OFF> __device__ __forceinline__ void dma_sc1(const char* base, unsigned voff, unsigned lds_base) {
  asm volatile("s_add_u32 m0, %2, %3\n\ts_nop 0\n\tglobal_load_lds_dwordx4 %0, %1 sc1" :: "v"(voff), "s"(base), "s"(lds_base), "i"(OFF) : "memory", "scc");
}
template <int OFF> __device__ __forceinline__ void dma2_sc1(const char* base, unsigned voff, unsigned lds_base) {
  asm volatile("s_add_u32 m0, %2, %3\n\ts_nop 0\n\tglobal_load_lds_dwordx4 %0, %1 sc1\n\tglobal_load_lds_dwordx4 %0, %1 offset:1024 sc1"
               :: "v"(voff), "s"(base), "s"(lds_base), "i"(OFF) : "memory", "scc");
}
template <int OFF> __device__ __forceinline__ void dma3_sc1(const char* base, unsigned voff, unsigned lds_base) {
  asm volatile("s_add_u32 m0, %2, %3\n\ts_nop 0\n\tglobal_load_lds_dwordx4 %0, %1 sc1\n\tglobal_load_lds_dwordx4 %0, %1 offset:1024 sc1\n\t"
               "global_load_lds_dwordx4 %0, %1 offset:2048 sc1" :: "v"(voff), "s"(base), "s"(lds_base), "i"(OFF) : "memory", "scc");
}
typedef short v4i16_t __attribute__((ext_vector_type(4)));
__device__ __forceinline__ s16x4 trd(lds_cptr p) {
  return __builtin_bit_cast(s16x4, __builtin_amdgcn_ds_read_tr16_b64_v4i16((LAS v4i16_t*)p));
}
__device__ __forceinline__ bf16x8 cat8(s16x4 a, s16x4 b) { return __builtin_shufflevector(a, b, 0, 1, 2, 3, 4, 5, 6, 7); }
__device__ __forceinline__ bf16x8 ldsr128(lds_cptr p) { return *(const LAS bf16x8*)p; }
__device__ __forceinline__ s16x4 ldsr64(lds_cptr p) { return *(const LAS s16x4*)p; }
__device__ __forceinline__ u32x4 pack8(const f32x16& a, int s, float sc) {
  return u32x4{cvtpk(a[8 * s] * sc, a[8 * s + 1] * sc), cvtpk(a[8 * s + 2] * sc, a[8 * s + 3] * sc),
               cvtpk(a[8 * s + 4] * sc, a[8 * s + 5] * sc), cvtpk(a[8 * s + 6] * sc, a[8 * s + 7] * sc)};
}
__device__ __forceinline__ int swap23(int r) { return (r & 0x13) | ((r & 4) << 1) | ((r & 8) >> 1); }
__device__ __forceinline__ u64 uni64(unsigned lo, unsigned hi) {
  return ((u64)(unsigned)__builtin_amdgcn_readfirstlane(hi) << 32) | (unsigned)__builtin_amdgcn_readfirstlane(lo);
}

typedef __amdgpu_buffer_rsrc_t rsrc_t;
__device__ __forceinline__ void st128(rsrc_t rs, unsigned off, u32x4 v) { __builtin_amdgcn_raw_buffer_store_b128(v, rs, off, 0, SC1); }
__device__ __forceinline__ void st64(rsrc_t rs, unsigned off, u32x2 v) { __builtin_amdgcn_raw_buffer_store_b64(v, rs, off, 0, SC1); }
__device__ __forceinline__ u32x4 ld128(rsrc_t rs, unsigned off) { return __builtin_amdgcn_raw_buffer_load_b128(rs, off, 0, SC1); }
__device__ __forceinline__ u32x2 ld64(rsrc_t rs, unsigned off) { return __builtin_amdgcn_raw_buffer_load_b64(rs, off, 0, SC1); }
__device__ __forceinline__ unsigned ld32(rsrc_t rs, unsigned off) { return __builtin_amdgcn_raw_buffer_load_b32(rs, off, 0, SC1); }

template <int SLEEP = 6>
__device__ __forceinline__ void wait_flags(gu64* flg, unsigned idx, u64 tag) {
  bool ok = false;
#pragma unroll 1
  for (unsigned spins = 0; spins < (1u << 18); ++spins) {
    if (!ok) ok = __hip_atomic_load(flg + idx * 16, RLX_AGENT) == tag;
    if (__all(ok)) break;
    __builtin_amdgcn_s_sleep(SLEEP);
  }
  asm volatile("" ::: "memory");
}
__device__ __forceinline__ void sample_flags(u64& fa, u64& fb, unsigned ia, unsigned ib, const char* base, u64 miss) {
  asm volatile("s_cmp_eq_u64 %5, 0\n\ts_cbranch_scc1 1f\n\tglobal_load_dwordx2 %0, %2, %4 sc1\n\tglobal_load_dwordx2 %1, %3, %4 sc1\n1:"
               : "+v"(fa), "+v"(fb) : "v"(ia * 128), "v"(ib * 128), "s"(base), "s"(miss) : "memory", "scc");
}

__device__ __forceinline__ unsigned o_main(int tok, int h, int hh) {
  const int m = h >> 1;
  return (unsigned)((((tok >> 4) * 20 + 5 * (m >> 2) + (m & 3)) * 64 + (2 * (h & 1) + hh) * 16 + (tok & 15)) * 16);
}
__device__ __forceinline__ unsigned o_tail(int tok, int h) {
  const int m = h >> 1;
  return (unsigned)(((tok >> 4) * 20 + 5 * (m >> 2) + 4) * 1024 + 512 * (m & 1) + ((2 * ((m >> 1) & 1) + (h & 1)) * 16 + (tok & 15)) * 8);
}

#define G1_ORDER "BBBBBBBBBBAAAAAAAAAA"
constexpr int G1_PUB = 14;
constexpr int G1_EP = 12;
constexpr int G1_SAMP = 72, G1_LOOK = 76;
struct G1 {
  int isB[24] = {}, kb[24] = {}, uf[25] = {};
  int lastB = 0;
  int iss_lo[20] = {}, iss_hi[20] = {};
  int wait[20] = {};
  constexpr G1() {
    const char* ord = G1_ORDER;
    int nb = 0, na = 0;
    for (int o = 0; o < 24; ++o) {
      isB[o] = o < 20 && ord[o] == 'B'; kb[o] = o < 20 ? (isB[o] ? nb++ : na++) : 0;
      uf[o + 1] = uf[o] + (o < 20 ? 1 + isB[o] : 0);
      if (isB[o]) lastB = o;
    }
    int pos[60] = {}, cnt = 0, lastst = 0, done = 2 * uf[2];
    for (int d = 0; d < done; ++d) pos[d] = cnt++;
    for (int o = 0; o < 20; ++o) {
      int need = pos[2 * uf[o + 1] - 1];
      if (o == G1_PUB && lastst > need) need = lastst;
      wait[o] = o >= 18 ? 0 : cnt - 1 - need;
      if (o == G1_EP) lastst = cnt++;
      iss_lo[o] = done; iss_hi[o] = 2 * uf[o + 4];
      for (int ks = 0; ks < 4; ++ks) {
        const int per = (iss_hi[o] - iss_lo[o] + 3) / 4;
        for (int z = 0; z < per && done < iss_hi[o]; ++z) pos[done++] = cnt++;
        const int n = 4 * o + ks - 2 - (4 * lastB + 3);
        if (n >= 0 && n < 4 * BST && n % BST == 0) lastst = cnt++;
      }
    }
  }
};
constexpr int g1_slot(int u) { return ((u + 6) % 10) * 16384; }
template <bool VT, class FPiece, class FStart, class FSetup>
__device__ __forceinline__ void g1_body(char* ws, rsrc_t rs, char* lds, int ti, int cj, int w, int lane, u64& tag, bool& early, unsigned& fiKV, unsigned& fiQ,
                                        u32x4 (&pk)[4], unsigned (&dstB)[2], FPiece&& piece, FStart&& att_start, FSetup&& setup) {
  constexpr G1 P;
  static_assert(P.uf[G1_EP - 1] > 20 && G1_EP < G1_PUB && P.uf[4] <= 9, "epoch / prefetch scratch slots");
  const int tg = w & 3, cg = w >> 2;
  const int r = lane & 31, hh = lane >> 5, g16 = (lane >> 4) & 1, q4 = (lane & 15) >> 2, p4 = lane & 3;
  const int tr = VT ? swap23(r) : r;
#pragma unroll
  for (int t2 = 0; t2 < 2; ++t2) {
    if (VT) {
      const int F = (cj - 10) * 128 + 64 * cg + 32 * t2 + r, hd = F / 20;
      int d = F - 20 * hd - 4 * (hd & 1); if (d < 0) d += 20;
      dstB[t2] = WS_VB + (unsigned)((hd * 64 + 4 * ti + tg) * 2048 + hh * 512 + d * 16);
    } else {
      dstB[t2] = WS_QKB + (unsigned)(cj * 16 + 8 * cg + 4 * t2 + hh) * UNIT + (unsigned)(128 * ti + 32 * tg + r) * 16;
    }
  }
  unsigned xoff[4];
#pragma unroll
  for (int ks = 0; ks < 4; ++ks) xoff[ks] = (unsigned)((32 * tg + tr) * 128 + (((2 * ks + hh) ^ ((tr >> 1) & 7)) * 16));
  const int psw = ((p4 & 1) << 1) | (p4 >> 1);
  unsigned woff[2];
#pragma unroll
  for (int t2 = 0; t2 < 2; ++t2) woff[t2] = (unsigned)((8 * hh + q4) * 256 + (((2 * cg + t2) ^ q4) * 64) + 32 * g16 + 8 * (VT ? p4 : psw));
  const float sc = (!VT && cj < 5) ? QSCALE : 1.f;
  bf16x8 xf[40], wf[3][2];
  u64 fa, fb;
  f32x16 accB[2], accA[2];
  u64 miss = 1;
  auto look = [&](bool again) AINL {
    asm volatile("s_cmp_eq_u64 %2, 0\n\ts_cbranch_scc1 1f\n\ts_waitcnt vmcnt(0)\n1:" : "+v"(fa), "+v"(fb) : "s"(miss) : "memory", "scc");
    const u64 m = __ballot(fa != tag || fb != tag);
    if (m == 0 && miss != 0) att_start();
    miss = m;
    if (again) sample_flags(fa, fb, fiKV, fiQ, ws + (F1B + 256) * 128, miss);
  };
  sfor<0, 82>([&](auto ic) AINL {
    constexpr int nn = decltype(ic)::value, n = nn - 2;
    constexpr int o = nn >> 2, ks = nn & 3;
    if constexpr (nn < 80) {
      if constexpr (ks == 0) {
        if constexpr (o < 19) waitv_bar<P.wait[o]>();
        if constexpr (o == G1_EP) dma_sc1<L_EP>(ws, 0, (unsigned)(uintptr_t)lds + w * 1024);
        if constexpr (o == G1_PUB) { const u32x2 ep = *(const LAS u32x2*)((lds_cptr)lds + L_EP + w * 1024); tag = uni64(ep[0], ep[1]) + TAG_STEP; }
        if constexpr (o == G1_PUB) { if (w == 0 && lane < NREP) __hip_atomic_store((gu64*)ws + (F1B + lane * 256 + cj * 16 + ti) * 16, tag, RLX_AGENT); }
      }
      if constexpr (nn >= 62 && nn < 70 && !(nn & 1)) setup(IC<(nn - 62) / 2>{});
      if constexpr (nn == G1_SAMP) { fa = 0; fb = 0; sample_flags(fa, fb, fiKV, fiQ, ws + F1B * 128, miss); }
      if constexpr (nn == G1_LOOK) look(true);
      if constexpr (P.isB[o]) xf[4 * P.kb[o] + ks] = ldsr128((lds_cptr)lds + g1_slot(P.uf[o] + 1) + xoff[ks]);
#pragma unroll
      for (int t2 = 0; t2 < 2; ++t2) {
        const lds_cptr wp = (lds_cptr)lds + g1_slot(P.uf[o]) + woff[t2] + ks * 4096;
        wf[nn % 3][t2] = cat8(trd(wp), trd(wp + 1024));
      }
      constexpr int per = (P.iss_hi[o] - P.iss_lo[o] + 3) / 4, d0 = P.iss_lo[o] + per * ks;
      sfor<d0, (d0 + per < P.iss_hi[o] ? d0 + per : P.iss_hi[o])>(piece);
    }
    if constexpr (n >= 0) {
      constexpr int om = n >> 2, xi = 4 * P.kb[om] + (n & 3);
#pragma unroll
      for (int t2 = 0; t2 < 2; ++t2) {
        f32x16& ac = P.isB[om] ? accB[t2] : accA[t2];
        const f32x16 cin = xi == 0 ? f32x16{} : ac;
        ac = VT ? MFMA32(xf[xi], wf[n % 3][t2], cin) : MFMA32(wf[n % 3][t2], xf[xi], cin);
      }
    }
    SBAR();
    constexpr int nb = n - (4 * P.lastB + 3);
    if constexpr (nb == 0) {
#pragma unroll
      for (int i = 0; i < 4; ++i) { pk[i] = VT ? pack8(accB[i >> 1], i & 1, 1.f) : pack8(accB[i >> 1], i & 1, sc); PIN(pk[i]); }
    }
    if constexpr (nb >= 0 && nb < 4 * BST && nb % BST == 0) {
      constexpr int i = nb / BST;
      st128(rs, dstB[i >> 1] + (i & 1) * (VT ? 1024 : 2 * UNIT), pk[i]);
      SBAR();
    }
  });
  look(false);
  early = miss == 0;
#pragma unroll
  for (int i = 0; i < 4; ++i) pk[i] = pack8(accA[i >> 1], i & 1, 1.f);
}

__device__ __forceinline__ f32x16 smf(bf16x8 a, const f32x16& p, int s, f32x16 o, int idx) {
  f32x8 b = s ? __builtin_shufflevector(p, p, 8, 9, 10, 11, 12, 13, 14, 15) : __builtin_shufflevector(p, p, 0, 1, 2, 3, 4, 5, 6, 7);
  return __builtin_amdgcn_smfmac_f32_32x32x32_bf16(__builtin_bit_cast(bfv8, a), __builtin_bit_cast(bfv16, b), o, idx, 0, 0);
}
__device__ __forceinline__ float max16(const f32x16& s) {
  float a = fmaxf(fmaxf(s[0], s[1]), s[2]), b = fmaxf(fmaxf(s[3], s[4]), s[5]);
  a = fmaxf(fmaxf(a, s[6]), s[7]); b = fmaxf(fmaxf(b, s[8]), s[9]);
  a = fmaxf(fmaxf(a, s[10]), s[11]); b = fmaxf(fmaxf(b, s[12]), s[13]);
  a = fmaxf(fmaxf(a, s[14]), s[15]);
  return fmaxf(a, b);
}
__device__ __forceinline__ float swap_max(float m) {
  auto rr = __builtin_amdgcn_permlane32_swap(__float_as_uint(m), __float_as_uint(m), false, false);
  return fmaxf(__uint_as_float(rr[0]), __uint_as_float(rr[1]));
}
__device__ __forceinline__ float swap_add(float m) {
  auto rr = __builtin_amdgcn_permlane32_swap(__float_as_uint(m), __float_as_uint(m), false, false);
  return __uint_as_float(rr[0]) + __uint_as_float(rr[1]);
}
__device__ __forceinline__ void set_ref(s16x4& q1, float m, bool spare) {
  const unsigned a = __float_as_uint(-m) & 0xffff0000u;
  const float r1 = -m - __uint_as_float(a);
  const unsigned b = __float_as_uint(r1) & 0xffff0000u;
  const unsigned c = __float_as_uint(r1 - __uint_as_float(b));
  if (spare) q1 = __builtin_bit_cast(s16x4, u32x2{(a >> 16) | b, c >> 16});
}

__global__ __launch_bounds__(512, 2) void fused_kernel(const char* __restrict__ x, const char* __restrict__ wA, const char* __restrict__ wB,
                                                       const char* __restrict__ woA, const char* __restrict__ woB,
                                                       const char* __restrict__ gw, char* ws, char* __restrict__ out) {
  extern __shared__ __attribute__((aligned(16))) char lds[];
  const int tid = threadIdx.x, lane = tid & 63;
  const int w = __builtin_amdgcn_readfirstlane(tid >> 6);
  const int xcd = blockIdx.x & 7, slot = blockIdx.x >> 3;
  const unsigned lds0 = (unsigned)(uintptr_t)lds;
  gu64* flg = (gu64*)ws;
  const rsrc_t rs = __builtin_amdgcn_make_buffer_rsrc(ws, 0, WS_END, 0x00020000);
  const int ti = 8 * (xcd & 1) + (slot & 7);
  const int cj = 4 * (xcd >> 1) + (slot >> 3);
  const unsigned ldpf = lds0 + (cj < 15 ? L_PF : L_RING);
  if (w == 6) {
    u64 pc; unsigned len;
    asm volatile("s_getpc_b64 %0\nGM_CODE_BEG:\n\ts_mov_b32 %1, GM_CODE_END-GM_CODE_BEG" : "=s"(pc), "=s"(len));
    asm volatile("s_add_u32 m0, %2, %3\n\ts_nop 0\n\tglobal_load_lds_dword %0, %1"
                 :: "v"(min((unsigned)((slot & 7) * 64 + lane) * 128, len)), "s"(pc), "s"(ldpf), "i"(0) : "memory", "scc");
  }
  const int rot = ((slot & 7) * 5 / 4 + (slot >> 3) * 5 / 2 + xcd) % 10;
  unsigned xlo[2], wlo[2];
#pragma unroll
  for (int i = 0; i < 2; ++i) {
    const int row = 16 * w + 8 * i + (lane >> 3);
    xlo[i] = (unsigned)(row * (D_ * 2) + (((lane & 7) ^ ((row >> 1) & 7)) * 16));
    wlo[i] = (unsigned)((8 * w + 4 * i + (lane >> 4)) * (N3_ * 2) + (((lane & 15) ^ (4 * (lane >> 4))) * 16));
  }
  const char* xb = x + (size_t)(128 * ti) * (D_ * 2);
  const char* wBb = wB + cj * 256;
  const char* wAb = wA + cj * 256;
  const unsigned ldw = lds0 + w * 2048;
  auto piece = [&](auto dc) AINL {
    constexpr G1 P;
    constexpr int d = decltype(dc)::value, u = d >> 1, base = g1_slot(u) + (d & 1) * 1024;
    constexpr int o = [&]() { int q = 0; while (P.uf[q + 1] <= u) ++q; return q; }();
    int kb = P.kb[o] + rot; if (kb >= 10) kb -= 10;
    if constexpr (u != P.uf[o]) dma<base>(xb + kb * 128, xlo[d & 1], ldw);
    else dma<base>((P.isB[o] ? wBb : wAb) + (size_t)kb * (64 * N3_ * 2), wlo[d & 1], ldw);
  };
  if (cj < 15) {
    sfor<0, 8>(piece);
  }
  SBAR();
  u64 tag;
  const int h = 4 * xcd + (slot & 3), c = slot >> 2;
  const int e = h & 1, ua = 5 * (h >> 1) + 3 * e, us = 5 * (h >> 1) + 2;
  const int jlo = (5 * (h >> 1) + 2 * e) >> 4, jhi = (5 * (h >> 1) + 2 * e + 2) >> 4;
  const int kh = w >> 2, tq = w & 3;
  const char* kbase = ws + WS_QKB + (size_t)(80 + (tq >= 2 ? us : ua + tq)) * UNIT + 1024 * kh * 16;
  if (w == 5) { const unsigned cv = (lane & 1) ? 0u : 0x3f803f80u; st128(rs, lane < 8 ? WS_C + blockIdx.x * 4096 + (lane >> 1) * 1024 + (lane & 1) * 16 : OOB, u32x4{cv, cv, cv, cv}); }
  const unsigned ldr = lds0 + kh * AT_HALF + tq * 2048;
  unsigned vlo, vstr;
  auto kpiece = [&](auto stc) AINL {
    constexpr int st = decltype(stc)::value;
    if (tq < 3) dma2_sc1<L_RING + (st & 1) * AT_STAGE>(kbase + st * 2048, lane * 16, ldr);
  };
  auto vpiece = [&](auto stc) AINL {
    constexpr int st = decltype(stc)::value;
    dma2_sc1<L_RING + (st & 1) * AT_STAGE + 6144>(ws, vlo + st * vstr, ldr);
  };
  unsigned fiKV, fiQ;
  auto att_start = [&]() AINL {
    kpiece(IC<0>{});
    if (tq == 3) {
      const char* qb = ws + WS_QKB + (256 * c + 128 * kh) * 16;
      dma2_sc1<L_Q>(qb + (size_t)ua * UNIT, lane * 16, lds0 + kh * 2048);
      dma2_sc1<L_Q + 4096>(qb + (size_t)(ua + 1) * UNIT, lane * 16, lds0 + kh * 2048);
      dma2_sc1<L_Q + 8192>(qb + (size_t)us * UNIT, lane * 16, lds0 + kh * 2048);
    }
    vpiece(IC<0>{});
  };

  const int hh = lane >> 5, r = lane & 31;
  const int rt = 4 * xcd + (slot & 3), ct = slot >> 2;
  const int SWP = ct & 1;
  const char* wob = ws + WS_WO + (size_t)ct * 102400 + w * 3072;
  auto woquarter = [&](auto brc, auto qc, auto dc) AINL {
    constexpr int br = decltype(brc)::value, q = decltype(qc)::value, d = decltype(dc)::value;
    const int off = br * 819200 + (q ^ (br & SWP)) * WQ;
    dma3_sc1<d>(wob + off, lane * 16, lds0 + w * 3072);
    if (w == 0) dma_sc1<d + 24576>(wob + off + 24576, lane * 16, lds0);
  };
  const unsigned ladst = lds0 + (w < 3 ? L_LK + w * LA_UNIT : L_LV + min(4 * (w - 3), 13) * 1024);
  unsigned osrc, fiA, obm, obt, lalo; int idx;
  lds_cptr kp0, kp1, vp, qp;
  f32x16 oa, ob;
  auto setup = [&](auto pc) AINL {
    constexpr int p = decltype(pc)::value;
    int ln = lane; PIN(ln);
    const int hh = ln >> 5, r = ln & 31;
    if constexpr (p == 0) {
      const unsigned crow = WS_C + blockIdx.x * 4096 + (r == 20 ? 0 : 16);
      vlo = r < 20 ? WS_VB + (h * 64 + 32 * kh + tq) * 2048 + ln * 16 : crow; vstr = r < 20 ? 8192u : 0u;
      const unsigned lab = w < 3 ? WS_QKB + BR_A + (80 + (w == 2 ? us : ua + w)) * UNIT + (256 * c - 16) * 16
                                 : WS_VB + BR_A + (h * 64 + 8 * c) * 2048 - 1024 + min(4 * (w - 3), 13) * 1024;
      lalo = (w < 3 || r < 20) ? lab + ln * 16 : crow;
      osrc = (unsigned)((rt * 4 + tq) * 20 + 10 * kh) * 1024 + ln * 16;
      PIN(vlo); PIN(vstr); PIN(lalo); PIN(osrc);
    }
    if constexpr (p == 1) {
      fiA = ln < 18 ? F1A + (5 * (ln / 6) + (((ln / 3) & 1) ? jhi : jlo)) * 16 + max(2 * c - 1 + ln % 3, 0) : FG + (ln == 18 ? ct : 8 + ct);
      obm = WS_OB + o_main(256 * c + 64 * tq + 32 * kh + r, h, hh); obt = (hh ? OOB : 0u) + WS_OB + o_tail(256 * c + 64 * tq + 32 * kh + r, h);
      PIN(fiA); PIN(obm); PIN(obt);
    }
    if constexpr (p == 2) {
      const lds_cptr ring = (lds_cptr)lds + L_RING + kh * AT_HALF;
      kp0 = ring + hh * 2048 + r * 16;
      kp1 = (hh == e) ? ring + 2 * 2048 + r * 16 + 8 * hh : ring + 6144 + 20 * 16;
      vp = ring + 6144 + hh * 512 + r * 16;
      qp = (lds_cptr)lds + L_Q + (64 * tq + r) * 16;
      idx = 0xDDDD;
      PIN(kp0); PIN(kp1); PIN(vp); PIN(qp); PIN(idx);
    }
    if constexpr (p == 3) {
      oa = f32x16{}; ob = f32x16{}; PIN(oa); PIN(ob);
      fiKV = (5 + 5 * hh + ((ln & 16) ? jhi : jlo)) * 16 + (ln & 15); fiQ = ((ln & 1) ? jhi : jlo) * 16 + 2 * c + ((ln >> 1) & 1);
      PIN(fiKV); PIN(fiQ);
    }
  };

  bool early = false;
  u32x4 pk[4]; unsigned dstA[2];
  if (cj < 10) g1_body<false>(ws, rs, lds, ti, cj, w, lane, tag, early, fiKV, fiQ, pk, dstA, piece, att_start, setup);
  else if (cj < 15) g1_body<true>(ws, rs, lds, ti, cj, w, lane, tag, early, fiKV, fiQ, pk, dstA, piece, att_start, setup);
  else {
    const char* wo = ((ti & 8) ? woB : woA) + (size_t)(80 * w) * (D_ * 2) + (ti & 7) * 160;
#pragma unroll 1
    for (int p = 0; p < 13; ++p) {
      const int s0 = min(64 * p, 736), sl = s0 + lane;
      dma<0>(wo, (unsigned)((sl / 10) * (D_ * 2) + (sl % 10) * 16), lds0 + w * 12800 + s0 * 16);
    }
    const u64 ep = __hip_atomic_load(flg, RLX_AGENT);
    tag = uni64((unsigned)ep, (unsigned)(ep >> 32)) + TAG_STEP;
    wait_bar<0>();
    const int g4 = lane >> 4, rl = 320 * kh + 4 * (g4 & 1) + ((lane & 15) >> 2);
#pragma unroll
    for (int ff = 0; ff < 13; ++ff) {
      const int f = min(tq + 4 * ff, 49), j = f / 5, n = f - 5 * j;
      const bool mn = j % 5 < 4;
      const int row = rl + 160 * (j / 5) + (mn ? 40 * (j % 5) + 24 * (g4 >> 1) : 80 * (g4 >> 1) + 16);
      const lds_cptr p = (lds_cptr)lds + row * 160 + n * 32 + 8 * (lane & 3);
      st128(rs, WS_WO + (unsigned)(ti * 100 + kh * 50 + f) * 1024 + lane * 16, __builtin_bit_cast(u32x4, cat8(trd(p), trd(p + (mn ? 8 : 40) * 160))));
    }
    wait_bar<0>();
    if (tid == 0) __hip_atomic_store(flg + (FG + ti) * 16, tag, RLX_AGENT);
    if (w == 0) { wait_flags<16>(flg, F1B + (NREP - 1) * 256 + jlo * 16 + 2 * c, tag); }
    wait_bar<63>();
    sfor<0, 4>(setup);
    dstA[0] = OOB; dstA[1] = OOB;
#pragma unroll
    for (int i = 0; i < 4; ++i) pk[i] = u32x4{0u, 0u, 0u, 0u};
  }
  const unsigned flag1 = F1A + cj * 16 + ti;

  if (!early) {
    bool oka = false, okb = false;
#pragma unroll 1
    for (unsigned spins = 0; spins < (1u << 18); ++spins) {
      const unsigned rp = F1B + (2 + min(spins, NREP - 4)) * 256;
      const u64 a = __hip_atomic_load(flg + (rp + (oka ? fiQ : fiKV)) * 16, RLX_AGENT), b = __hip_atomic_load(flg + (rp + fiQ) * 16, RLX_AGENT);
      oka = oka || a == tag; okb = okb || b == tag;
      if (__all(oka && okb)) break;
      __builtin_amdgcn_s_sleep(2);
    }
    asm volatile("" ::: "memory");
    att_start();
  }
  wait_bar<0>();
  float gate;
  const unsigned fiOA = FOA + (rt >> 2) * 32 + r;
  u32x4 lq; u32x2 lqt;
  f32x16 sa, sb;
  bf16x8 q0[2]; s16x4 q1[2];
#pragma unroll
  for (int t = 0; t < 2; ++t) {
    q0[t] = ldsr128(qp + hh * 4096 + ((t ^ kh ^ 1) * 512));
    q1[t] = ldsr64(qp + 8192 + 8 * e + ((t ^ kh ^ 1) * 512));
  }
  const bf16x8 kr0 = ldsr128(kp0 - kh * AT_HALF); const s16x4 kr1 = ldsr64(kp1 - kh * AT_HALF);
  bf16x8 k0 = ldsr128(kp0); s16x4 k1 = ldsr64(kp1);
  bf16x8 v0 = ldsr128(vp), v1 = ldsr128(vp + 1024);
  bf16x8 v1p = v1;
  SBAR();
  {
    set_ref(q1[0], 0.f, hh != e); set_ref(q1[1], 0.f, hh != e);
    f32x16 s = MFMA32(kr0, q0[0], f32x16{}), t = MFMA32(kr0, q0[1], f32x16{});
    s = MFMA8(kr1, q1[0], s); t = MFMA8(kr1, q1[1], t);
    sa = MFMA32(k0, q0[0], f32x16{});
    SBAR();
    kpiece(IC<1>{}); vpiece(IC<1>{});
    SBAR();
    set_ref(q1[0], swap_max(max16(s)), hh != e);
    sa = MFMA8(k1, q1[0], sa);
    SBAR();
    set_ref(q1[1], swap_max(max16(t)), hh != e);
  }
  u32x4 of[10];
  unsigned gx[5], gg[5]; float gs;

  auto local_attention = [&]() AINL {
    const int tok = 256 * c + 32 * w + r;
    const lds_cptr lk = (lds_cptr)lds + L_LK + (32 * w + r) * 16, lv = (lds_cptr)lds + L_LV + (4 * w + hh) * 512 + r * 16;
    const bf16x8 qa = __builtin_bit_cast(bf16x8, lq);
    const s16x4 qb = __builtin_bit_cast(s16x4, (hh == e) ? lqt : u32x2{0u, 0u});
    f32x16 sx = MFMA32(ldsr128(lk + hh * LA_UNIT), qa, f32x16{}), sy = MFMA32(ldsr128(lk + hh * LA_UNIT + 512), qa, f32x16{});
    sx = MFMA8(ldsr64(lk + 2 * LA_UNIT + 8 * e), qb, sx); sy = MFMA8(ldsr64(lk + 2 * LA_UNIT + 8 * e + 512), qb, sy);
    const bf16x8 va = ldsr128(lv), vb = ldsr128(lv + 1024), vc = ldsr128(lv + 2048);
    const u64 edge = (c == 0 && w == 0) ? 0ull : ~0ull;
    const float ninf = -INFINITY;
    sfor<0, 24>([&](auto jc) AINL {
      constexpr int j = decltype(jc)::value;
      constexpr u64 mk = []() { u64 m = 0; for (int l = 0; l < 64; ++l) { const int d = (l & 31) + 16 - ((j & 3) + 8 * (j >> 2) + 4 * (l >> 5)); if (d >= 0 && d <= 14 && !(d & 1)) m |= 1ull << l; } return m; }();
      const bool ok = __builtin_amdgcn_inverse_ballot_w64(j < 8 ? mk & edge : mk);
      if constexpr (j < 16) sx[j] = ok ? sx[j] : ninf; else sy[j - 16] = ok ? sy[j - 16] : ninf;
    });
    float m = fmaxf(fmaxf(max16(sx), sy[0]), sy[1]);
    m = fmaxf(fmaxf(m, sy[2]), sy[3]); m = fmaxf(fmaxf(m, sy[4]), sy[5]); m = -QSCALE * swap_max(fmaxf(fmaxf(m, sy[6]), sy[7]));
#pragma unroll
    for (int j = 0; j < 16; ++j) sx[j] = __builtin_amdgcn_exp2f(__builtin_fmaf(sx[j], QSCALE, m));
#pragma unroll
    for (int j = 0; j < 8; ++j) sy[j] = __builtin_amdgcn_exp2f(__builtin_fmaf(sy[j], QSCALE, m));
    f32x16 o = smf(va, sx, 0, f32x16{}, idx); o = smf(vb, sx, 1, o, idx); o = smf(vc, sy, 0, o, idx);
    auto lr = __builtin_amdgcn_permlane32_swap(__float_as_uint(o[8]), __float_as_uint(o[8]), false, false);
    const float rl = __builtin_amdgcn_rcpf(__uint_as_float(lr[1]));
    st128(rs, WS_OA + o_main(tok, h, hh), pack8(o, 0, rl));
    st64(rs, hh ? OOB : WS_OA + o_tail(tok, h), u32x2{cvtpk(o[8] * rl, o[9] * rl), cvtpk(o[10] * rl, o[11] * rl)});
    gs += __builtin_bit_cast(float, __builtin_amdgcn_update_dpp(0, __builtin_bit_cast(int, gs), 0xB1, 0xf, 0xf, true));
    gs += __builtin_bit_cast(float, __builtin_amdgcn_update_dpp(0, __builtin_bit_cast(int, gs), 0x4E, 0xf, 0xf, true));
    gs += __builtin_bit_cast(float, __builtin_amdgcn_update_dpp(0, __builtin_bit_cast(int, gs), 0x141, 0xf, 0xf, true));
    gs += __builtin_bit_cast(float, __builtin_amdgcn_update_dpp(0, __builtin_bit_cast(int, gs), 0x140, 0xf, 0xf, true));
    { auto g2 = __builtin_amdgcn_permlane16_swap(__float_as_uint(gs), __float_as_uint(gs), false, false); gs = __uint_as_float(g2[0]) + __uint_as_float(g2[1]); }
    gs = swap_add(gs);
    const float gv = __builtin_amdgcn_rcpf(1.f + __builtin_amdgcn_exp2f(-1.4426950408889634f * gs));
    __builtin_amdgcn_raw_buffer_store_b32(__float_as_uint(gv), rs, lane == 0 ? WS_G + (256 * c + 8 * h + w) * 4 : OOB, 0, SC1);
    SBAR();
  };

  const lds_cptr wl = (lds_cptr)lds + L_R + kh * (2 * WQ) + lane * 16;
  f32x4 accA[5];
  bf16x8 wq[2][5], wf[25];
  auto opa = [&](auto hc, auto gc) AINL {
    constexpr int hs = decltype(hc)::value, g = decltype(gc)::value;
    if constexpr (hs >= 0 && hs <= 5) SBAR();
    if constexpr (hs >= 1 && hs <= 5) accA[g] = MFMA16(wq[(hs - 1) & 1][g], of[hs - 1], hs == 1 ? f32x4{} : accA[g]);
    if constexpr (hs >= 0 && hs < 5) wq[hs & 1][g] = ldsr128(wl + (hs * 5 + g) * 1024);
    if constexpr (hs >= 0 && hs <= 5) SBAR();
  };
#define EX(S, j) S[j] = __builtin_amdgcn_exp2f(S[j])
#define EX2(S, j) do { EX(S, j); EX(S, j + 1); } while (0)
  sfor<0, 8>([&](auto stc) AINL {
    constexpr int st = decltype(stc)::value;
    constexpr int cur = (st & 1) * AT_STAGE, nxt = ((st + 1) & 1) * AT_STAGE;
    sfor<0, 4>([&](auto ic) AINL {
      constexpr int i = decltype(ic)::value;
      constexpr int nk = (i < 3) ? cur + (i + 1) * 512 : nxt, nv = (i < 3) ? cur + (i + 1) * 2048 : nxt;
      constexpr int sl = (i + 1) & 3, sst = (i == 3) ? st + 1 : st;
      constexpr int ha = st == 6 ? 2 * i : -9, hb = st == 6 ? 2 * i + 1 : -9;
      SBAR();
      EX2(sa, 0); PIN(sa); SBAR();
      if constexpr (st + i > 0) ob = smf(v1p, sb, 1, ob, idx);
      SBAR(); EX2(sa, 2); opa(IC<ha>{}, IC<0>{}); EX2(sa, 4); PIN(sa); SBAR();
      sb = MFMA32(k0, q0[1], f32x16{}); SBAR(); EX2(sa, 6); opa(IC<ha>{}, IC<1>{}); EX2(sa, 8); PIN(sa); SBAR();
      sb = MFMA8(k1, q1[1], sb); SBAR();
      if constexpr (i == 3) {
        wait_bar<0>();
        if constexpr (st == 0) { if (tid == 0) __hip_atomic_store(flg + flag1 * 16, tag, RLX_AGENT); }
        if constexpr (st == 2) {
          PIN(lq); PIN(lqt);
        }
        if constexpr (st == 1) {
          gs = 0.f;
#pragma unroll
          for (int i = 0; i < 5; ++i) gs = dot2(gx[i], gg[i], gs);
          PIN(gs);
        }
        if constexpr (st == 3) { if (tid == 0) __hip_atomic_store(flg + (FOA + c * 32 + h) * 16, tag, RLX_AGENT); }
        if constexpr (st == 5 || st == 7) {
#pragma unroll
          for (int j = 0; j < 5; ++j) PIN(of[(st == 7 ? 5 : 0) + j]);
          if constexpr (st == 7) PIN(gate);
        }
        if constexpr (st == 1 || st == 4) {
          if (!__all(*(const LAS u64*)((lds_cptr)lds + L_FL + lane * 16) == tag)) wait_flags(flg, st == 1 ? fiA : fiOA, tag);
        }
      }
      __builtin_amdgcn_s_setprio(3 - sl);
      k0 = ldsr128(kp0 + nk); k1 = ldsr64(kp1 + nk);
      if constexpr (sst >= 1 && sst + 1 < 8 && sl == 0) kpiece(IC<sst + 1>{});
      if constexpr (sst == 0 && sl < 3) st128(rs, dstA[sl - 1] + BR_A, pk[2 * sl - 2]);
      if constexpr ((sst == 1 || sst == 4) && sl == 3) { if (w == 7) dma_sc1<L_FL>(ws, (sst == 1 ? fiA : fiOA) * 128, lds0); }
      if constexpr (sst == 2 && sl == 0) dma_sc1<0>(ws, lalo + ((c == 0 && (w == 3 || (w < 3 && lane < 16))) ? 1024 : 0), ladst);
      if constexpr (sst == 2 && sl == 1) { dma3_sc1<1024>(ws + 1024, lalo, ladst); if (w < 3) dma_sc1<3328>(ws + 3328, lalo, ladst); }
      if constexpr ((sst == 4 || sst == 5) && (sl == 1 || sl == 2)) woquarter(IC<0>{}, IC<2 * sl - 2 + sst - 4>{}, IC<L_R + (2 * sl - 2 + sst - 4) * WQ>{});
      if constexpr (sst == 7 && sl == 0) woquarter(IC<1>{}, IC<1>{}, IC<L_RING>{});
      if constexpr (sst == 8) woquarter(IC<1>{}, IC<3>{}, IC<L_RING + AT_STAGE>{});
      if constexpr (sst == 7 && sl > 0) { wf[4 * sl - 4] = ldsr128(wl + WQ + (4 * sl - 4) * 1024); wf[4 * sl - 3] = ldsr128(wl + WQ + (4 * sl - 3) * 1024); }
      if constexpr (sst == 7 && (sl == 1 || sl == 2)) woquarter(IC<1>{}, IC<2 * sl - 2>{}, IC<L_R + (2 * sl - 2) * WQ>{});
      EX2(sa, 10); opa(IC<ha>{}, IC<2>{}); EX2(sa, 12); PIN(sa); SBAR();
      __builtin_amdgcn_s_waitcnt(0xC07F); SBAR();
      oa = smf(v0, sa, 0, oa, idx); SBAR(); EX2(sa, 14); opa(IC<ha>{}, IC<3>{}); PIN(sa); SBAR();
      EX2(sb, 0); opa(IC<ha>{}, IC<4>{}); PIN(sb); SBAR();
      oa = smf(v1, sa, 1, oa, idx); SBAR(); EX2(sb, 2); opa(IC<hb>{}, IC<0>{}); EX2(sb, 4); PIN(sb); SBAR();
      sa = MFMA32(k0, q0[0], f32x16{}); SBAR(); EX2(sb, 6); opa(IC<hb>{}, IC<1>{}); EX2(sb, 8); PIN(sb); SBAR();
      sa = MFMA8(k1, q1[0], sa); SBAR();
      if constexpr (sst >= 1 && sst + 1 < 8 && sl == 0) vpiece(IC<sst + 1>{});
      if constexpr (sst == 0 && sl < 3) st128(rs, dstA[sl - 1] + (BR_A + (cj >= 10 ? 1024 : 2 * UNIT)), pk[2 * sl - 1]);
      if constexpr (sst == 1 && sl == 1) {
        st128(rs, obm, u32x4{~0u, ~0u, ~0u, ~0u}); st64(rs, obt, u32x2{~0u, ~0u});
      }
      if constexpr (sst == 2 && sl == 0) {
        const unsigned to = WS_QKB + BR_A + (unsigned)(256 * c + 32 * w + r) * 16;
        lq = ld128(rs, (ua + hh) * UNIT + to);
        lqt = ld64(rs, us * UNIT + to + 8 * e);
      }
      if constexpr (sst == 1 && sl == 0) {
#pragma unroll
        for (int i = 0; i < 5; ++i) { gx[i] = ((const unsigned*)(x + (size_t)(256 * c + 8 * h + w) * (D_ * 2)))[lane * 5 + i]; gg[i] = ((const unsigned*)gw)[lane * 5 + i]; }
      }
      if constexpr ((sst == 5 || sst == 7) && (sl == 1 || sl == 2)) {
#pragma unroll
        for (int j = (sst == 7 ? 5 : 0) + 3 * sl - 3; j < (sst == 7 ? 5 : 0) + (sl == 1 ? 3 : 5); ++j) {
          if (j % 5 < 4) of[j] = ld128(rs, WS_OA + osrc + j * 1024);
          else { const u32x2 a = ld64(rs, WS_OA + osrc - lane * 8 + j * 1024), b = ld64(rs, WS_OA + osrc - lane * 8 + j * 1024 + 512); of[j] = u32x4{a[0], a[1], b[0], b[1]}; }
        }
        if constexpr (sst == 7 && sl == 2) gate = __uint_as_float(ld32(rs, WS_G + (rt * 64 + tq * 16 + (lane & 15)) * 4));
      }
      if constexpr (sst == 7 && sl > 0) { wf[4 * sl - 2] = ldsr128(wl + WQ + (4 * sl - 2) * 1024); wf[4 * sl - 1] = ldsr128(wl + WQ + (4 * sl - 1) * 1024); }
      v1p = v1;
      const bf16x8 v0n = ldsr128(vp + nv); v1 = ldsr128(vp + nv + 1024);
      EX2(sb, 10); opa(IC<hb>{}, IC<2>{}); EX2(sb, 12); PIN(sb); SBAR();
      ob = smf(v0, sb, 0, ob, idx); SBAR();
      v0 = v0n;
      EX(sb, 14); opa(IC<hb>{}, IC<3>{}); EX(sb, 15); opa(IC<hb>{}, IC<4>{}); PIN(sb); SBAR();
      if constexpr (st == 2 && i == 3) local_attention();
    });
  });
  ob = smf(v1p, sb, 1, ob, idx);
  __builtin_amdgcn_s_setprio(0);
  float ofin[12];
  {
    f32x4* xw = (f32x4*)(lds + L_R + (2 * kh + 1) * WQ + tq * 3072) + lane;
    const f32x4* xr = (const f32x4*)(lds + L_R + (3 - 2 * kh) * WQ + tq * 3072) + lane;
#pragma unroll
    for (int i = 0; i < 3; ++i) xw[i * 64] = f32x4{oa[4 * i], oa[4 * i + 1], oa[4 * i + 2], oa[4 * i + 3]};
    wait_bar<63>();
#pragma unroll
    for (int i = 0; i < 3; ++i) { const f32x4 v = xr[i * 64]; for (int k = 0; k < 4; ++k) ofin[4 * i + k] = ob[4 * i + k] + v[k]; }
  }
  {
    auto rr = __builtin_amdgcn_permlane32_swap(__float_as_uint(ofin[8]), __float_as_uint(ofin[8]), false, false);
    float l = __uint_as_float(rr[1]);
    if (__builtin_expect(__any(!(l > 8.67e-19f && l < 1.15e18f)), 0)) {
      const bf16x8 qa = q0[1]; s16x4 qb = q1[1];
      float m = -INFINITY;
#pragma unroll 1
      for (int pass = 0; pass < 2; ++pass) {
        set_ref(qb, pass ? m : 0.f, hh != e);
        f32x16 o = f32x16{};
#pragma unroll 1
        for (int kt = 0; kt < 64; ++kt) {
          const unsigned to = (unsigned)(32 * kt + r) * 16;
          const bf16x8 a0 = __builtin_bit_cast(bf16x8, ld128(rs, WS_QKB + (80 + ua + hh) * UNIT + to));
          const s16x4 a1 = __builtin_bit_cast(s16x4, ld64(rs, hh == e ? WS_QKB + (80 + us) * UNIT + to + 8 * e : WS_C + blockIdx.x * 4096));
          f32x16 s = MFMA32(a0, qa, f32x16{}); s = MFMA8(a1, qb, s);
          if (pass == 0) { m = fmaxf(m, max16(s)); continue; }
#pragma unroll
          for (int j = 0; j < 16; ++j) EX(s, j);
#pragma unroll
          for (int s2 = 0; s2 < 2; ++s2) {
            const u32x4 vv = ld128(rs, r < 20 ? WS_VB + (h * 64 + kt) * 2048 + (2 * s2 + hh) * 512 + r * 16 : WS_C + blockIdx.x * 4096 + (r == 20 ? 0 : 16));
            o = smf(__builtin_bit_cast(bf16x8, vv), s, s2, o, idx);
          }
        }
        if (pass == 0) m = swap_max(m);
        else {
#pragma unroll
          for (int i = 0; i < 12; ++i) ofin[i] = o[i];
        }
      }
      auto r2 = __builtin_amdgcn_permlane32_swap(__float_as_uint(ofin[8]), __float_as_uint(ofin[8]), false, false);
      l = __uint_as_float(r2[1]);
    }
    const float rl = __builtin_amdgcn_rcpf(l);
    unsigned ow[6];
#pragma unroll
    for (int i = 0; i < 6; ++i) ow[i] = min(cvtpk(ofin[2 * i] * rl, ofin[2 * i + 1] * rl), 0xfffffffeu);
    st128(rs, obm, u32x4{ow[0], ow[1], ow[2], ow[3]});
    st64(rs, obt, u32x2{ow[4], ow[5]});
  }
  SBAR();
#pragma unroll
  for (int f = 12; f < 25; ++f) wf[f] = ldsr128(wl + WQ + f * 1024);
  SBAR();

  f32x4 accB[5];
  u32x4 og[8]; u32x2 ot[4];
  const unsigned ob0 = osrc + SWP * 5120, ob1 = osrc + (SWP ^ 1) * 5120, ot0 = ob0 - lane * 8 + 4096, ot1 = ob1 - lane * 8 + 4096;
#define OBLD(k, r, a, o) "s_bitcmp1_b32 %17, " #k "\n\ts_cbranch_scc0 1f\n\tglobal_load_dwordx4 %" #r ", %" #a ", %16 offset:" #o " sc1\n1:\n\t"
#define OBLT(k, r0, r1, a) "s_bitcmp1_b32 %17, " #k "\n\ts_cbranch_scc0 1f\n\tglobal_load_dwordx2 %" #r0 ", %" #a ", %16 sc1\n\tglobal_load_dwordx2 %" #r1 ", %" #a ", %16 offset:512 sc1\n1:\n\t"
#define OB_ALL OBLD(0, 0, 12, 0) OBLD(1, 1, 12, 1024) OBLD(2, 2, 12, 2048) OBLD(3, 3, 12, 3072) OBLT(4, 8, 9, 14) OBLD(5, 4, 13, 0) OBLD(6, 5, 13, 1024) OBLD(7, 6, 13, 2048) OBLD(8, 7, 13, 3072) OBLT(9, 10, 11, 15)
#define OG(c) c(og[0]), c(og[1]), c(og[2]), c(og[3]), c(og[4]), c(og[5]), c(og[6]), c(og[7]), c(ot[0]), c(ot[1]), c(ot[2]), c(ot[3])
#define OBL1(k, a, o) og[k] = ld128(rs, WS_OB + a + o)
#define OBL2(k, a, o) ot[k] = ld64(rs, WS_OB + a + o)
#define OFR(j) ((j) % 5 < 4 ? og[((j) - (j) / 5) & 7] : u32x4{ot[2 * ((j) / 5)][0], ot[2 * ((j) / 5)][1], ot[2 * ((j) / 5) + 1][0], ot[2 * ((j) / 5) + 1][1]})
#pragma unroll
  for (int j = 0; j < 5; ++j) {
    if (j == 0) __builtin_amdgcn_s_setprio(3); if (j == 2) __builtin_amdgcn_s_setprio(2); if (j == 3) __builtin_amdgcn_s_setprio(1); if (j == 4) __builtin_amdgcn_s_setprio(0);
#pragma unroll
    for (int n = 0; n < 5; ++n) accA[n] = MFMA16(wf[j * 5 + n], of[5 + j], accA[n]);
    SBAR();
#pragma unroll
    for (int n = 0; n < 5; ++n) wf[j * 5 + n] = ldsr128(wl + (j * 5 + n) * 1024);
    if (j == 2) { OBL1(0, ob0, 0); OBL1(1, ob0, 1024); OBL1(2, ob0, 2048); }
    if (j == 3) { OBL1(3, ob0, 3072); OBL2(0, ot0, 0); OBL2(1, ot0, 512); }
    if (j == 4) { OBL1(4, ob1, 0); OBL1(5, ob1, 1024); OBL1(6, ob1, 2048); }
    if (j == 4) { OBL1(7, ob1, 3072); OBL2(2, ot1, 0); OBL2(3, ot1, 512); }
    SBAR();
  }
  waitv_bar<12>();
  SBAR();
  const lds_cptr wh = (lds_cptr)lds + L_RING + kh * AT_STAGE + lane * 16;
#pragma unroll
  for (int j = 0; j < 10; ++j) {
    if (j % 5 == 0) {
      unsigned seen = 0;
#pragma unroll
      for (int k = j; k < j + 5; ++k) { const u32x4 v = OFR(k); seen = max(max(seen, v[0]), v[1]); seen = max(max(seen, v[2]), v[3]); }
      if (__any(seen == ~0u)) {
#pragma unroll 1
        for (unsigned spins = 0; spins < (1u << 16); ++spins) {
          unsigned pend = 0;
#pragma unroll
          for (int k = j; k < 10; ++k) { const u32x4 v = OFR(k); pend |= __any(max(max(v[0], v[1]), max(v[2], v[3])) == ~0u) ? (1u << k) : 0u; }
          if (!(pend & (0x1fu << j))) break;
          asm volatile(OB_ALL "s_waitcnt vmcnt(0)" : OG("+v") : "v"(ob0), "v"(ob1), "v"(ot0), "v"(ot1), "s"(ws + WS_OB), "s"(pend) : "memory", "scc");
        }
      }
    }
    if (j == 0) __builtin_amdgcn_s_setprio(3); if (j == 3) __builtin_amdgcn_s_setprio(2); if (j == 6) __builtin_amdgcn_s_setprio(1); if (j == 8) __builtin_amdgcn_s_setprio(0);
#pragma unroll
    for (int n = 0; n < 5; ++n) accB[n] = MFMA16(wf[(j % 5) * 5 + n], OFR(j), j == 0 ? f32x4{} : accB[n]);
    if (j < 5) {
#pragma unroll
      for (int n = 0; n < 5; ++n) wf[j * 5 + n] = ldsr128(wh + (j * 5 + n) * 1024);
    }
    SBAR();
  }
#pragma unroll
  for (int n = 0; n < 5; ++n) accB[n] = accA[n] * gate + accB[n] * (1.f - gate);
  {
    char* img = lds + L_R + (2 * kh + 1) * WQ + (tq * 16 + (lane & 15)) * 336 + (lane >> 4) * 16;
#pragma unroll
    for (int n = 0; n < 5; ++n) *(f32x4*)(img + n * 64) = accB[n];
    wait_bar<63>();
#pragma unroll
    for (int it = 0; it < 2; ++it) {
      const int u = tid + 512 * it, row = u / 10, pc = u - 10 * row;
      if (u < 640) {
        const char* a = lds + L_R + WQ + row * 336 + pc * 32;
        const f32x4 v0 = *(const f32x4*)a + *(const f32x4*)(a + 2 * WQ), v1 = *(const f32x4*)(a + 16) + *(const f32x4*)(a + 2 * WQ + 16);
        const u32x4 r4 = u32x4{cvtpk(v0[0], v0[1]), cvtpk(v0[2], v0[3]), cvtpk(v1[0], v1[1]), cvtpk(v1[2], v1[3])};
        char* op = out + (size_t)(rt * 64 + row) * (D_ * 2) + ct * 160 + pc * 16;
        *(u32x4*)op = r4;
      }
    }
  }
  asm volatile("GM_CODE_END:");
  if (blockIdx.x == 0 && w == 7) {
#pragma unroll 1
    for (int i = 0; i < 4; ++i) wait_flags(flg, FOA + i * 64 + lane, tag);
    if (lane == 0) __hip_atomic_store(flg, tag, RLX_AGENT);
  }
}

extern "C" void kernel_launch(void* const* d_in, const int* in_sizes, int n_in, void* d_out, int out_size, void* d_ws,
                              size_t ws_size, hipStream_t stream) {
  (void)hipFuncSetAttribute((const void*)fused_kernel, hipFuncAttributeMaxDynamicSharedMemorySize, LDS_BYTES);
  fused_kernel<<<256, 512, LDS_BYTES, stream>>>((const char*)d_in[0], (const char*)d_in[1], (const char*)d_in[4], (const char*)d_in[2],
                                                (const char*)d_in[5], (const char*)d_in[6], (char*)d_ws, (char*)d_out);
}
```

```cpp
#include <hip/hip_runtime.h>
#include <hip/hip_bf16.h>
#include <stdint.h>

typedef short bf16x8 __attribute__((ext_vector_type(8)));
typedef short s16x4 __attribute__((ext_vector_type(4)));
typedef __bf16 bfv8 __attribute__((ext_vector_type(8)));
typedef __bf16 bfv2 __attribute__((ext_vector_type(2)));
typedef __bf16 bfv16 __attribute__((ext_vector_type(16)));
typedef float f32x16 __attribute__((ext_vector_type(16)));
typedef float f32x8 __attribute__((ext_vector_type(8)));
typedef float f32x4 __attribute__((ext_vector_type(4)));
typedef unsigned u32x4 __attribute__((ext_vector_type(4)));
typedef unsigned u32x2 __attribute__((ext_vector_type(2)));
typedef unsigned long long u64;
#define LAS __attribute__((address_space(3)))
#define GAS __attribute__((address_space(1)))
#define AINL __attribute__((always_inline))
typedef LAS const char* lds_cptr;
typedef LAS char* lds_ptr;
typedef GAS u64 gu64;

constexpr int D_ = 640, N3_ = 1920;
constexpr u64 TAG_STEP = 0x9E3779B97F4A7C15ull;
constexpr float QSCALE = 0.22360679774997896f * 1.4426950408889634f;
constexpr unsigned OOB = 0x70000000u;

constexpr unsigned UNIT = 2048 * 16;
constexpr unsigned F1A = 512, FG = 768, FOA = 1024, F1B = 2048, NREP = 16;
#define REPSTEP 256
constexpr unsigned WS_QKB = (2048 + NREP * 256) * 128;
constexpr unsigned WS_VB = WS_QKB + 160 * UNIT;
constexpr unsigned BR_A = 160 * UNIT + 32 * 64 * 2048;
constexpr unsigned WS_OA = WS_QKB + 2 * BR_A;
constexpr unsigned WS_OB = WS_OA + 2048 * 640 * 2;
constexpr unsigned WS_WO = WS_OB + 2048 * 640 * 2;
constexpr unsigned WS_G = WS_WO + 2 * 640 * 640 * 2;
constexpr unsigned WS_C = WS_G + 2048 * 4;
constexpr unsigned WS_END = WS_C + 256 * 4096;

constexpr int L_R = 0, WQ = 25600;
constexpr int L_RING = 106496, AT_HALF = 14336, AT_STAGE = 2 * AT_HALF;
constexpr int L_FL = 104448, L_EP = 98304, L_PF = 81920, L_Q = 0;
constexpr int L_LK = 51200, LA_UNIT = 4608, L_LV = L_LK + 3 * LA_UNIT;
constexpr int LDS_BYTES = 163840;

#define STAMP(kid, i) do {} while (0)
#define DBG_DECL
#define DBG_ARG
#define DBG_PASS
#define DBG_OUT() do {} while (0)
#define BST 2
#define OB0 2
#define OB1 3
#define OB2 4
#define OB3 4
#define TPRIO(x) __builtin_amdgcn_s_setprio(x)
#define SNAP 3
#define FIN 1
#define SBAR() __builtin_amdgcn_sched_barrier(0)
#define PIN(x) asm volatile("" : "+v"(x))
#define MFMA32(a, b, c) __builtin_amdgcn_mfma_f32_32x32x16_bf16(__builtin_bit_cast(bfv8, a), __builtin_bit_cast(bfv8, b), c, 0, 0, 0)
#define MFMA8(a, b, c) __builtin_amdgcn_mfma_f32_32x32x8bf16_1k(a, b, c, 0, 0, 0)
#define MFMA16(a, b, c) __builtin_amdgcn_mfma_f32_16x16x32_bf16(__builtin_bit_cast(bfv8, a), __builtin_bit_cast(bfv8, b), c, 0, 0, 0)
#define RLX_AGENT __ATOMIC_RELAXED, __HIP_MEMORY_SCOPE_AGENT
#define SC1 16

template <int V> struct IC { static constexpr int value = V; };
template <int I, int N, class F> __device__ __forceinline__ void sfor(F&& f) { if constexpr (I < N) { f(IC<I>{}); sfor<I + 1, N>(f); } }
template <int N> __device__ __forceinline__ void wait_bar() { asm volatile("s_waitcnt vmcnt(%0) lgkmcnt(0)\n\ts_barrier" :: "i"(N) : "memory"); }
template <int N> __device__ __forceinline__ void waitv_bar() { asm volatile("s_waitcnt vmcnt(%0)\n\ts_barrier" :: "i"(N) : "memory"); }

__device__ __forceinline__ unsigned cvtpk(float lo, float hi) {
  bfv2 r; r[0] = (__bf16)lo; r[1] = (__bf16)hi;
  return __builtin_bit_cast(unsigned, r);
}
__device__ __forceinline__ float dot2(unsigned a, unsigned b, float c) {
  return __builtin_amdgcn_fdot2_f32_bf16(__builtin_bit_cast(bfv2, a), __builtin_bit_cast(bfv2, b), c, false);
}
template <int OFF> __device__ __forceinline__ void dma(const char* base, unsigned voff, unsigned lds_base) {
  asm volatile("s_add_u32 m0, %2, %3\n\ts_nop 0\n\tglobal_load_lds_dwordx4 %0, %1" :: "v"(voff), "s"(base), "s"(lds_base), "i"(OFF) : "memory", "scc");
}
template <int OFF> __device__ __forceinline__ void dma_sc1(const char* base, unsigned voff, unsigned lds_base) {
  asm volatile("s_add_u32 m0, %2, %3\n\ts_nop 0\n\tglobal_load_lds_dwordx4 %0, %1 sc1" :: "v"(voff), "s"(base), "s"(lds_base), "i"(OFF) : "memory", "scc");
}
template <int OFF> __device__ __forceinline__ void dma2_sc1(const char* base, unsigned voff, unsigned lds_base) {
  asm volatile("s_add_u32 m0, %2, %3\n\ts_nop 0\n\tglobal_load_lds_dwordx4 %0, %1 sc1\n\tglobal_load_lds_dwordx4 %0, %1 offset:1024 sc1"
               :: "v"(voff), "s"(base), "s"(lds_base), "i"(OFF) : "memory", "scc");
}
template <int OFF> __device__ __forceinline__ void dma3_sc1(const char* base, unsigned voff, unsigned lds_base) {
  asm volatile("s_add_u32 m0, %2, %3\n\ts_nop 0\n\tglobal_load_lds_dwordx4 %0, %1 sc1\n\tglobal_load_lds_dwordx4 %0, %1 offset:1024 sc1\n\t"
               "global_load_lds_dwordx4 %0, %1 offset:2048 sc1" :: "v"(voff), "s"(base), "s"(lds_base), "i"(OFF) : "memory", "scc");
}
typedef short v4i16_t __attribute__((ext_vector_type(4)));
__device__ __forceinline__ s16x4 trd(lds_cptr p) {
  return __builtin_bit_cast(s16x4, __builtin_amdgcn_ds_read_tr16_b64_v4i16((LAS v4i16_t*)p));
}
__device__ __forceinline__ bf16x8 cat8(s16x4 a, s16x4 b) { return __builtin_shufflevector(a, b, 0, 1, 2, 3, 4, 5, 6, 7); }
__device__ __forceinline__ bf16x8 ldsr128(lds_cptr p) { return *(const LAS bf16x8*)p; }
__device__ __forceinline__ s16x4 ldsr64(lds_cptr p) { return *(const LAS s16x4*)p; }
__device__ __forceinline__ u32x4 pack8(const f32x16& a, int s, float sc) {
  return u32x4{cvtpk(a[8 * s] * sc, a[8 * s + 1] * sc), cvtpk(a[8 * s + 2] * sc, a[8 * s + 3] * sc),
               cvtpk(a[8 * s + 4] * sc, a[8 * s + 5] * sc), cvtpk(a[8 * s + 6] * sc, a[8 * s + 7] * sc)};
}
__device__ __forceinline__ int swap23(int r) { return (r & 0x13) | ((r & 4) << 1) | ((r & 8) >> 1); }
__device__ __forceinline__ u64 uni64(unsigned lo, unsigned hi) {
  return ((u64)(unsigned)__builtin_amdgcn_readfirstlane(hi) << 32) | (unsigned)__builtin_amdgcn_readfirstlane(lo);
}

typedef __amdgpu_buffer_rsrc_t rsrc_t;
__device__ __forceinline__ void st128(rsrc_t rs, unsigned off, u32x4 v) { __builtin_amdgcn_raw_buffer_store_b128(v, rs, off, 0, SC1); }
__device__ __forceinline__ void st64(rsrc_t rs, unsigned off, u32x2 v) { __builtin_amdgcn_raw_buffer_store_b64(v, rs, off, 0, SC1); }
__device__ __forceinline__ u32x4 ld128(rsrc_t rs, unsigned off) { return __builtin_amdgcn_raw_buffer_load_b128(rs, off, 0, SC1); }
__device__ __forceinline__ u32x2 ld64(rsrc_t rs, unsigned off) { return __builtin_amdgcn_raw_buffer_load_b64(rs, off, 0, SC1); }
__device__ __forceinline__ unsigned ld32(rsrc_t rs, unsigned off) { return __builtin_amdgcn_raw_buffer_load_b32(rs, off, 0, SC1); }

template <int SLEEP = 6, int ROT = 0>
__device__ __forceinline__ void wait_flags(gu64* flg, unsigned idx, u64 tag) {
  bool ok = false;
#pragma unroll 1
  for (unsigned spins = 0; spins < (1u << 18); ++spins) {
    if (!ok) ok = __hip_atomic_load(flg + (idx + min(spins, (unsigned)ROT) * 256) * 16, RLX_AGENT) == tag;
    if (__all(ok)) break;
    __builtin_amdgcn_s_sleep(SLEEP);
  }
  asm volatile("" ::: "memory");
}
__device__ __forceinline__ void sample_flags(u64& fa, u64& fb, unsigned ia, unsigned ib, const char* base, u64 miss) {
  asm volatile("s_cmp_eq_u64 %5, 0\n\ts_cbranch_scc1 1f\n\tglobal_load_dwordx2 %0, %2, %4 sc1\n\tglobal_load_dwordx2 %1, %3, %4 sc1\n1:"
               : "+v"(fa), "+v"(fb) : "v"(ia * 128), "v"(ib * 128), "s"(base), "s"(miss) : "memory", "scc");
}

__device__ __forceinline__ unsigned o_main(int tok, int h, int hh) {
  const int m = h >> 1;
  return (unsigned)((((tok >> 4) * 20 + 5 * (m >> 2) + (m & 3)) * 64 + (2 * (h & 1) + hh) * 16 + (tok & 15)) * 16);
}
__device__ __forceinline__ unsigned o_tail(int tok, int h) {
  const int m = h >> 1;
  return (unsigned)(((tok >> 4) * 20 + 5 * (m >> 2) + 4) * 1024 + 512 * (m & 1) + ((2 * ((m >> 1) & 1) + (h & 1)) * 16 + (tok & 15)) * 8);
}

#define G1_ORDER "BBBBBBBBBBAAAAAAAAAA"
#define G1_PUB 14
#define G1_EP 12
#define G1_SAMP 72
#define G1_LOOK 76
#define G1_AGAIN true
struct G1 {
  int isB[24] = {}, kb[24] = {}, uf[25] = {};
  int lastB = 0;
  int iss_lo[20] = {}, iss_hi[20] = {};
  int wait[20] = {};
  constexpr G1() {
    const char* ord = G1_ORDER;
    int nb = 0, na = 0;
    for (int o = 0; o < 24; ++o) {
      isB[o] = o < 20 && ord[o] == 'B'; kb[o] = o < 20 ? (isB[o] ? nb++ : na++) : 0;
      uf[o + 1] = uf[o] + (o < 20 ? 1 + isB[o] : 0);
      if (isB[o]) lastB = o;
    }
    int pos[60] = {}, cnt = 0, lastst = 0, done = 2 * uf[2];
    for (int d = 0; d < done; ++d) pos[d] = cnt++;
    for (int o = 0; o < 20; ++o) {
      int need = pos[2 * uf[o + 1] - 1];
      if (o == G1_PUB && lastst > need) need = lastst;
      wait[o] = o >= 18 ? 0 : cnt - 1 - need;
      if (o == G1_EP) lastst = cnt++;
      iss_lo[o] = done; iss_hi[o] = 2 * uf[o + 4];
      for (int ks = 0; ks < 4; ++ks) {
        const int per = (iss_hi[o] - iss_lo[o] + 3) / 4;
        for (int z = 0; z < per && done < iss_hi[o]; ++z) pos[done++] = cnt++;
        const int n = 4 * o + ks - 2 - (4 * lastB + 3);
        if (n >= 0 && n < 4 * BST && n % BST == 0) lastst = cnt++;
      }
    }
  }
};
constexpr int g1_slot(int u) { return ((u + 6) % 10) * 16384; }
template <bool VT, class FPiece, class FStart, class FSetup>
__device__ __forceinline__ void g1_body(char* ws, rsrc_t rs, char* lds, int ti, int cj, int w, int lane, u64& tag, bool& early, unsigned& fiKV, unsigned& fiQ,
                                        u32x4 (&pk)[4], unsigned (&dstB)[2], FPiece&& piece, FStart&& att_start, FSetup&& setup DBG_ARG) {
  constexpr G1 P;
  static_assert(P.uf[G1_EP - 1] > 20 && G1_EP < G1_PUB && P.uf[4] <= 9, "epoch / prefetch scratch slots");
  const int tg = w & 3, cg = w >> 2;
  const int r = lane & 31, hh = lane >> 5, g16 = (lane >> 4) & 1, q4 = (lane & 15) >> 2, p4 = lane & 3;
  const int tr = VT ? swap23(r) : r;
#pragma unroll
  for (int t2 = 0; t2 < 2; ++t2) {
    if (VT) {
      const int F = (cj - 10) * 128 + 64 * cg + 32 * t2 + r, hd = F / 20;
      int d = F - 20 * hd - 4 * (hd & 1); if (d < 0) d += 20;
      dstB[t2] = WS_VB + (unsigned)((hd * 64 + 4 * ti + tg) * 2048 + hh * 512 + d * 16);
    } else {
      dstB[t2] = WS_QKB + (unsigned)(cj * 16 + 8 * cg + 4 * t2 + hh) * UNIT + (unsigned)(128 * ti + 32 * tg + r) * 16;
    }
  }
  unsigned xoff[4];
#pragma unroll
  for (int ks = 0; ks < 4; ++ks) xoff[ks] = (unsigned)((32 * tg + tr) * 128 + (((2 * ks + hh) ^ ((tr >> 1) & 7)) * 16));
  const int psw = ((p4 & 1) << 1) | (p4 >> 1);
  unsigned woff[2];
#pragma unroll
  for (int t2 = 0; t2 < 2; ++t2) woff[t2] = (unsigned)((8 * hh + q4) * 256 + (((2 * cg + t2) ^ q4) * 64) + 32 * g16 + 8 * (VT ? p4 : psw));
  const float sc = (!VT && cj < 5) ? QSCALE : 1.f;
  bf16x8 xf[40], wf[3][2];
  u64 fa, fb;
  f32x16 accB[2], accA[2];
  u64 miss = 1;
  auto look = [&](bool again) AINL {
    asm volatile("s_cmp_eq_u64 %2, 0\n\ts_cbranch_scc1 1f\n\ts_waitcnt vmcnt(0)\n1:" : "+v"(fa), "+v"(fb) : "s"(miss) : "memory", "scc");
    const u64 m = __ballot(fa != tag || fb != tag);
    if (m == 0 && miss != 0) att_start();
    miss = m;
    if (again) sample_flags(fa, fb, fiKV, fiQ, ws + (F1B + REPSTEP) * 128, miss);
  };
  sfor<0, 82>([&](auto ic) AINL {
    constexpr int nn = decltype(ic)::value, n = nn - 2;
    constexpr int o = nn >> 2, ks = nn & 3;
    if constexpr (nn < 80) {
      if constexpr (ks == 0) {
        if constexpr (o < 19) waitv_bar<P.wait[o]>();
        if constexpr (o == G1_EP) dma_sc1<L_EP>(ws, 0, (unsigned)(uintptr_t)lds + w * 1024);
        if constexpr (o == G1_PUB) { const u32x2 ep = *(const LAS u32x2*)((lds_cptr)lds + L_EP + w * 1024); tag = uni64(ep[0], ep[1]) + TAG_STEP; }
        if constexpr (o == G1_PUB) { if (w == 0 && lane < NREP) __hip_atomic_store((gu64*)ws + (F1B + lane * 256 + cj * 16 + ti) * 16, tag, RLX_AGENT); }
        if constexpr (o % 2 == 0) STAMP(0, 1 + o / 2);
      }
      if constexpr (nn >= 62 && nn < 70 && !(nn & 1)) setup(IC<(nn - 62) / 2>{});
      if constexpr (nn == G1_SAMP) { fa = 0; fb = 0; sample_flags(fa, fb, fiKV, fiQ, ws + F1B * 128, miss); }
      if constexpr (nn == G1_LOOK) look(G1_AGAIN);
      if constexpr (P.isB[o]) xf[4 * P.kb[o] + ks] = ldsr128((lds_cptr)lds + g1_slot(P.uf[o] + 1) + xoff[ks]);
#pragma unroll
      for (int t2 = 0; t2 < 2; ++t2) {
        const lds_cptr wp = (lds_cptr)lds + g1_slot(P.uf[o]) + woff[t2] + ks * 4096;
        wf[nn % 3][t2] = cat8(trd(wp), trd(wp + 1024));
      }
      constexpr int per = (P.iss_hi[o] - P.iss_lo[o] + 3) / 4, d0 = P.iss_lo[o] + per * ks;
      sfor<d0, (d0 + per < P.iss_hi[o] ? d0 + per : P.iss_hi[o])>(piece);
    }
    if constexpr (n >= 0) {
      constexpr int om = n >> 2, xi = 4 * P.kb[om] + (n & 3);
#pragma unroll
      for (int t2 = 0; t2 < 2; ++t2) {
        f32x16& ac = P.isB[om] ? accB[t2] : accA[t2];
        const f32x16 cin = xi == 0 ? f32x16{} : ac;
        ac = VT ? MFMA32(xf[xi], wf[n % 3][t2], cin) : MFMA32(wf[n % 3][t2], xf[xi], cin);
      }
    }
    SBAR();
    constexpr int nb = n - (4 * P.lastB + 3);
    if constexpr (nb == 0) {
#pragma unroll
      for (int i = 0; i < 4; ++i) { pk[i] = VT ? pack8(accB[i >> 1], i & 1, 1.f) : pack8(accB[i >> 1], i & 1, sc); PIN(pk[i]); }
    }
    if constexpr (nb >= 0 && nb < 4 * BST && nb % BST == 0) {
      constexpr int i = nb / BST;
      st128(rs, dstB[i >> 1] + (i & 1) * (VT ? 1024 : 2 * UNIT), pk[i]);
      SBAR();
    }
  });
  STAMP(0, 13);
  look(false);
  early = miss == 0;
#pragma unroll
  for (int i = 0; i < 4; ++i) pk[i] = pack8(accA[i >> 1], i & 1, 1.f);
}

__device__ __forceinline__ f32x16 smf(bf16x8 a, const f32x16& p, int s, f32x16 o, int idx) {
  f32x8 b = s ? __builtin_shufflevector(p, p, 8, 9, 10, 11, 12, 13, 14, 15) : __builtin_shufflevector(p, p, 0, 1, 2, 3, 4, 5, 6, 7);
  return __builtin_amdgcn_smfmac_f32_32x32x32_bf16(__builtin_bit_cast(bfv8, a), __builtin_bit_cast(bfv16, b), o, idx, 0, 0);
}
__device__ __forceinline__ float max16(const f32x16& s) {
  float a = fmaxf(fmaxf(s[0], s[1]), s[2]), b = fmaxf(fmaxf(s[3], s[4]), s[5]);
  a = fmaxf(fmaxf(a, s[6]), s[7]); b = fmaxf(fmaxf(b, s[8]), s[9]);
  a = fmaxf(fmaxf(a, s[10]), s[11]); b = fmaxf(fmaxf(b, s[12]), s[13]);
  a = fmaxf(fmaxf(a, s[14]), s[15]);
  return fmaxf(a, b);
}
__device__ __forceinline__ float swap_max(float m) {
  auto rr = __builtin_amdgcn_permlane32_swap(__float_as_uint(m), __float_as_uint(m), false, false);
  return fmaxf(__uint_as_float(rr[0]), __uint_as_float(rr[1]));
}
__device__ __forceinline__ float swap_add(float m) {
  auto rr = __builtin_amdgcn_permlane32_swap(__float_as_uint(m), __float_as_uint(m), false, false);
  return __uint_as_float(rr[0]) + __uint_as_float(rr[1]);
}
__device__ __forceinline__ void set_ref(s16x4& q1, float m, bool spare) {
  const unsigned a = __float_as_uint(-m) & 0xffff0000u;
  const float r1 = -m - __uint_as_float(a);
  const unsigned b = __float_as_uint(r1) & 0xffff0000u;
  const unsigned c = __float_as_uint(r1 - __uint_as_float(b));
  if (spare) q1 = __builtin_bit_cast(s16x4, u32x2{(a >> 16) | b, c >> 16});
}

__global__ __launch_bounds__(512, 2) void fused_kernel(const char* __restrict__ x, const char* __restrict__ wA, const char* __restrict__ wB,
                                                       const char* __restrict__ woA, const char* __restrict__ woB,
                                                       const char* __restrict__ gw, char* ws, char* __restrict__ out) {
  extern __shared__ __attribute__((aligned(16))) char lds[];
  const int tid = threadIdx.x, lane = tid & 63;
  const int w = __builtin_amdgcn_readfirstlane(tid >> 6);
  const int xcd = blockIdx.x & 7, slot = blockIdx.x >> 3;
  const unsigned lds0 = (unsigned)(uintptr_t)lds;
  gu64* flg = (gu64*)ws;
  const rsrc_t rs = __builtin_amdgcn_make_buffer_rsrc(ws, 0, WS_END, 0x00020000);
  DBG_DECL
  STAMP(0, 0);
  const int ti = 8 * (xcd & 1) + (slot & 7);
  const int cj = 4 * (xcd >> 1) + (slot >> 3);
  const unsigned ldpf = lds0 + (cj < 15 ? L_PF : L_RING);
  if (w == 6) {
    u64 pc; unsigned len;
    asm volatile("s_getpc_b64 %0\nGM_CODE_BEG:\n\ts_mov_b32 %1, GM_CODE_END-GM_CODE_BEG" : "=s"(pc), "=s"(len));
    asm volatile("s_add_u32 m0, %2, %3\n\ts_nop 0\n\tglobal_load_lds_dword %0, %1"
                 :: "v"(min((unsigned)((slot & 7) * 64 + lane) * 128, len)), "s"(pc), "s"(ldpf), "i"(0) : "memory", "scc");
  }
  const int rot = ((slot & 7) * 5 / 4 + (slot >> 3) * 5 / 2 + xcd) % 10;
  unsigned xlo[2], wlo[2];
#pragma unroll
  for (int i = 0; i < 2; ++i) {
    const int row = 16 * w + 8 * i + (lane >> 3);
    xlo[i] = (unsigned)(row * (D_ * 2) + (((lane & 7) ^ ((row >> 1) & 7)) * 16));
    wlo[i] = (unsigned)((8 * w + 4 * i + (lane >> 4)) * (N3_ * 2) + (((lane & 15) ^ (4 * (lane >> 4))) * 16));
  }
  const char* xb = x + (size_t)(128 * ti) * (D_ * 2);
  const char* wBb = wB + cj * 256;
  const char* wAb = wA + cj * 256;
  const unsigned ldw = lds0 + w * 2048;
  auto piece = [&](auto dc) AINL {
    constexpr G1 P;
    constexpr int d = decltype(dc)::value, u = d >> 1, base = g1_slot(u) + (d & 1) * 1024;
    constexpr int o = [&]() { int q = 0; while (P.uf[q + 1] <= u) ++q; return q; }();
    int kb = P.kb[o] + rot; if (kb >= 10) kb -= 10;
    if constexpr (u != P.uf[o]) dma<base>(xb + kb * 128, xlo[d & 1], ldw);
    else dma<base>((P.isB[o] ? wBb : wAb) + (size_t)kb * (64 * N3_ * 2), wlo[d & 1], ldw);
  };
  if (cj < 15) {
    sfor<0, 4>(piece);
    sfor<4, 8>(piece);
  }
  STAMP(0, 14);
  SBAR();
  u64 tag;
  const int h = 4 * xcd + (slot & 3), c = slot >> 2;
  const int e = h & 1, ua = 5 * (h >> 1) + 3 * e, us = 5 * (h >> 1) + 2;
  const int jlo = (5 * (h >> 1) + 2 * e) >> 4, jhi = (5 * (h >> 1) + 2 * e + 2) >> 4;
  const int kh = w >> 2, tq = w & 3;
  const char* kbase = ws + WS_QKB + (size_t)(80 + (tq >= 2 ? us : ua + tq)) * UNIT + 1024 * kh * 16;
  if (w == 5) { const unsigned cv = (lane & 1) ? 0u : 0x3f803f80u; st128(rs, lane < 8 ? WS_C + blockIdx.x * 4096 + (lane >> 1) * 1024 + (lane & 1) * 16 : OOB, u32x4{cv, cv, cv, cv}); }
  const unsigned ldr = lds0 + kh * AT_HALF + tq * 2048;
  unsigned vlo, vstr;
  auto kpiece = [&](auto stc) AINL {
    constexpr int st = decltype(stc)::value;
    if (tq < 3) dma2_sc1<L_RING + (st & 1) * AT_STAGE>(kbase + st * 2048, lane * 16, ldr);
  };
  auto vpiece = [&](auto stc) AINL {
    constexpr int st = decltype(stc)::value;
    dma2_sc1<L_RING + (st & 1) * AT_STAGE + 6144>(ws, vlo + st * vstr, ldr);
  };
  unsigned fiKV, fiQ;
  auto att_start = [&]() AINL {
    kpiece(IC<0>{});
    if (tq == 3) {
      const char* qb = ws + WS_QKB + (256 * c + 128 * kh) * 16;
      dma2_sc1<L_Q>(qb + (size_t)ua * UNIT, lane * 16, lds0 + kh * 2048);
      dma2_sc1<L_Q + 4096>(qb + (size_t)(ua + 1) * UNIT, lane * 16, lds0 + kh * 2048);
      dma2_sc1<L_Q + 8192>(qb + (size_t)us * UNIT, lane * 16, lds0 + kh * 2048);
    }
    vpiece(IC<0>{});
  };

  const int hh = lane >> 5, r = lane & 31;
  const int rt = 4 * xcd + (slot & 3), ct = slot >> 2;
  const int SWP = ct & 1;
  const char* wob = ws + WS_WO + (size_t)ct * 102400 + w * 3072;
  auto woquarter = [&](auto brc, auto qc, auto dc) AINL {
    constexpr int br = decltype(brc)::value, q = decltype(qc)::value, d = decltype(dc)::value;
    const int off = br * 819200 + (q ^ (br & SWP)) * WQ;
    dma3_sc1<d>(wob + off, lane * 16, lds0 + w * 3072);
    if (w == 0) dma_sc1<d + 24576>(wob + off + 24576, lane * 16, lds0);
  };
  const unsigned ladst = lds0 + (w < 3 ? L_LK + w * LA_UNIT : L_LV + min(4 * (w - 3), 13) * 1024);
  unsigned osrc, fiA, obm, obt, lalo; int idx;
  lds_cptr kp0, kp1, vp, qp;
  f32x16 oa, ob;
  auto setup = [&](auto pc) AINL {
    constexpr int p = decltype(pc)::value;
    int ln = lane; PIN(ln);
    const int hh = ln >> 5, r = ln & 31;
    if constexpr (p == 0) {
      const unsigned crow = WS_C + blockIdx.x * 4096 + (r == 20 ? 0 : 16);
      vlo = r < 20 ? WS_VB + (h * 64 + 32 * kh + tq) * 2048 + ln * 16 : crow; vstr = r < 20 ? 8192u : 0u;
      const unsigned lab = w < 3 ? WS_QKB + BR_A + (80 + (w == 2 ? us : ua + w)) * UNIT + (256 * c - 16) * 16
                                 : WS_VB + BR_A + (h * 64 + 8 * c) * 2048 - 1024 + min(4 * (w - 3), 13) * 1024;
      lalo = (w < 3 || r < 20) ? lab + ln * 16 : crow;
      osrc = (unsigned)((rt * 4 + tq) * 20 + 10 * kh) * 1024 + ln * 16;
      PIN(vlo); PIN(vstr); PIN(lalo); PIN(osrc);
    }
    if constexpr (p == 1) {
      fiA = ln < 18 ? F1A + (5 * (ln / 6) + (((ln / 3) & 1) ? jhi : jlo)) * 16 + max(2 * c - 1 + ln % 3, 0) : FG + (ln == 18 ? ct : 8 + ct);
      obm = WS_OB + o_main(256 * c + 64 * tq + 32 * kh + r, h, hh); obt = (hh ? OOB : 0u) + WS_OB + o_tail(256 * c + 64 * tq + 32 * kh + r, h);
      PIN(fiA); PIN(obm); PIN(obt);
    }
    if constexpr (p == 2) {
      const lds_cptr ring = (lds_cptr)lds + L_RING + kh * AT_HALF;
      kp0 = ring + hh * 2048 + r * 16;
      kp1 = (hh == e) ? ring + 2 * 2048 + r * 16 + 8 * hh : ring + 6144 + 20 * 16;
      vp = ring + 6144 + hh * 512 + r * 16;
      qp = (lds_cptr)lds + L_Q + (64 * tq + r) * 16;
      idx = 0xDDDD;
      PIN(kp0); PIN(kp1); PIN(vp); PIN(qp); PIN(idx);
    }
    if constexpr (p == 3) {
      oa = f32x16{}; ob = f32x16{}; PIN(oa); PIN(ob);
      fiKV = (5 + 5 * hh + ((ln & 16) ? jhi : jlo)) * 16 + (ln & 15); fiQ = ((ln & 1) ? jhi : jlo) * 16 + 2 * c + ((ln >> 1) & 1);
      PIN(fiKV); PIN(fiQ);
    }
  };

  bool early = false;
  u32x4 pk[4]; unsigned dstA[2];
  if (cj < 10) g1_body<false>(ws, rs, lds, ti, cj, w, lane, tag, early, fiKV, fiQ, pk, dstA, piece, att_start, setup DBG_PASS);
  else if (cj < 15) g1_body<true>(ws, rs, lds, ti, cj, w, lane, tag, early, fiKV, fiQ, pk, dstA, piece, att_start, setup DBG_PASS);
  else {
    const char* wo = ((ti & 8) ? woB : woA) + (size_t)(80 * w) * (D_ * 2) + (ti & 7) * 160;
#pragma unroll 1
    for (int p = 0; p < 13; ++p) {
      const int s0 = min(64 * p, 736), sl = s0 + lane;
      dma<0>(wo, (unsigned)((sl / 10) * (D_ * 2) + (sl % 10) * 16), lds0 + w * 12800 + s0 * 16);
    }
    const u64 ep = __hip_atomic_load(flg, RLX_AGENT);
    tag = uni64((unsigned)ep, (unsigned)(ep >> 32)) + TAG_STEP;
    wait_bar<0>();
    const int g4 = lane >> 4, rl = 320 * kh + 4 * (g4 & 1) + ((lane & 15) >> 2);
#pragma unroll
    for (int ff = 0; ff < 13; ++ff) {
      const int f = min(tq + 4 * ff, 49), j = f / 5, n = f - 5 * j;
      const bool mn = j % 5 < 4;
      const int row = rl + 160 * (j / 5) + (mn ? 40 * (j % 5) + 24 * (g4 >> 1) : 80 * (g4 >> 1) + 16);
      const lds_cptr p = (lds_cptr)lds + row * 160 + n * 32 + 8 * (lane & 3);
      st128(rs, WS_WO + (unsigned)(ti * 100 + kh * 50 + f) * 1024 + lane * 16, __builtin_bit_cast(u32x4, cat8(trd(p), trd(p + (mn ? 8 : 40) * 160))));
    }
    wait_bar<0>();
    if (tid == 0) __hip_atomic_store(flg + (FG + ti) * 16, tag, RLX_AGENT);
    if (w == 0) { wait_flags<16>(flg, F1B + (NREP - 1) * 256 + jlo * 16 + 2 * c, tag); }
    wait_bar<63>();
    sfor<0, 4>(setup);
    dstA[0] = OOB; dstA[1] = OOB;
#pragma unroll
    for (int i = 0; i < 4; ++i) pk[i] = u32x4{0u, 0u, 0u, 0u};
  }
  const unsigned flag1 = F1A + cj * 16 + ti;

  STAMP(1, 0);
  if (!early) {
    bool oka = false, okb = false;
#pragma unroll 1
    for (unsigned spins = 0; spins < (1u << 18); ++spins) {
      const unsigned rp = F1B + (2 + min(spins, NREP - 4)) * REPSTEP;
      const u64 a = __hip_atomic_load(flg + (rp + (oka ? fiQ : fiKV)) * 16, RLX_AGENT), b = __hip_atomic_load(flg + (rp + fiQ) * 16, RLX_AGENT);
      oka = oka || a == tag; okb = okb || b == tag;
      if (__all(oka && okb)) break;
      __builtin_amdgcn_s_sleep(2);
    }
    asm volatile("" ::: "memory");
    att_start();
  }
  STAMP(1, 1);
  wait_bar<0>();
  STAMP(1, 2);
  float gate;
  const unsigned fiOA = FOA + (rt >> 2) * 32 + r;
  u32x4 lq; u32x2 lqt;
  f32x16 sa, sb;
  bf16x8 q0[2]; s16x4 q1[2];
#pragma unroll
  for (int t = 0; t < 2; ++t) {
    q0[t] = ldsr128(qp + hh * 4096 + ((t ^ kh ^ 1) * 512));
    q1[t] = ldsr64(qp + 8192 + 8 * e + ((t ^ kh ^ 1) * 512));
  }
  const bf16x8 kr0 = ldsr128(kp0 - kh * AT_HALF); const s16x4 kr1 = ldsr64(kp1 - kh * AT_HALF);
  bf16x8 k0 = ldsr128(kp0); s16x4 k1 = ldsr64(kp1);
  bf16x8 v0 = ldsr128(vp), v1 = ldsr128(vp + 1024);
  bf16x8 v1p = v1;
  SBAR();
  {
    set_ref(q1[0], 0.f, hh != e); set_ref(q1[1], 0.f, hh != e);
    f32x16 s = MFMA32(kr0, q0[0], f32x16{}), t = MFMA32(kr0, q0[1], f32x16{});
    s = MFMA8(kr1, q1[0], s); t = MFMA8(kr1, q1[1], t);
    sa = MFMA32(k0, q0[0], f32x16{});
    SBAR();
    kpiece(IC<1>{}); vpiece(IC<1>{});
    SBAR();
    set_ref(q1[0], swap_max(max16(s)), hh != e);
    sa = MFMA8(k1, q1[0], sa);
    SBAR();
    set_ref(q1[1], swap_max(max16(t)), hh != e);
  }
  STAMP(1, 3);
  u32x4 of[10];
  unsigned gx[5], gg[5]; float gs;

  auto local_attention = [&]() AINL {
    const int tok = 256 * c + 32 * w + r;
    const lds_cptr lk = (lds_cptr)lds + L_LK + (32 * w + r) * 16, lv = (lds_cptr)lds + L_LV + (4 * w + hh) * 512 + r * 16;
    const bf16x8 qa = __builtin_bit_cast(bf16x8, lq);
    const s16x4 qb = __builtin_bit_cast(s16x4, (hh == e) ? lqt : u32x2{0u, 0u});
    f32x16 sx = MFMA32(ldsr128(lk + hh * LA_UNIT), qa, f32x16{}), sy = MFMA32(ldsr128(lk + hh * LA_UNIT + 512), qa, f32x16{});
    sx = MFMA8(ldsr64(lk + 2 * LA_UNIT + 8 * e), qb, sx); sy = MFMA8(ldsr64(lk + 2 * LA_UNIT + 8 * e + 512), qb, sy);
    const bf16x8 va = ldsr128(lv), vb = ldsr128(lv + 1024), vc = ldsr128(lv + 2048);
    const u64 edge = (c == 0 && w == 0) ? 0ull : ~0ull;
    const float ninf = -INFINITY;
    sfor<0, 24>([&](auto jc) AINL {
      constexpr int j = decltype(jc)::value;
      constexpr u64 mk = []() { u64 m = 0; for (int l = 0; l < 64; ++l) { const int d = (l & 31) + 16 - ((j & 3) + 8 * (j >> 2) + 4 * (l >> 5)); if (d >= 0 && d <= 14 && !(d & 1)) m |= 1ull << l; } return m; }();
      const bool ok = __builtin_amdgcn_inverse_ballot_w64(j < 8 ? mk & edge : mk);
      if constexpr (j < 16) sx[j] = ok ? sx[j] : ninf; else sy[j - 16] = ok ? sy[j - 16] : ninf;
    });
    float m = fmaxf(fmaxf(max16(sx), sy[0]), sy[1]);
    m = fmaxf(fmaxf(m, sy[2]), sy[3]); m = fmaxf(fmaxf(m, sy[4]), sy[5]); m = -QSCALE * swap_max(fmaxf(fmaxf(m, sy[6]), sy[7]));
#pragma unroll
    for (int j = 0; j < 16; ++j) sx[j] = __builtin_amdgcn_exp2f(__builtin_fmaf(sx[j], QSCALE, m));
#pragma unroll
    for (int j = 0; j < 8; ++j) sy[j] = __builtin_amdgcn_exp2f(__builtin_fmaf(sy[j], QSCALE, m));
    f32x16 o = smf(va, sx, 0, f32x16{}, idx); o = smf(vb, sx, 1, o, idx); o = smf(vc, sy, 0, o, idx);
    auto lr = __builtin_amdgcn_permlane32_swap(__float_as_uint(o[8]), __float_as_uint(o[8]), false, false);
    const float rl = __builtin_amdgcn_rcpf(__uint_as_float(lr[1]));
    st128(rs, WS_OA + o_main(tok, h, hh), pack8(o, 0, rl));
    st64(rs, hh ? OOB : WS_OA + o_tail(tok, h), u32x2{cvtpk(o[8] * rl, o[9] * rl), cvtpk(o[10] * rl, o[11] * rl)});
    gs += __builtin_bit_cast(float, __builtin_amdgcn_update_dpp(0, __builtin_bit_cast(int, gs), 0xB1, 0xf, 0xf, true));
    gs += __builtin_bit_cast(float, __builtin_amdgcn_update_dpp(0, __builtin_bit_cast(int, gs), 0x4E, 0xf, 0xf, true));
    gs += __builtin_bit_cast(float, __builtin_amdgcn_update_dpp(0, __builtin_bit_cast(int, gs), 0x141, 0xf, 0xf, true));
    gs += __builtin_bit_cast(float, __builtin_amdgcn_update_dpp(0, __builtin_bit_cast(int, gs), 0x140, 0xf, 0xf, true));
    { auto g2 = __builtin_amdgcn_permlane16_swap(__float_as_uint(gs), __float_as_uint(gs), false, false); gs = __uint_as_float(g2[0]) + __uint_as_float(g2[1]); }
    gs = swap_add(gs);
    const float gv = __builtin_amdgcn_rcpf(1.f + __builtin_amdgcn_exp2f(-1.4426950408889634f * gs));
    __builtin_amdgcn_raw_buffer_store_b32(__float_as_uint(gv), rs, lane == 0 ? WS_G + (256 * c + 8 * h + w) * 4 : OOB, 0, SC1);
    SBAR();
  };

  const lds_cptr wl = (lds_cptr)lds + L_R + kh * (2 * WQ) + lane * 16;
  f32x4 accA[5];
  bf16x8 wq[2][5], wf[25];
  auto opa = [&](auto hc, auto gc) AINL {
    constexpr int hs = decltype(hc)::value, g = decltype(gc)::value;
    if constexpr (hs >= 0 && hs <= 5) SBAR();
    if constexpr (hs >= 1 && hs <= 5) accA[g] = MFMA16(wq[(hs - 1) & 1][g], of[hs - 1], hs == 1 ? f32x4{} : accA[g]);
    if constexpr (hs >= 0 && hs < 5) wq[hs & 1][g] = ldsr128(wl + (hs * 5 + g) * 1024);
    if constexpr (hs >= 0 && hs <= 5) SBAR();
  };
#define EX(S, j) S[j] = __builtin_amdgcn_exp2f(S[j])
#define EX2(S, j) do { EX(S, j); EX(S, j + 1); } while (0)
#define EX4(S, j) do { EX(S, j); EX(S, j + 1); EX(S, j + 2); EX(S, j + 3); } while (0)
  sfor<0, 8>([&](auto stc) AINL {
    constexpr int st = decltype(stc)::value;
    constexpr int cur = (st & 1) * AT_STAGE, nxt = ((st + 1) & 1) * AT_STAGE;
    sfor<0, 4>([&](auto ic) AINL {
      constexpr int i = decltype(ic)::value;
      constexpr int nk = (i < 3) ? cur + (i + 1) * 512 : nxt, nv = (i < 3) ? cur + (i + 1) * 2048 : nxt;
      constexpr int sl = (i + 1) & 3, sst = (i == 3) ? st + 1 : st;
      constexpr int ha = st == 6 ? 2 * i : -9, hb = st == 6 ? 2 * i + 1 : -9;
      SBAR();
      EX2(sa, 0); PIN(sa); SBAR();
      if constexpr (st + i > 0) ob = smf(v1p, sb, 1, ob, idx);
      SBAR(); EX2(sa, 2); opa(IC<ha>{}, IC<0>{}); EX2(sa, 4); PIN(sa); SBAR();
      sb = MFMA32(k0, q0[1], f32x16{}); SBAR(); EX2(sa, 6); opa(IC<ha>{}, IC<1>{}); EX2(sa, 8); PIN(sa); SBAR();
      sb = MFMA8(k1, q1[1], sb); SBAR();
      if constexpr (i == 3) {
        wait_bar<(st == 4 && SNAP == 2) ? 3 : 0>();
        if constexpr (st == 0) { if (tid == 0) __hip_atomic_store(flg + flag1 * 16, tag, RLX_AGENT); }
        if constexpr (st == 2) {
          PIN(lq); PIN(lqt);
        }
        if constexpr (st == 1) {
          gs = 0.f;
#pragma unroll
          for (int i = 0; i < 5; ++i) gs = dot2(gx[i], gg[i], gs);
          PIN(gs);
        }
        if constexpr (st == 3) { if (tid == 0) __hip_atomic_store(flg + (FOA + c * 32 + h) * 16, tag, RLX_AGENT); }
        if constexpr (st == 5 || st == 7) {
#pragma unroll
          for (int j = 0; j < 5; ++j) PIN(of[(st == 7 ? 5 : 0) + j]);
          if constexpr (st == 7) PIN(gate);
        }
        if constexpr (st == 1 || st == 4) {
          if (!__all(*(const LAS u64*)((lds_cptr)lds + L_FL + lane * 16) == tag)) wait_flags(flg, st == 1 ? fiA : fiOA, tag);
        }
      }
      __builtin_amdgcn_s_setprio(3 - sl);
      k0 = ldsr128(kp0 + nk); k1 = ldsr64(kp1 + nk);
      if constexpr (sst >= 1 && sst + 1 < 8 && sl == 0) kpiece(IC<sst + 1>{});
      if constexpr (sst == 0 && sl < 3) st128(rs, dstA[sl - 1] + BR_A, pk[2 * sl - 2]);
      if constexpr ((sst == 1 || sst == 4) && sl == SNAP) { if (w == 7) dma_sc1<L_FL>(ws, (sst == 1 ? fiA : fiOA) * 128, lds0); }
      if constexpr (sst == 2 && sl == 0) dma_sc1<0>(ws, lalo + ((c == 0 && (w == 3 || (w < 3 && lane < 16))) ? 1024 : 0), ladst);
      if constexpr (sst == 2 && sl == 1) { dma3_sc1<1024>(ws + 1024, lalo, ladst); if (w < 3) dma_sc1<3328>(ws + 3328, lalo, ladst); }
      if constexpr ((sst == 4 || sst == 5) && (sl == 1 || sl == 2)) woquarter(IC<0>{}, IC<2 * sl - 2 + sst - 4>{}, IC<L_R + (2 * sl - 2 + sst - 4) * WQ>{});
      if constexpr (sst == 7 && sl == 0) woquarter(IC<1>{}, IC<1>{}, IC<L_RING>{});
      if constexpr (sst == 8) woquarter(IC<1>{}, IC<3>{}, IC<L_RING + AT_STAGE>{});
      if constexpr (sst == 7 && sl > 0) { wf[4 * sl - 4] = ldsr128(wl + WQ + (4 * sl - 4) * 1024); wf[4 * sl - 3] = ldsr128(wl + WQ + (4 * sl - 3) * 1024); }
      if constexpr (sst == 7 && (sl == 1 || sl == 2)) woquarter(IC<1>{}, IC<2 * sl - 2>{}, IC<L_R + (2 * sl - 2) * WQ>{});
      EX2(sa, 10); opa(IC<ha>{}, IC<2>{}); EX2(sa, 12); PIN(sa); SBAR();
      __builtin_amdgcn_s_waitcnt(0xC07F); SBAR();
      oa = smf(v0, sa, 0, oa, idx); SBAR(); EX2(sa, 14); opa(IC<ha>{}, IC<3>{}); PIN(sa); SBAR();
      EX2(sb, 0); opa(IC<ha>{}, IC<4>{}); PIN(sb); SBAR();
      oa = smf(v1, sa, 1, oa, idx); SBAR(); EX2(sb, 2); opa(IC<hb>{}, IC<0>{}); EX2(sb, 4); PIN(sb); SBAR();
      sa = MFMA32(k0, q0[0], f32x16{}); SBAR(); EX2(sb, 6); opa(IC<hb>{}, IC<1>{}); EX2(sb, 8); PIN(sb); SBAR();
      sa = MFMA8(k1, q1[0], sa); SBAR();
      if constexpr (sst >= 1 && sst + 1 < 8 && sl == 0) vpiece(IC<sst + 1>{});
      if constexpr (sst == 0 && sl < 3) st128(rs, dstA[sl - 1] + (BR_A + (cj >= 10 ? 1024 : 2 * UNIT)), pk[2 * sl - 1]);
      if constexpr (sst == 1 && sl == 1) {
        st128(rs, obm, u32x4{~0u, ~0u, ~0u, ~0u}); st64(rs, obt, u32x2{~0u, ~0u});
      }
      if constexpr (sst == 2 && sl == 0) {
        const unsigned to = WS_QKB + BR_A + (unsigned)(256 * c + 32 * w + r) * 16;
        lq = ld128(rs, (ua + hh) * UNIT + to);
        lqt = ld64(rs, us * UNIT + to + 8 * e);
      }
      if constexpr (sst == 1 && sl == 0) {
#pragma unroll
        for (int i = 0; i < 5; ++i) { gx[i] = ((const unsigned*)(x + (size_t)(256 * c + 8 * h + w) * (D_ * 2)))[lane * 5 + i]; gg[i] = ((const unsigned*)gw)[lane * 5 + i]; }
      }
      if constexpr ((sst == 5 || sst == 7) && (sl == 1 || sl == 2)) {
#pragma unroll
        for (int j = (sst == 7 ? 5 : 0) + 3 * sl - 3; j < (sst == 7 ? 5 : 0) + (sl == 1 ? 3 : 5); ++j) {
          if (j % 5 < 4) of[j] = ld128(rs, WS_OA + osrc + j * 1024);
          else { const u32x2 a = ld64(rs, WS_OA + osrc - lane * 8 + j * 1024), b = ld64(rs, WS_OA + osrc - lane * 8 + j * 1024 + 512); of[j] = u32x4{a[0], a[1], b[0], b[1]}; }
        }
        if constexpr (sst == 7 && sl == 2) gate = __uint_as_float(ld32(rs, WS_G + (rt * 64 + tq * 16 + (lane & 15)) * 4));
      }
      if constexpr (sst == 7 && sl > 0) { wf[4 * sl - 2] = ldsr128(wl + WQ + (4 * sl - 2) * 1024); wf[4 * sl - 1] = ldsr128(wl + WQ + (4 * sl - 1) * 1024); }
      v1p = v1;
      const bf16x8 v0n = ldsr128(vp + nv); v1 = ldsr128(vp + nv + 1024);
      EX2(sb, 10); opa(IC<hb>{}, IC<2>{}); EX2(sb, 12); PIN(sb); SBAR();
      ob = smf(v0, sb, 0, ob, idx); SBAR();
      v0 = v0n;
      EX(sb, 14); opa(IC<hb>{}, IC<3>{}); EX(sb, 15); opa(IC<hb>{}, IC<4>{}); PIN(sb); SBAR();
      if constexpr (st == 2 && i == 3) local_attention();
    });
    STAMP(1, 4 + st);
  });
  ob = smf(v1p, sb, 1, ob, idx);
  __builtin_amdgcn_s_setprio(0);
  float ofin[12];
  {
    f32x4* xw = (f32x4*)(lds + L_R + (2 * kh + 1) * WQ + tq * 3072) + lane;
    const f32x4* xr = (const f32x4*)(lds + L_R + (3 - 2 * kh) * WQ + tq * 3072) + lane;
#pragma unroll
    for (int i = 0; i < 3; ++i) xw[i * 64] = f32x4{oa[4 * i], oa[4 * i + 1], oa[4 * i + 2], oa[4 * i + 3]};
    wait_bar<63>();
#pragma unroll
    for (int i = 0; i < 3; ++i) { const f32x4 v = xr[i * 64]; for (int k = 0; k < 4; ++k) ofin[4 * i + k] = ob[4 * i + k] + v[k]; }
  }
  {
    auto rr = __builtin_amdgcn_permlane32_swap(__float_as_uint(ofin[8]), __float_as_uint(ofin[8]), false, false);
    float l = __uint_as_float(rr[1]);
    if (__builtin_expect(__any(!(l > 8.67e-19f && l < 1.15e18f)), 0)) {
      const bf16x8 qa = q0[1]; s16x4 qb = q1[1];
      float m = -INFINITY;
#pragma unroll 1
      for (int pass = 0; pass < 2; ++pass) {
        set_ref(qb, pass ? m : 0.f, hh != e);
        f32x16 o = f32x16{};
#pragma unroll 1
        for (int kt = 0; kt < 64; ++kt) {
          const unsigned to = (unsigned)(32 * kt + r) * 16;
          const bf16x8 a0 = __builtin_bit_cast(bf16x8, ld128(rs, WS_QKB + (80 + ua + hh) * UNIT + to));
          const s16x4 a1 = __builtin_bit_cast(s16x4, ld64(rs, hh == e ? WS_QKB + (80 + us) * UNIT + to + 8 * e : WS_C + blockIdx.x * 4096));
          f32x16 s = MFMA32(a0, qa, f32x16{}); s = MFMA8(a1, qb, s);
          if (pass == 0) { m = fmaxf(m, max16(s)); continue; }
#pragma unroll
          for (int j = 0; j < 16; ++j) EX(s, j);
#pragma unroll
          for (int s2 = 0; s2 < 2; ++s2) {
            const u32x4 vv = ld128(rs, r < 20 ? WS_VB + (h * 64 + kt) * 2048 + (2 * s2 + hh) * 512 + r * 16 : WS_C + blockIdx.x * 4096 + (r == 20 ? 0 : 16));
            o = smf(__builtin_bit_cast(bf16x8, vv), s, s2, o, idx);
          }
        }
        if (pass == 0) m = swap_max(m);
        else {
#pragma unroll
          for (int i = 0; i < 12; ++i) ofin[i] = o[i];
        }
      }
      auto r2 = __builtin_amdgcn_permlane32_swap(__float_as_uint(ofin[8]), __float_as_uint(ofin[8]), false, false);
      l = __uint_as_float(r2[1]);
    }
    const float rl = __builtin_amdgcn_rcpf(l);
    unsigned ow[6];
#pragma unroll
    for (int i = 0; i < 6; ++i) ow[i] = min(cvtpk(ofin[2 * i] * rl, ofin[2 * i + 1] * rl), 0xfffffffeu);
    st128(rs, obm, u32x4{ow[0], ow[1], ow[2], ow[3]});
    st64(rs, obt, u32x2{ow[4], ow[5]});
  }
  SBAR();
  STAMP(1, 12);
#pragma unroll
  for (int f = 12; f < 25; ++f) wf[f] = ldsr128(wl + WQ + f * 1024);
  SBAR();

  f32x4 accB[5];
  u32x4 og[8]; u32x2 ot[4];
  const unsigned ob0 = osrc + SWP * 5120, ob1 = osrc + (SWP ^ 1) * 5120, ot0 = ob0 - lane * 8 + 4096, ot1 = ob1 - lane * 8 + 4096;
#define OBLD(k, r, a, o) "s_bitcmp1_b32 %17, " #k "\n\ts_cbranch_scc0 1f\n\tglobal_load_dwordx4 %" #r ", %" #a ", %16 offset:" #o " sc1\n1:\n\t"
#define OBLT(k, r0, r1, a) "s_bitcmp1_b32 %17, " #k "\n\ts_cbranch_scc0 1f\n\tglobal_load_dwordx2 %" #r0 ", %" #a ", %16 sc1\n\tglobal_load_dwordx2 %" #r1 ", %" #a ", %16 offset:512 sc1\n1:\n\t"
#define OB_ALL OBLD(0, 0, 12, 0) OBLD(1, 1, 12, 1024) OBLD(2, 2, 12, 2048) OBLD(3, 3, 12, 3072) OBLT(4, 8, 9, 14) OBLD(5, 4, 13, 0) OBLD(6, 5, 13, 1024) OBLD(7, 6, 13, 2048) OBLD(8, 7, 13, 3072) OBLT(9, 10, 11, 15)
#define OG(c) c(og[0]), c(og[1]), c(og[2]), c(og[3]), c(og[4]), c(og[5]), c(og[6]), c(og[7]), c(ot[0]), c(ot[1]), c(ot[2]), c(ot[3])
#define OBL1(k, a, o) og[k] = ld128(rs, WS_OB + a + o)
#define OBL2(k, a, o) ot[k] = ld64(rs, WS_OB + a + o)
#define OFR(j) ((j) % 5 < 4 ? og[((j) - (j) / 5) & 7] : u32x4{ot[2 * ((j) / 5)][0], ot[2 * ((j) / 5)][1], ot[2 * ((j) / 5) + 1][0], ot[2 * ((j) / 5) + 1][1]})
#pragma unroll
  for (int j = 0; j < 5; ++j) {
    if (j == 0) TPRIO(3); if (j == 2) TPRIO(2); if (j == 3) TPRIO(1); if (j == 4) TPRIO(0);
#pragma unroll
    for (int n = 0; n < 5; ++n) accA[n] = MFMA16(wf[j * 5 + n], of[5 + j], accA[n]);
    SBAR();
#pragma unroll
    for (int n = 0; n < 5; ++n) wf[j * 5 + n] = ldsr128(wl + (j * 5 + n) * 1024);
    if (j == OB0) { OBL1(0, ob0, 0); OBL1(1, ob0, 1024); OBL1(2, ob0, 2048); }
    if (j == OB1) { OBL1(3, ob0, 3072); OBL2(0, ot0, 0); OBL2(1, ot0, 512); }
    if (j == OB2) { OBL1(4, ob1, 0); OBL1(5, ob1, 1024); OBL1(6, ob1, 2048); }
    if (j == OB3) { OBL1(7, ob1, 3072); OBL2(2, ot1, 0); OBL2(3, ot1, 512); }
    SBAR();
  }
  STAMP(2, 0);
  waitv_bar<12>();
  SBAR();
  const lds_cptr wh = (lds_cptr)lds + L_RING + kh * AT_STAGE + lane * 16;
#pragma unroll
  for (int j = 0; j < 10; ++j) {
    if (j % 5 == 0) {
      unsigned seen = 0;
#pragma unroll
      for (int k = j; k < j + 5; ++k) { const u32x4 v = OFR(k); seen = max(max(seen, v[0]), v[1]); seen = max(max(seen, v[2]), v[3]); }
      if (__any(seen == ~0u)) {
#pragma unroll 1
        for (unsigned spins = 0; spins < (1u << 16); ++spins) {
          unsigned pend = 0;
#pragma unroll
          for (int k = j; k < 10; ++k) { const u32x4 v = OFR(k); pend |= __any(max(max(v[0], v[1]), max(v[2], v[3])) == ~0u) ? (1u << k) : 0u; }
          if (!(pend & (0x1fu << j))) break;
          asm volatile(OB_ALL "s_waitcnt vmcnt(0)" : OG("+v") : "v"(ob0), "v"(ob1), "v"(ot0), "v"(ot1), "s"(ws + WS_OB), "s"(pend) : "memory", "scc");
        }
      }
      if (j == 0) STAMP(2, 1);
    }
    if (j == 0) TPRIO(3); if (j == 3) TPRIO(2); if (j == 6) TPRIO(1); if (j == 8) TPRIO(0);
#pragma unroll
    for (int n = 0; n < 5; ++n) accB[n] = MFMA16(wf[(j % 5) * 5 + n], OFR(j), j == 0 ? f32x4{} : accB[n]);
    if (j < 5) {
#pragma unroll
      for (int n = 0; n < 5; ++n) wf[j * 5 + n] = ldsr128(wh + (j * 5 + n) * 1024);
    }
    SBAR();
  }
  STAMP(2, 2);
#pragma unroll
  for (int n = 0; n < 5; ++n) accB[n] = accA[n] * gate + accB[n] * (1.f - gate);
  {
    char* img = lds + L_R + (2 * kh + 1) * WQ + (tq * 16 + (lane & 15)) * 336 + (lane >> 4) * 16;
#pragma unroll
    for (int n = 0; n < 5; ++n) *(f32x4*)(img + n * 64) = accB[n];
    wait_bar<63>();
#pragma unroll
    for (int it = 0; it < 2; ++it) {
      const int u = tid + 512 * it, row = u / 10, pc = u - 10 * row;
      if (u < 640) {
        const char* a = lds + L_R + WQ + row * 336 + pc * 32;
        const f32x4 v0 = *(const f32x4*)a + *(const f32x4*)(a + 2 * WQ), v1 = *(const f32x4*)(a + 16) + *(const f32x4*)(a + 2 * WQ + 16);
        const u32x4 r4 = u32x4{cvtpk(v0[0], v0[1]), cvtpk(v0[2], v0[3]), cvtpk(v1[0], v1[1]), cvtpk(v1[2], v1[3])};
        char* op = out + (size_t)(rt * 64 + row) * (D_ * 2) + ct * 160 + pc * 16;
        if (FIN == 1) *(u32x4*)op = r4; else __builtin_nontemporal_store(r4, (u32x4*)op);
      }
    }
  }
  STAMP(2, 3);
  DBG_OUT();
  asm volatile("GM_CODE_END:");
  if (blockIdx.x == 0 && w == 7) {
#pragma unroll 1
    for (int i = 0; i < 4; ++i) wait_flags(flg, FOA + i * 64 + lane, tag);
    if (lane == 0) __hip_atomic_store(flg, tag, RLX_AGENT);
  }
}

extern "C" void kernel_launch(void* const* d_in, const int* in_sizes, int n_in, void* d_out, int out_size, void* d_ws,
                              size_t ws_size, hipStream_t stream) {
  (void)hipFuncSetAttribute((const void*)fused_kernel, hipFuncAttributeMaxDynamicSharedMemorySize, LDS_BYTES);
  fused_kernel<<<256, 512, LDS_BYTES, stream>>>((const char*)d_in[0], (const char*)d_in[1], (const char*)d_in[4], (const char*)d_in[2],
                                                (const char*)d_in[5], (const char*)d_in[6], (char*)d_ws, (char*)d_out);
}
```

```cpp
#define FULLROT
#define G1_DELAY 16
#include <hip/hip_runtime.h>
#include <hip/hip_bf16.h>
#include <stdint.h>

typedef short bf16x8 __attribute__((ext_vector_type(8)));
typedef short s16x4 __attribute__((ext_vector_type(4)));
typedef __bf16 bfv8 __attribute__((ext_vector_type(8)));
typedef __bf16 bfv2 __attribute__((ext_vector_type(2)));
typedef __bf16 bfv16 __attribute__((ext_vector_type(16)));
typedef float f32x16 __attribute__((ext_vector_type(16)));
typedef float f32x8 __attribute__((ext_vector_type(8)));
typedef float f32x4 __attribute__((ext_vector_type(4)));
typedef unsigned u32x4 __attribute__((ext_vector_type(4)));
typedef unsigned u32x2 __attribute__((ext_vector_type(2)));
typedef unsigned long long u64;
#define LAS __attribute__((address_space(3)))
#define GAS __attribute__((address_space(1)))
#define AINL __attribute__((always_inline))
typedef LAS const char* lds_cptr;
typedef LAS char* lds_ptr;
typedef GAS u64 gu64;

constexpr int D_ = 640, N3_ = 1920;
constexpr u64 TAG_STEP = 0x9E3779B97F4A7C15ull;
constexpr float QSCALE = 0.22360679774997896f * 1.4426950408889634f;
constexpr unsigned OOB = 0x70000000u;

constexpr unsigned UNIT = 2048 * 16;
constexpr unsigned F1A = 512, FG = 768, FOA = 1024, F1B = 2048, NREP = 16;
#define REPSTEP 256
constexpr unsigned WS_QKB = (2048 + NREP * 256) * 128;
constexpr unsigned WS_VB = WS_QKB + 160 * UNIT;
constexpr unsigned BR_A = 160 * UNIT + 32 * 64 * 2048;
constexpr unsigned WS_OA = WS_QKB + 2 * BR_A;
constexpr unsigned WS_OB = WS_OA + 2048 * 640 * 2;
constexpr unsigned WS_WO = WS_OB + 2048 * 640 * 2;
constexpr unsigned WS_G = WS_WO + 2 * 640 * 640 * 2;
constexpr unsigned WS_C = WS_G + 2048 * 4;
constexpr unsigned WS_END = WS_C + 256 * 4096;

constexpr int L_R = 0, WQ = 25600;
constexpr int L_RING = 106496, AT_HALF = 14336, AT_STAGE = 2 * AT_HALF;
constexpr int L_FL = 104448, L_EP = 98304, L_PF = 81920, L_Q = 0;
constexpr int L_LK = 51200, LA_UNIT = 4608, L_LV = L_LK + 3 * LA_UNIT;
constexpr int LDS_BYTES = 163840;

#define STAMP(kid, i) do {} while (0)
#define DBG_DECL
#define DBG_ARG
#define DBG_PASS
#define DBG_OUT() do {} while (0)
#define BST 2
#define OB0 2
#define OB1 3
#define OB2 4
#define OB3 4
#define TPRIO(x) __builtin_amdgcn_s_setprio(x)
#define SNAP 3
#define FIN 1
#define PRO 2
#define TB 0
#define G1_LA 4
#define TLBO -1
#define TBO -1
#define TA -1
#define SBAR() __builtin_amdgcn_sched_barrier(0)
#define PIN(x) asm volatile("" : "+v"(x))
#define MFMA32(a, b, c) __builtin_amdgcn_mfma_f32_32x32x16_bf16(__builtin_bit_cast(bfv8, a), __builtin_bit_cast(bfv8, b), c, 0, 0, 0)
#define MFMA8(a, b, c) __builtin_amdgcn_mfma_f32_32x32x8bf16_1k(a, b, c, 0, 0, 0)
#define MFMA16(a, b, c) __builtin_amdgcn_mfma_f32_16x16x32_bf16(__builtin_bit_cast(bfv8, a), __builtin_bit_cast(bfv8, b), c, 0, 0, 0)
#define RLX_AGENT __ATOMIC_RELAXED, __HIP_MEMORY_SCOPE_AGENT
#define SC1 16

template <int V> struct IC { static constexpr int value = V; };
template <int I, int N, class F> __device__ __forceinline__ void sfor(F&& f) { if constexpr (I < N) { f(IC<I>{}); sfor<I + 1, N>(f); } }
template <int N> __device__ __forceinline__ void wait_bar() { asm volatile("s_waitcnt vmcnt(%0) lgkmcnt(0)\n\ts_barrier" :: "i"(N) : "memory"); }
template <int N> __device__ __forceinline__ void waitv_bar() { asm volatile("s_waitcnt vmcnt(%0)\n\ts_barrier" :: "i"(N) : "memory"); }

__device__ __forceinline__ unsigned cvtpk(float lo, float hi) {
  bfv2 r; r[0] = (__bf16)lo; r[1] = (__bf16)hi;
  return __builtin_bit_cast(unsigned, r);
}
__device__ __forceinline__ float dot2(unsigned a, unsigned b, float c) {
  return __builtin_amdgcn_fdot2_f32_bf16(__builtin_bit_cast(bfv2, a), __builtin_bit_cast(bfv2, b), c, false);
}
template <int OFF> __device__ __forceinline__ void dma(const char* base, unsigned voff, unsigned lds_base) {
  asm volatile("s_add_u32 m0, %2, %3\n\ts_nop 0\n\tglobal_load_lds_dwordx4 %0, %1" :: "v"(voff), "s"(base), "s"(lds_base), "i"(OFF) : "memory", "scc");
}
template <int OFF> __device__ __forceinline__ void dma_sc1(const char* base, unsigned voff, unsigned lds_base) {
  asm volatile("s_add_u32 m0, %2, %3\n\ts_nop 0\n\tglobal_load_lds_dwordx4 %0, %1 sc1" :: "v"(voff), "s"(base), "s"(lds_base), "i"(OFF) : "memory", "scc");
}
template <int OFF> __device__ __forceinline__ void dma2_sc1(const char* base, unsigned voff, unsigned lds_base) {
  asm volatile("s_add_u32 m0, %2, %3\n\ts_nop 0\n\tglobal_load_lds_dwordx4 %0, %1 sc1\n\tglobal_load_lds_dwordx4 %0, %1 offset:1024 sc1"
               :: "v"(voff), "s"(base), "s"(lds_base), "i"(OFF) : "memory", "scc");
}
template <int OFF> __device__ __forceinline__ void dma3_sc1(const char* base, unsigned voff, unsigned lds_base) {
  asm volatile("s_add_u32 m0, %2, %3\n\ts_nop 0\n\tglobal_load_lds_dwordx4 %0, %1 sc1\n\tglobal_load_lds_dwordx4 %0, %1 offset:1024 sc1\n\t"
               "global_load_lds_dwordx4 %0, %1 offset:2048 sc1" :: "v"(voff), "s"(base), "s"(lds_base), "i"(OFF) : "memory", "scc");
}
typedef short v4i16_t __attribute__((ext_vector_type(4)));
__device__ __forceinline__ s16x4 trd(lds_cptr p) {
  return __builtin_bit_cast(s16x4, __builtin_amdgcn_ds_read_tr16_b64_v4i16((LAS v4i16_t*)p));
}
__device__ __forceinline__ bf16x8 cat8(s16x4 a, s16x4 b) { return __builtin_shufflevector(a, b, 0, 1, 2, 3, 4, 5, 6, 7); }
__device__ __forceinline__ bf16x8 ldsr128(lds_cptr p) { return *(const LAS bf16x8*)p; }
__device__ __forceinline__ s16x4 ldsr64(lds_cptr p) { return *(const LAS s16x4*)p; }
__device__ __forceinline__ u32x4 pack8(const f32x16& a, int s, float sc) {
  return u32x4{cvtpk(a[8 * s] * sc, a[8 * s + 1] * sc), cvtpk(a[8 * s + 2] * sc, a[8 * s + 3] * sc),
               cvtpk(a[8 * s + 4] * sc, a[8 * s + 5] * sc), cvtpk(a[8 * s + 6] * sc, a[8 * s + 7] * sc)};
}
__device__ __forceinline__ int swap23(int r) { return (r & 0x13) | ((r & 4) << 1) | ((r & 8) >> 1); }
__device__ __forceinline__ u64 uni64(unsigned lo, unsigned hi) {
  return ((u64)(unsigned)__builtin_amdgcn_readfirstlane(hi) << 32) | (unsigned)__builtin_amdgcn_readfirstlane(lo);
}

typedef __amdgpu_buffer_rsrc_t rsrc_t;
__device__ __forceinline__ void st128(rsrc_t rs, unsigned off, u32x4 v) { __builtin_amdgcn_raw_buffer_store_b128(v, rs, off, 0, SC1); }
__device__ __forceinline__ void st64(rsrc_t rs, unsigned off, u32x2 v) { __builtin_amdgcn_raw_buffer_store_b64(v, rs, off, 0, SC1); }
__device__ __forceinline__ u32x4 ld128(rsrc_t rs, unsigned off) { return __builtin_amdgcn_raw_buffer_load_b128(rs, off, 0, SC1); }
__device__ __forceinline__ u32x2 ld64(rsrc_t rs, unsigned off) { return __builtin_amdgcn_raw_buffer_load_b64(rs, off, 0, SC1); }
__device__ __forceinline__ unsigned ld32(rsrc_t rs, unsigned off) { return __builtin_amdgcn_raw_buffer_load_b32(rs, off, 0, SC1); }

template <int SLEEP = 6, int ROT = 0>
__device__ __forceinline__ void wait_flags(gu64* flg, unsigned idx, u64 tag) {
  bool ok = false;
#pragma unroll 1
  for (unsigned spins = 0; spins < (1u << 18); ++spins) {
    if (!ok) ok = __hip_atomic_load(flg + (idx + min(spins, (unsigned)ROT) * 256) * 16, RLX_AGENT) == tag;
    if (__all(ok)) break;
    __builtin_amdgcn_s_sleep(SLEEP);
  }
  asm volatile("" ::: "memory");
}
__device__ __forceinline__ void sample_flags(u64& fa, u64& fb, unsigned ia, unsigned ib, const char* base, u64 miss) {
  asm volatile("s_cmp_eq_u64 %5, 0\n\ts_cbranch_scc1 1f\n\tglobal_load_dwordx2 %0, %2, %4 sc1\n\tglobal_load_dwordx2 %1, %3, %4 sc1\n1:"
               : "+v"(fa), "+v"(fb) : "v"(ia * 128), "v"(ib * 128), "s"(base), "s"(miss) : "memory", "scc");
}

__device__ __forceinline__ unsigned o_main(int tok, int h, int hh) {
  const int m = h >> 1;
  return (unsigned)((((tok >> 4) * 20 + 5 * (m >> 2) + (m & 3)) * 64 + (2 * (h & 1) + hh) * 16 + (tok & 15)) * 16);
}
__device__ __forceinline__ unsigned o_tail(int tok, int h) {
  const int m = h >> 1;
  return (unsigned)(((tok >> 4) * 20 + 5 * (m >> 2) + 4) * 1024 + 512 * (m & 1) + ((2 * ((m >> 1) & 1) + (h & 1)) * 16 + (tok & 15)) * 8);
}

#define G1_ORDER "BBBBBBBBBBAAAAAAAAAA"
#define G1_PUB 14
#define G1_EP 12
#define G1_SAMP 72
#define G1_LOOK 76
#define G1_AGAIN true
struct G1 {
  int isB[24] = {}, kb[24] = {}, uf[25] = {};
  int lastB = 0;
  int iss_lo[20] = {}, iss_hi[20] = {};
  int wait[20] = {};
  constexpr G1() {
    const char* ord = G1_ORDER;
    int nb = 0, na = 0;
    for (int o = 0; o < 24; ++o) {
      isB[o] = o < 20 && ord[o] == 'B'; kb[o] = o < 20 ? (isB[o] ? nb++ : na++) : 0;
      uf[o + 1] = uf[o] + (o < 20 ? 1 + isB[o] : 0);
      if (isB[o]) lastB = o;
    }
    int pos[60] = {}, cnt = 0, lastst = 0, done = 2 * uf[PRO];
    for (int d = 0; d < done; ++d) pos[d] = cnt++;
    if (TB) cnt += 2;
    for (int o = 0; o < 20; ++o) {
      int need = pos[2 * uf[o + 1] - 1];
      if (o == G1_PUB && lastst > need) need = lastst;
      wait[o] = o >= 18 ? 0 : cnt - 1 - need;
      if (o == G1_EP) lastst = cnt++;
      if (o == TA) cnt++;
      if (o == TBO) cnt += 2;
      if (o == TLBO) cnt += 2;
      iss_lo[o] = done; iss_hi[o] = 2 * uf[o + 4];
      if (G1_LA > 4 && o > 0) iss_hi[o] = 2 * (uf[o - 1] + 10 < 30 ? uf[o - 1] + 10 : 30);
      for (int ks = 0; ks < 4; ++ks) {
        const int per = (iss_hi[o] - iss_lo[o] + 3) / 4;
        for (int z = 0; z < per && done < iss_hi[o]; ++z) pos[done++] = cnt++;
        const int n = 4 * o + ks - 2 - (4 * lastB + 3);
        if (n >= 0 && n < 4 * BST && n % BST == 0) lastst = cnt++;
      }
    }
  }
};
constexpr int g1_slot(int u) { return ((u + 6) % 10) * 16384; }
template <bool VT, class FPiece, class FStart, class FSetup, class FT>
__device__ __forceinline__ void g1_body(char* ws, rsrc_t rs, char* lds, int ti, int cj, int w, int lane, u64& tag, bool& early, unsigned& fiKV, unsigned& fiQ,
                                        u32x4 (&pk)[4], unsigned (&dstB)[2], FPiece&& piece, FStart&& att_start, FSetup&& setup, FT&& touchA DBG_ARG) {
  constexpr G1 P;
  static_assert(P.uf[G1_EP - 1] > 20 && G1_EP < G1_PUB && P.uf[4] <= 9, "epoch / prefetch scratch slots");
  const int tg = w & 3, cg = w >> 2;
  const int r = lane & 31, hh = lane >> 5, g16 = (lane >> 4) & 1, q4 = (lane & 15) >> 2, p4 = lane & 3;
  const int tr = VT ? swap23(r) : r;
  unsigned xoff[4];
#pragma unroll
  for (int ks = 0; ks < 4; ++ks) xoff[ks] = (unsigned)((32 * tg + tr) * 128 + (((2 * ks + hh) ^ ((tr >> 1) & 7)) * 16));
  const int psw = ((p4 & 1) << 1) | (p4 >> 1);
  unsigned woff[2];
#pragma unroll
  for (int t2 = 0; t2 < 2; ++t2) woff[t2] = (unsigned)((8 * hh + q4) * 256 + (((2 * cg + t2) ^ q4) * 64) + 32 * g16 + 8 * (VT ? p4 : psw));
  const float sc = (!VT && cj < 5) ? QSCALE : 1.f;
  bf16x8 xf[40], wf[3][2];
  u64 fa, fb;
  f32x16 accB[2], accA[2];
  u64 miss = 1;
  auto look = [&](bool again) AINL {
    asm volatile("s_cmp_eq_u64 %2, 0\n\ts_cbranch_scc1 1f\n\ts_waitcnt vmcnt(0)\n1:" : "+v"(fa), "+v"(fb) : "s"(miss) : "memory", "scc");
    const u64 m = __ballot(fa != tag || fb != tag);
    if (m == 0 && miss != 0) att_start();
    miss = m;
    if (again) sample_flags(fa, fb, fiKV, fiQ, ws + (F1B + REPSTEP) * 128, miss);
  };
  sfor<0, 82>([&](auto ic) AINL {
    constexpr int nn = decltype(ic)::value, n = nn - 2;
    constexpr int o = nn >> 2, ks = nn & 3;
    if constexpr (nn < 80) {
      if constexpr (ks == 0) {
        if constexpr (o < 19) waitv_bar<P.wait[o]>();
        if constexpr (o == G1_EP) dma_sc1<L_EP>(ws, 0, (unsigned)(uintptr_t)lds + w * 1024);
        if constexpr (o == TA) touchA(IC<0>{});
        if constexpr (o == TBO) touchA(IC<1>{});
        if constexpr (o == TLBO) touchA(IC<2>{});
        if constexpr (o == G1_PUB) { const u32x2 ep = *(const LAS u32x2*)((lds_cptr)lds + L_EP + w * 1024); tag = uni64(ep[0], ep[1]) + TAG_STEP; }
        if constexpr (o == G1_PUB) { if (w == 0 && lane < NREP) __hip_atomic_store((gu64*)ws + (F1B + lane * 256 + cj * 16 + ti) * 16, tag, RLX_AGENT); }
        if constexpr (o % 2 == 0) STAMP(0, 1 + o / 2);
      }
      if constexpr (nn >= 62 && nn < 70 && !(nn & 1)) setup(IC<(nn - 62) / 2>{});
      if constexpr (nn == G1_SAMP) { fa = 0; fb = 0; sample_flags(fa, fb, fiKV, fiQ, ws + F1B * 128, miss); }
      if constexpr (nn == G1_LOOK) look(G1_AGAIN);
      if constexpr (P.isB[o]) xf[4 * P.kb[o] + ks] = ldsr128((lds_cptr)lds + g1_slot(P.uf[o] + 1) + xoff[ks]);
#pragma unroll
      for (int t2 = 0; t2 < 2; ++t2) {
        const lds_cptr wp = (lds_cptr)lds + g1_slot(P.uf[o]) + woff[t2] + ks * 4096;
        wf[nn % 3][t2] = cat8(trd(wp), trd(wp + 1024));
      }
      constexpr int per = (P.iss_hi[o] - P.iss_lo[o] + 3) / 4, d0 = P.iss_lo[o] + per * ks;
      sfor<d0, (d0 + per < P.iss_hi[o] ? d0 + per : P.iss_hi[o])>(piece);
    }
    if constexpr (n >= 0) {
      constexpr int om = n >> 2, xi = 4 * P.kb[om] + (n & 3);
#pragma unroll
      for (int t2 = 0; t2 < 2; ++t2) {
        f32x16& ac = P.isB[om] ? accB[t2] : accA[t2];
        const f32x16 cin = xi == 0 ? f32x16{} : ac;
        ac = VT ? MFMA32(xf[xi], wf[n % 3][t2], cin) : MFMA32(wf[n % 3][t2], xf[xi], cin);
      }
    }
    SBAR();
    constexpr int nb = n - (4 * P.lastB + 3);
    if constexpr (nb == 0) {
#pragma unroll
      for (int i = 0; i < 4; ++i) { pk[i] = VT ? pack8(accB[i >> 1], i & 1, 1.f) : pack8(accB[i >> 1], i & 1, sc); PIN(pk[i]); }
      int ln = lane; PIN(ln);
#pragma unroll
      for (int t2 = 0; t2 < 2; ++t2) {
        if (VT) {
          const int F = (cj - 10) * 128 + 64 * cg + 32 * t2 + (ln & 31), hd = F / 20;
          int d = F - 20 * hd - 4 * (hd & 1); if (d < 0) d += 20;
          dstB[t2] = WS_VB + (unsigned)((hd * 64 + 4 * ti + tg) * 2048 + (ln >> 5) * 512 + d * 16);
        } else {
          dstB[t2] = WS_QKB + (unsigned)(cj * 16 + 8 * cg + 4 * t2 + (ln >> 5)) * UNIT + (unsigned)(128 * ti + 32 * tg + (ln & 31)) * 16;
        }
      }
    }
    if constexpr (nb >= 0 && nb < 4 * BST && nb % BST == 0) {
      constexpr int i = nb / BST;
      st128(rs, dstB[i >> 1] + (i & 1) * (VT ? 1024 : 2 * UNIT), pk[i]);
      SBAR();
    }
  });
  STAMP(0, 13);
  look(false);
  early = miss == 0;
#pragma unroll
  for (int i = 0; i < 4; ++i) pk[i] = pack8(accA[i >> 1], i & 1, 1.f);
}

__device__ __forceinline__ f32x16 smf(bf16x8 a, const f32x16& p, int s, f32x16 o, int idx) {
  f32x8 b = s ? __builtin_shufflevector(p, p, 8, 9, 10, 11, 12, 13, 14, 15) : __builtin_shufflevector(p, p, 0, 1, 2, 3, 4, 5, 6, 7);
  return __builtin_amdgcn_smfmac_f32_32x32x32_bf16(__builtin_bit_cast(bfv8, a), __builtin_bit_cast(bfv16, b), o, idx, 0, 0);
}
__device__ __forceinline__ float max16(const f32x16& s) {
  float a = fmaxf(fmaxf(s[0], s[1]), s[2]), b = fmaxf(fmaxf(s[3], s[4]), s[5]);
  a = fmaxf(fmaxf(a, s[6]), s[7]); b = fmaxf(fmaxf(b, s[8]), s[9]);
  a = fmaxf(fmaxf(a, s[10]), s[11]); b = fmaxf(fmaxf(b, s[12]), s[13]);
  a = fmaxf(fmaxf(a, s[14]), s[15]);
  return fmaxf(a, b);
}
__device__ __forceinline__ float swap_max(float m) {
  auto rr = __builtin_amdgcn_permlane32_swap(__float_as_uint(m), __float_as_uint(m), false, false);
  return fmaxf(__uint_as_float(rr[0]), __uint_as_float(rr[1]));
}
__device__ __forceinline__ float swap_add(float m) {
  auto rr = __builtin_amdgcn_permlane32_swap(__float_as_uint(m), __float_as_uint(m), false, false);
  return __uint_as_float(rr[0]) + __uint_as_float(rr[1]);
}
__device__ __forceinline__ void set_ref(s16x4& q1, float m, bool spare) {
  const unsigned a = __float_as_uint(-m) & 0xffff0000u;
  const float r1 = -m - __uint_as_float(a);
  const unsigned b = __float_as_uint(r1) & 0xffff0000u;
  const unsigned c = __float_as_uint(r1 - __uint_as_float(b));
  if (spare) q1 = __builtin_bit_cast(s16x4, u32x2{(a >> 16) | b, c >> 16});
}

__global__ __launch_bounds__(512, 2) void fused_kernel(const char* __restrict__ x, const char* __restrict__ wA, const char* __restrict__ wB,
                                                       const char* __restrict__ woA, const char* __restrict__ woB,
                                                       const char* __restrict__ gw, char* ws, char* __restrict__ out) {
  extern __shared__ __attribute__((aligned(16))) char lds[];
  const int tid = threadIdx.x, lane = tid & 63;
  const int w = __builtin_amdgcn_readfirstlane(tid >> 6);
  const int xcd = blockIdx.x & 7, slot = blockIdx.x >> 3;
  const unsigned lds0 = (unsigned)(uintptr_t)lds;
  gu64* flg = (gu64*)ws;
  const rsrc_t rs = __builtin_amdgcn_make_buffer_rsrc(ws, 0, WS_END, 0x00020000);
  DBG_DECL
  STAMP(0, 0);
  const int ti = 8 * (xcd & 1) + (slot & 7);
  const int cj = 4 * (xcd >> 1) + (slot >> 3);
  const unsigned ldpf = lds0 + (cj < 15 ? L_PF : L_RING);
  if (w == 6) {
    u64 pc; unsigned len;
    asm volatile("s_getpc_b64 %0\nGM_CODE_BEG:\n\ts_mov_b32 %1, GM_CODE_END-GM_CODE_BEG" : "=s"(pc), "=s"(len));
    asm volatile("s_add_u32 m0, %2, %3\n\ts_nop 0\n\tglobal_load_lds_dword %0, %1"
                 :: "v"(min((unsigned)((slot & 7) * 64 + lane) * 128, len)), "s"(pc), "s"(ldpf), "i"(0) : "memory", "scc");
  }
  const int rot = ((slot & 7) * 5 / 4 + (slot >> 3) * 5 / 2 + xcd) % 10;
  unsigned xlo[2], wlo[2];
#pragma unroll
  for (int i = 0; i < 2; ++i) {
    const int row = 16 * w + 8 * i + (lane >> 3);
    xlo[i] = (unsigned)(row * (D_ * 2) + (((lane & 7) ^ ((row >> 1) & 7)) * 16));
    wlo[i] = (unsigned)((8 * w + 4 * i + (lane >> 4)) * (N3_ * 2) + (((lane & 15) ^ (4 * (lane >> 4))) * 16));
  }
  const char* xb = x + (size_t)(128 * ti) * (D_ * 2);
  const char* wBb = wB + cj * 256;
  const char* wAb = wA + cj * 256;
  const unsigned ldw = lds0 + w * 2048;
  auto piece = [&](auto dc) AINL {
    constexpr G1 P;
    constexpr int d = decltype(dc)::value, u = d >> 1, base = g1_slot(u) + (d & 1) * 1024;
    constexpr int o = [&]() { int q = 0; while (P.uf[q + 1] <= u) ++q; return q; }();
    int kb = P.kb[o] + rot; if (kb >= 10) kb -= 10;
    if constexpr (u != P.uf[o]) dma<base>(xb + kb * 128, xlo[d & 1], ldw);
    else dma<base>((P.isB[o] ? wBb : wAb) + (size_t)kb * (64 * N3_ * 2), wlo[d & 1], ldw);
  };
  auto touch = [&](const char* base, unsigned voff, unsigned dst) AINL { asm volatile("s_mov_b32 m0, %2\n\ts_nop 0\n\tglobal_load_lds_dword %0, %1" :: "v"(voff), "s"(base), "s"(dst) : "memory"); };
  auto touchA = [&](auto bc) AINL { constexpr G1 P;
    if constexpr (decltype(bc)::value == 2) {
      const unsigned dst = ldw + g1_slot(P.iss_lo[TLBO < 0 ? 0 : TLBO] / 2), n = (slot * 8 + w) * 2 + (lane & 1);
      touch(ws, min(n * 65536u, WS_END - 4), dst);
      touch(out, min((n & 63) * 65536u, 2621436u), dst);
    } else if constexpr (decltype(bc)::value) {
      constexpr int K0 = TBO + 4; const unsigned dst = ldw + g1_slot(P.iss_lo[TBO < 0 ? 0 : TBO] / 2);
      int kx = K0 + rot + min(lane & 7, 9 - K0); kx -= kx >= 10 ? 10 : 0;
      touch(xb, (unsigned)(32 * (cj & 3) + 4 * w + ((lane >> 3) & 3)) * (D_ * 2) + kx * 128, dst);
      int kw = K0 + rot + min(lane >> 1, 9 - K0); kw -= kw >= 10 ? 10 : 0;
      touch(wBb, (unsigned)(64 * kw + 8 * (ti & 7) + w) * (N3_ * 2) + (lane & 1) * 128, dst);
    } else touch(wAb, (unsigned)(80 * (ti & 7) + 10 * w + min(lane >> 1, 9)) * (N3_ * 2) + (lane & 1) * 128, ldw + g1_slot(P.iss_lo[TA < 0 ? 0 : TA] / 2)); };
  if (cj < 15) {
    sfor<0, 4>(piece);
    __builtin_amdgcn_s_sleep(G1_DELAY);
    sfor<4, 4 * PRO>(piece);
    if (TB) {
      int kx = PRO + rot + min(lane & 7, 9 - PRO); kx -= kx >= 10 ? 10 : 0;
      touch(xb, (unsigned)(32 * (cj & 3) + 4 * w + ((lane >> 3) & 3)) * (D_ * 2) + kx * 128, ldw + g1_slot(8));
      int kw = PRO + rot + min(lane >> 1, 9 - PRO); kw -= kw >= 10 ? 10 : 0;
      touch(wBb, (unsigned)(64 * kw + 8 * (ti & 7) + w) * (N3_ * 2) + (lane & 1) * 128, ldw + g1_slot(8));
    }
  }
  STAMP(0, 14);
  SBAR();
  u64 tag;
  const int h = 4 * xcd + (slot & 3), c = slot >> 2;
  const int e = h & 1, ua = 5 * (h >> 1) + 3 * e, us = 5 * (h >> 1) + 2;
  const int jlo = (5 * (h >> 1) + 2 * e) >> 4, jhi = (5 * (h >> 1) + 2 * e + 2) >> 4;
  const int kh = w >> 2, tq = w & 3;
  const char* kbase = ws + WS_QKB + (size_t)(80 + (tq >= 2 ? us : ua + tq)) * UNIT + 1024 * kh * 16;
  if (w == 5) { const unsigned cv = (lane & 1) ? 0u : 0x3f803f80u; st128(rs, lane < 8 ? WS_C + blockIdx.x * 4096 + (lane >> 1) * 1024 + (lane & 1) * 16 : OOB, u32x4{cv, cv, cv, cv}); }
  const unsigned ldr = lds0 + kh * AT_HALF + tq * 2048;
  unsigned vlo, vstr;
  auto kpiece = [&](auto stc) AINL {
    constexpr int st = decltype(stc)::value;
    if (tq < 3) dma2_sc1<L_RING + (st & 1) * AT_STAGE>(kbase + st * 2048, lane * 16, ldr);
  };
  auto vpiece = [&](auto stc) AINL {
    constexpr int st = decltype(stc)::value;
    dma2_sc1<L_RING + (st & 1) * AT_STAGE + 6144>(ws, vlo + st * vstr, ldr);
  };
  unsigned fiKV, fiQ;
  auto att_start = [&]() AINL {
    kpiece(IC<0>{});
    if (tq == 3) {
      const char* qb = ws + WS_QKB + (256 * c + 128 * kh) * 16;
      dma2_sc1<L_Q>(qb + (size_t)ua * UNIT, lane * 16, lds0 + kh * 2048);
      dma2_sc1<L_Q + 4096>(qb + (size_t)(ua + 1) * UNIT, lane * 16, lds0 + kh * 2048);
      dma2_sc1<L_Q + 8192>(qb + (size_t)us * UNIT, lane * 16, lds0 + kh * 2048);
    }
    vpiece(IC<0>{});
  };

  const int hh = lane >> 5, r = lane & 31;
  const int rt = 4 * xcd + (slot & 3), ct = slot >> 2;
  const int SWP = ct & 1;
  const char* wob = ws + WS_WO + (size_t)ct * 102400 + w * 3072;
  auto woquarter = [&](auto brc, auto qc, auto dc) AINL {
    constexpr int br = decltype(brc)::value, q = decltype(qc)::value, d = decltype(dc)::value;
    const int off = br * 819200 + (q ^ (br & SWP)) * WQ;
    dma3_sc1<d>(wob + off, lane * 16, lds0 + w * 3072);
    if (w == 0) dma_sc1<d + 24576>(wob + off + 24576, lane * 16, lds0);
  };
  const unsigned ladst = lds0 + (w < 3 ? L_LK + w * LA_UNIT : L_LV + min(4 * (w - 3), 13) * 1024);
  unsigned osrc, fiA, obm, obt, lalo; int idx;
  lds_cptr kp0, kp1, vp, qp;
  f32x16 oa, ob;
  auto setup = [&](auto pc) AINL {
    constexpr int p = decltype(pc)::value;
    int ln = lane; PIN(ln);
    const int hh = ln >> 5, r = ln & 31;
    if constexpr (p == 0) {
      const unsigned crow = WS_C + blockIdx.x * 4096 + (r == 20 ? 0 : 16);
      vlo = r < 20 ? WS_VB + (h * 64 + 32 * kh + tq) * 2048 + ln * 16 : crow; vstr = r < 20 ? 8192u : 0u;
      const unsigned lab = w < 3 ? WS_QKB + BR_A + (80 + (w == 2 ? us : ua + w)) * UNIT + (256 * c - 16) * 16
                                 : WS_VB + BR_A + (h * 64 + 8 * c) * 2048 - 1024 + min(4 * (w - 3), 13) * 1024;
      lalo = (w < 3 || r < 20) ? lab + ln * 16 : crow;
      osrc = (unsigned)((rt * 4 + tq) * 20 + 10 * kh) * 1024 + ln * 16;
      PIN(vlo); PIN(vstr); PIN(lalo); PIN(osrc);
    }
    if constexpr (p == 1) {
      fiA = ln < 18 ? F1A + (5 * (ln / 6) + (((ln / 3) & 1) ? jhi : jlo)) * 16 + max(2 * c - 1 + ln % 3, 0) : FG + (ln == 18 ? ct : 8 + ct);
      obm = WS_OB + o_main(256 * c + 64 * tq + 32 * kh + r, h, hh); obt = (hh ? OOB : 0u) + WS_OB + o_tail(256 * c + 64 * tq + 32 * kh + r, h);
      PIN(fiA); PIN(obm); PIN(obt);
    }
    if constexpr (p == 2) {
      const lds_cptr ring = (lds_cptr)lds + L_RING + kh * AT_HALF;
      kp0 = ring + hh * 2048 + r * 16;
      kp1 = (hh == e) ? ring + 2 * 2048 + r * 16 + 8 * hh : ring + 6144 + 20 * 16;
      vp = ring + 6144 + hh * 512 + r * 16;
      qp = (lds_cptr)lds + L_Q + (64 * tq + r) * 16;
      idx = 0xDDDD;
      PIN(kp0); PIN(kp1); PIN(vp); PIN(qp); PIN(idx);
    }
    if constexpr (p == 3) {
      oa = f32x16{}; ob = f32x16{}; PIN(oa); PIN(ob);
      fiKV = (5 + 5 * hh + ((ln & 16) ? jhi : jlo)) * 16 + (ln & 15); fiQ = ((ln & 1) ? jhi : jlo) * 16 + 2 * c + ((ln >> 1) & 1);
      PIN(fiKV); PIN(fiQ);
    }
  };

  bool early = false;
  u32x4 pk[4]; unsigned dstA[2];
  if (cj < 10) g1_body<false>(ws, rs, lds, ti, cj, w, lane, tag, early, fiKV, fiQ, pk, dstA, piece, att_start, setup, touchA DBG_PASS);
  else if (cj < 15) g1_body<true>(ws, rs, lds, ti, cj, w, lane, tag, early, fiKV, fiQ, pk, dstA, piece, att_start, setup, touchA DBG_PASS);
  else {
    const char* wo = ((ti & 8) ? woB : woA) + (size_t)(80 * w) * (D_ * 2) + (ti & 7) * 160;
#pragma unroll 1
    for (int p = 0; p < 13; ++p) {
      const int s0 = min(64 * p, 736), sl = s0 + lane;
      dma<0>(wo, (unsigned)((sl / 10) * (D_ * 2) + (sl % 10) * 16), lds0 + w * 12800 + s0 * 16);
    }
    const u64 ep = __hip_atomic_load(flg, RLX_AGENT);
    tag = uni64((unsigned)ep, (unsigned)(ep >> 32)) + TAG_STEP;
    wait_bar<0>();
    const int g4 = lane >> 4, rl = 320 * kh + 4 * (g4 & 1) + ((lane & 15) >> 2);
#pragma unroll
    for (int ff = 0; ff < 13; ++ff) {
      const int f = min(tq + 4 * ff, 49), j = f / 5, n = f - 5 * j;
      const bool mn = j % 5 < 4;
      const int row = rl + 160 * (j / 5) + (mn ? 40 * (j % 5) + 24 * (g4 >> 1) : 80 * (g4 >> 1) + 16);
      const lds_cptr p = (lds_cptr)lds + row * 160 + n * 32 + 8 * (lane & 3);
      st128(rs, WS_WO + (unsigned)(ti * 100 + kh * 50 + f) * 1024 + lane * 16, __builtin_bit_cast(u32x4, cat8(trd(p), trd(p + (mn ? 8 : 40) * 160))));
    }
    wait_bar<0>();
    if (tid == 0) __hip_atomic_store(flg + (FG + ti) * 16, tag, RLX_AGENT);
    if (w == 0) { wait_flags<16>(flg, F1B + (NREP - 1) * 256 + jlo * 16 + 2 * c, tag); }
    wait_bar<63>();
    sfor<0, 4>(setup);
    dstA[0] = OOB; dstA[1] = OOB;
#pragma unroll
    for (int i = 0; i < 4; ++i) pk[i] = u32x4{0u, 0u, 0u, 0u};
  }
  const unsigned flag1 = F1A + cj * 16 + ti;

  STAMP(1, 0);
  if (!early) {
    bool oka = false, okb = false;
#pragma unroll 1
    for (unsigned spins = 0; spins < (1u << 18); ++spins) {
      const unsigned rp = F1B + (2 + min(spins, NREP - 4)) * REPSTEP;
      const u64 a = __hip_atomic_load(flg + (rp + (oka ? fiQ : fiKV)) * 16, RLX_AGENT), b = __hip_atomic_load(flg + (rp + fiQ) * 16, RLX_AGENT);
      oka = oka || a == tag; okb = okb || b == tag;
      if (__all(oka && okb)) break;
      __builtin_amdgcn_s_sleep(2);
    }
    asm volatile("" ::: "memory");
    att_start();
  }
  STAMP(1, 1);
  wait_bar<0>();
  STAMP(1, 2);
  float gate;
  const unsigned fiOA = FOA + (rt >> 2) * 32 + r;
  u32x4 lq; u32x2 lqt;
  f32x16 sa, sb;
  bf16x8 q0[2]; s16x4 q1[2];
#pragma unroll
  for (int t = 0; t < 2; ++t) {
    q0[t] = ldsr128(qp + hh * 4096 + ((t ^ kh ^ 1) * 512));
    q1[t] = ldsr64(qp + 8192 + 8 * e + ((t ^ kh ^ 1) * 512));
  }
  const bf16x8 kr0 = ldsr128(kp0 - kh * AT_HALF); const s16x4 kr1 = ldsr64(kp1 - kh * AT_HALF);
  bf16x8 k0 = ldsr128(kp0); s16x4 k1 = ldsr64(kp1);
  bf16x8 v0 = ldsr128(vp), v1 = ldsr128(vp + 1024);
  bf16x8 v1p = v1;
  SBAR();
  {
    set_ref(q1[0], 0.f, hh != e); set_ref(q1[1], 0.f, hh != e);
    f32x16 s = MFMA32(kr0, q0[0], f32x16{}), t = MFMA32(kr0, q0[1], f32x16{});
    s = MFMA8(kr1, q1[0], s); t = MFMA8(kr1, q1[1], t);
    sa = MFMA32(k0, q0[0], f32x16{});
    SBAR();
    kpiece(IC<1>{}); vpiece(IC<1>{});
    SBAR();
    set_ref(q1[0], swap_max(max16(s)), hh != e);
    sa = MFMA8(k1, q1[0], sa);
    SBAR();
    set_ref(q1[1], swap_max(max16(t)), hh != e);
  }
  STAMP(1, 3);
  u32x4 of[10];
  unsigned gx[5], gg[5]; float gs;

  auto local_attention = [&]() AINL {
    const int tok = 256 * c + 32 * w + r;
    const lds_cptr lk = (lds_cptr)lds + L_LK + (32 * w + r) * 16, lv = (lds_cptr)lds + L_LV + (4 * w + hh) * 512 + r * 16;
    const bf16x8 qa = __builtin_bit_cast(bf16x8, lq);
    const s16x4 qb = __builtin_bit_cast(s16x4, (hh == e) ? lqt : u32x2{0u, 0u});
    f32x16 sx = MFMA32(ldsr128(lk + hh * LA_UNIT), qa, f32x16{}), sy = MFMA32(ldsr128(lk + hh * LA_UNIT + 512), qa, f32x16{});
    sx = MFMA8(ldsr64(lk + 2 * LA_UNIT + 8 * e), qb, sx); sy = MFMA8(ldsr64(lk + 2 * LA_UNIT + 8 * e + 512), qb, sy);
    const bf16x8 va = ldsr128(lv), vb = ldsr128(lv + 1024), vc = ldsr128(lv + 2048);
    const u64 edge = (c == 0 && w == 0) ? 0ull : ~0ull;
    const float ninf = -INFINITY;
    sfor<0, 24>([&](auto jc) AINL {
      constexpr int j = decltype(jc)::value;
      constexpr u64 mk = []() { u64 m = 0; for (int l = 0; l < 64; ++l) { const int d = (l & 31) + 16 - ((j & 3) + 8 * (j >> 2) + 4 * (l >> 5)); if (d >= 0 && d <= 14 && !(d & 1)) m |= 1ull << l; } return m; }();
      const bool ok = __builtin_amdgcn_inverse_ballot_w64(j < 8 ? mk & edge : mk);
      if constexpr (j < 16) sx[j] = ok ? sx[j] : ninf; else sy[j - 16] = ok ? sy[j - 16] : ninf;
    });
    float m = fmaxf(fmaxf(max16(sx), sy[0]), sy[1]);
    m = fmaxf(fmaxf(m, sy[2]), sy[3]); m = fmaxf(fmaxf(m, sy[4]), sy[5]); m = -QSCALE * swap_max(fmaxf(fmaxf(m, sy[6]), sy[7]));
#pragma unroll
    for (int j = 0; j < 16; ++j) sx[j] = __builtin_amdgcn_exp2f(__builtin_fmaf(sx[j], QSCALE, m));
#pragma unroll
    for (int j = 0; j < 8; ++j) sy[j] = __builtin_amdgcn_exp2f(__builtin_fmaf(sy[j], QSCALE, m));
    f32x16 o = smf(va, sx, 0, f32x16{}, idx); o = smf(vb, sx, 1, o, idx); o = smf(vc, sy, 0, o, idx);
    auto lr = __builtin_amdgcn_permlane32_swap(__float_as_uint(o[8]), __float_as_uint(o[8]), false, false);
    const float rl = __builtin_amdgcn_rcpf(__uint_as_float(lr[1]));
    st128(rs, WS_OA + o_main(tok, h, hh), pack8(o, 0, rl));
    st64(rs, hh ? OOB : WS_OA + o_tail(tok, h), u32x2{cvtpk(o[8] * rl, o[9] * rl), cvtpk(o[10] * rl, o[11] * rl)});
    gs += __builtin_bit_cast(float, __builtin_amdgcn_update_dpp(0, __builtin_bit_cast(int, gs), 0xB1, 0xf, 0xf, true));
    gs += __builtin_bit_cast(float, __builtin_amdgcn_update_dpp(0, __builtin_bit_cast(int, gs), 0x4E, 0xf, 0xf, true));
    gs += __builtin_bit_cast(float, __builtin_amdgcn_update_dpp(0, __builtin_bit_cast(int, gs), 0x141, 0xf, 0xf, true));
    gs += __builtin_bit_cast(float, __builtin_amdgcn_update_dpp(0, __builtin_bit_cast(int, gs), 0x140, 0xf, 0xf, true));
    { auto g2 = __builtin_amdgcn_permlane16_swap(__float_as_uint(gs), __float_as_uint(gs), false, false); gs = __uint_as_float(g2[0]) + __uint_as_float(g2[1]); }
    gs = swap_add(gs);
    const float gv = __builtin_amdgcn_rcpf(1.f + __builtin_amdgcn_exp2f(-1.4426950408889634f * gs));
    __builtin_amdgcn_raw_buffer_store_b32(__float_as_uint(gv), rs, lane == 0 ? WS_G + (256 * c + 8 * h + w) * 4 : OOB, 0, SC1);
    SBAR();
  };

  const lds_cptr wl = (lds_cptr)lds + L_R + kh * (2 * WQ) + lane * 16;
  f32x4 accA[5];
  bf16x8 wq[2][5], wf[25];
  auto opa = [&](auto hc, auto gc) AINL {
    constexpr int hs = decltype(hc)::value, g = decltype(gc)::value;
    if constexpr (hs >= 0 && hs <= 5) SBAR();
    if constexpr (hs >= 1 && hs <= 5) accA[g] = MFMA16(wq[(hs - 1) & 1][g], of[hs - 1], hs == 1 ? f32x4{} : accA[g]);
    if constexpr (hs >= 0 && hs < 5) wq[hs & 1][g] = ldsr128(wl + (hs * 5 + g) * 1024);
    if constexpr (hs >= 0 && hs <= 5) SBAR();
  };
#define EX(S, j) S[j] = __builtin_amdgcn_exp2f(S[j])
#define EX2(S, j) do { EX(S, j); EX(S, j + 1); } while (0)
#define EX4(S, j) do { EX(S, j); EX(S, j + 1); EX(S, j + 2); EX(S, j + 3); } while (0)
  sfor<0, 8>([&](auto stc) AINL {
    constexpr int st = decltype(stc)::value;
    constexpr int cur = (st & 1) * AT_STAGE, nxt = ((st + 1) & 1) * AT_STAGE;
    sfor<0, 4>([&](auto ic) AINL {
      constexpr int i = decltype(ic)::value;
      constexpr int nk = (i < 3) ? cur + (i + 1) * 512 : nxt, nv = (i < 3) ? cur + (i + 1) * 2048 : nxt;
      constexpr int sl = (i + 1) & 3, sst = (i == 3) ? st + 1 : st;
      constexpr int ha = st == 6 ? 2 * i : -9, hb = st == 6 ? 2 * i + 1 : -9;
      SBAR();
      EX2(sa, 0); PIN(sa); SBAR();
      if constexpr (st + i > 0) ob = smf(v1p, sb, 1, ob, idx);
      SBAR(); EX2(sa, 2); opa(IC<ha>{}, IC<0>{}); EX2(sa, 4); PIN(sa); SBAR();
      sb = MFMA32(k0, q0[1], f32x16{}); SBAR(); EX2(sa, 6); opa(IC<ha>{}, IC<1>{}); EX2(sa, 8); PIN(sa); SBAR();
      sb = MFMA8(k1, q1[1], sb); SBAR();
      if constexpr (i == 3) {
        wait_bar<(st == 4 && SNAP == 2) ? 3 : 0>();
        if constexpr (st == 0) { if (tid == 0) __hip_atomic_store(flg + flag1 * 16, tag, RLX_AGENT); }
        if constexpr (st == 2) {
          PIN(lq); PIN(lqt);
        }
        if constexpr (st == 1) {
          gs = 0.f;
#pragma unroll
          for (int i = 0; i < 5; ++i) gs = dot2(gx[i], gg[i], gs);
          PIN(gs);
        }
        if constexpr (st == 3) { if (tid == 0) __hip_atomic_store(flg + (FOA + c * 32 + h) * 16, tag, RLX_AGENT); }
        if constexpr (st == 5 || st == 7) {
#pragma unroll
          for (int j = 0; j < 5; ++j) PIN(of[(st == 7 ? 5 : 0) + j]);
          if constexpr (st == 7) PIN(gate);
        }
        if constexpr (st == 1 || st == 4) {
          if (!__all(*(const LAS u64*)((lds_cptr)lds + L_FL + lane * 16) == tag)) wait_flags(flg, st == 1 ? fiA : fiOA, tag);
        }
      }
      __builtin_amdgcn_s_setprio(3 - sl);
      k0 = ldsr128(kp0 + nk); k1 = ldsr64(kp1 + nk);
      if constexpr (sst >= 1 && sst + 1 < 8 && sl == 0) kpiece(IC<sst + 1>{});
      if constexpr (sst == 0 && sl < 3) st128(rs, dstA[sl - 1] + BR_A, pk[2 * sl - 2]);
      if constexpr ((sst == 1 || sst == 4) && sl == SNAP) { if (w == 7) dma_sc1<L_FL>(ws, (sst == 1 ? fiA : fiOA) * 128, lds0); }
      if constexpr (sst == 2 && sl == 0) dma_sc1<0>(ws, lalo + ((c == 0 && (w == 3 || (w < 3 && lane < 16))) ? 1024 : 0), ladst);
      if constexpr (sst == 2 && sl == 1) { dma3_sc1<1024>(ws + 1024, lalo, ladst); if (w < 3) dma_sc1<3328>(ws + 3328, lalo, ladst); }
      if constexpr ((sst == 4 || sst == 5) && (sl == 1 || sl == 2)) woquarter(IC<0>{}, IC<2 * sl - 2 + sst - 4>{}, IC<L_R + (2 * sl - 2 + sst - 4) * WQ>{});
      if constexpr (sst == 7 && sl == 0) woquarter(IC<1>{}, IC<1>{}, IC<L_RING>{});
      if constexpr (sst == 8) woquarter(IC<1>{}, IC<3>{}, IC<L_RING + AT_STAGE>{});
      if constexpr (sst == 7 && sl > 0) { wf[4 * sl - 4] = ldsr128(wl + WQ + (4 * sl - 4) * 1024); wf[4 * sl - 3] = ldsr128(wl + WQ + (4 * sl - 3) * 1024); }
      if constexpr (sst == 7 && (sl == 1 || sl == 2)) woquarter(IC<1>{}, IC<2 * sl - 2>{}, IC<L_R + (2 * sl - 2) * WQ>{});
      EX2(sa, 10); opa(IC<ha>{}, IC<2>{}); EX2(sa, 12); PIN(sa); SBAR();
      __builtin_amdgcn_s_waitcnt(0xC07F); SBAR();
      oa = smf(v0, sa, 0, oa, idx); SBAR(); EX2(sa, 14); opa(IC<ha>{}, IC<3>{}); PIN(sa); SBAR();
      EX2(sb, 0); opa(IC<ha>{}, IC<4>{}); PIN(sb); SBAR();
      oa = smf(v1, sa, 1, oa, idx); SBAR(); EX2(sb, 2); opa(IC<hb>{}, IC<0>{}); EX2(sb, 4); PIN(sb); SBAR();
      sa = MFMA32(k0, q0[0], f32x16{}); SBAR(); EX2(sb, 6); opa(IC<hb>{}, IC<1>{}); EX2(sb, 8); PIN(sb); SBAR();
      sa = MFMA8(k1, q1[0], sa); SBAR();
      if constexpr (sst >= 1 && sst + 1 < 8 && sl == 0) vpiece(IC<sst + 1>{});
      if constexpr (sst == 0 && sl < 3) st128(rs, dstA[sl - 1] + (BR_A + (cj >= 10 ? 1024 : 2 * UNIT)), pk[2 * sl - 1]);
      if constexpr (sst == 1 && sl == 1) {
        st128(rs, obm, u32x4{~0u, ~0u, ~0u, ~0u}); st64(rs, obt, u32x2{~0u, ~0u});
      }
      if constexpr (sst == 2 && sl == 0) {
        const unsigned to = WS_QKB + BR_A + (unsigned)(256 * c + 32 * w + r) * 16;
        lq = ld128(rs, (ua + hh) * UNIT + to);
        lqt = ld64(rs, us * UNIT + to + 8 * e);
      }
      if constexpr (sst == 1 && sl == 0) {
#pragma unroll
        for (int i = 0; i < 5; ++i) { gx[i] = ((const unsigned*)(x + (size_t)(256 * c + 8 * h + w) * (D_ * 2)))[lane * 5 + i]; gg[i] = ((const unsigned*)gw)[lane * 5 + i]; }
      }
      if constexpr ((sst == 5 || sst == 7) && (sl == 1 || sl == 2)) {
#pragma unroll
        for (int j = (sst == 7 ? 5 : 0) + 3 * sl - 3; j < (sst == 7 ? 5 : 0) + (sl == 1 ? 3 : 5); ++j) {
          if (j % 5 < 4) of[j] = ld128(rs, WS_OA + osrc + j * 1024);
          else { const u32x2 a = ld64(rs, WS_OA + osrc - lane * 8 + j * 1024), b = ld64(rs, WS_OA + osrc - lane * 8 + j * 1024 + 512); of[j] = u32x4{a[0], a[1], b[0], b[1]}; }
        }
        if constexpr (sst == 7 && sl == 2) gate = __uint_as_float(ld32(rs, WS_G + (rt * 64 + tq * 16 + (lane & 15)) * 4));
      }
      if constexpr (sst == 7 && sl > 0) { wf[4 * sl - 2] = ldsr128(wl + WQ + (4 * sl - 2) * 1024); wf[4 * sl - 1] = ldsr128(wl + WQ + (4 * sl - 1) * 1024); }
      v1p = v1;
      const bf16x8 v0n = ldsr128(vp + nv); v1 = ldsr128(vp + nv + 1024);
      EX2(sb, 10); opa(IC<hb>{}, IC<2>{}); EX2(sb, 12); PIN(sb); SBAR();
      ob = smf(v0, sb, 0, ob, idx); SBAR();
      v0 = v0n;
      EX(sb, 14); opa(IC<hb>{}, IC<3>{}); EX(sb, 15); opa(IC<hb>{}, IC<4>{}); PIN(sb); SBAR();
      if constexpr (st == 2 && i == 3) local_attention();
    });
    STAMP(1, 4 + st);
  });
  ob = smf(v1p, sb, 1, ob, idx);
  __builtin_amdgcn_s_setprio(0);
  float ofin[12];
  {
    f32x4* xw = (f32x4*)(lds + L_R + (2 * kh + 1) * WQ + tq * 3072) + lane;
    const f32x4* xr = (const f32x4*)(lds + L_R + (3 - 2 * kh) * WQ + tq * 3072) + lane;
#pragma unroll
    for (int i = 0; i < 3; ++i) xw[i * 64] = f32x4{oa[4 * i], oa[4 * i + 1], oa[4 * i + 2], oa[4 * i + 3]};
    wait_bar<63>();
#pragma unroll
    for (int i = 0; i < 3; ++i) { const f32x4 v = xr[i * 64]; for (int k = 0; k < 4; ++k) ofin[4 * i + k] = ob[4 * i + k] + v[k]; }
  }
  {
    auto rr = __builtin_amdgcn_permlane32_swap(__float_as_uint(ofin[8]), __float_as_uint(ofin[8]), false, false);
    float l = __uint_as_float(rr[1]);
    if (__builtin_expect(__any(!(l > 8.67e-19f && l < 1.15e18f)), 0)) {
      const bf16x8 qa = q0[1]; s16x4 qb = q1[1];
      float m = -INFINITY;
#pragma unroll 1
      for (int pass = 0; pass < 2; ++pass) {
        set_ref(qb, pass ? m : 0.f, hh != e);
        f32x16 o = f32x16{};
#pragma unroll 1
        for (int kt = 0; kt < 64; ++kt) {
          const unsigned to = (unsigned)(32 * kt + r) * 16;
          const bf16x8 a0 = __builtin_bit_cast(bf16x8, ld128(rs, WS_QKB + (80 + ua + hh) * UNIT + to));
          const s16x4 a1 = __builtin_bit_cast(s16x4, ld64(rs, hh == e ? WS_QKB + (80 + us) * UNIT + to + 8 * e : WS_C + blockIdx.x * 4096));
          f32x16 s = MFMA32(a0, qa, f32x16{}); s = MFMA8(a1, qb, s);
          if (pass == 0) { m = fmaxf(m, max16(s)); continue; }
#pragma unroll
          for (int j = 0; j < 16; ++j) EX(s, j);
#pragma unroll
          for (int s2 = 0; s2 < 2; ++s2) {
            const u32x4 vv = ld128(rs, r < 20 ? WS_VB + (h * 64 + kt) * 2048 + (2 * s2 + hh) * 512 + r * 16 : WS_C + blockIdx.x * 4096 + (r == 20 ? 0 : 16));
            o = smf(__builtin_bit_cast(bf16x8, vv), s, s2, o, idx);
          }
        }
        if (pass == 0) m = swap_max(m);
        else {
#pragma unroll
          for (int i = 0; i < 12; ++i) ofin[i] = o[i];
        }
      }
      auto r2 = __builtin_amdgcn_permlane32_swap(__float_as_uint(ofin[8]), __float_as_uint(ofin[8]), false, false);
      l = __uint_as_float(r2[1]);
    }
    const float rl = __builtin_amdgcn_rcpf(l);
    unsigned ow[6];
#pragma unroll
    for (int i = 0; i < 6; ++i) ow[i] = min(cvtpk(ofin[2 * i] * rl, ofin[2 * i + 1] * rl), 0xfffffffeu);
    st128(rs, obm, u32x4{ow[0], ow[1], ow[2], ow[3]});
    st64(rs, obt, u32x2{ow[4], ow[5]});
  }
  SBAR();
  STAMP(1, 12);
#pragma unroll
  for (int f = 12; f < 25; ++f) wf[f] = ldsr128(wl + WQ + f * 1024);
  SBAR();

  f32x4 accB[5];
  u32x4 og[8]; u32x2 ot[4];
  const unsigned ob0 = osrc + SWP * 5120, ob1 = osrc + (SWP ^ 1) * 5120, ot0 = ob0 - lane * 8 + 4096, ot1 = ob1 - lane * 8 + 4096;
#define OBLD(k, r, a, o) "s_bitcmp1_b32 %17, " #k "\n\ts_cbranch_scc0 1f\n\tglobal_load_dwordx4 %" #r ", %" #a ", %16 offset:" #o " sc1\n1:\n\t"
#define OBLT(k, r0, r1, a) "s_bitcmp1_b32 %17, " #k "\n\ts_cbranch_scc0 1f\n\tglobal_load_dwordx2 %" #r0 ", %" #a ", %16 sc1\n\tglobal_load_dwordx2 %" #r1 ", %" #a ", %16 offset:512 sc1\n1:\n\t"
#define OB_ALL OBLD(0, 0, 12, 0) OBLD(1, 1, 12, 1024) OBLD(2, 2, 12, 2048) OBLD(3, 3, 12, 3072) OBLT(4, 8, 9, 14) OBLD(5, 4, 13, 0) OBLD(6, 5, 13, 1024) OBLD(7, 6, 13, 2048) OBLD(8, 7, 13, 3072) OBLT(9, 10, 11, 15)
#define OG(c) c(og[0]), c(og[1]), c(og[2]), c(og[3]), c(og[4]), c(og[5]), c(og[6]), c(og[7]), c(ot[0]), c(ot[1]), c(ot[2]), c(ot[3])
#define OBL1(k, a, o) og[k] = ld128(rs, WS_OB + a + o)
#define OBL2(k, a, o) ot[k] = ld64(rs, WS_OB + a + o)
#define OFR(j) ((j) % 5 < 4 ? og[((j) - (j) / 5) & 7] : u32x4{ot[2 * ((j) / 5)][0], ot[2 * ((j) / 5)][1], ot[2 * ((j) / 5) + 1][0], ot[2 * ((j) / 5) + 1][1]})
#pragma unroll
  for (int j = 0; j < 5; ++j) {
    if (j == 0) TPRIO(3); if (j == 2) TPRIO(2); if (j == 3) TPRIO(1); if (j == 4) TPRIO(0);
#pragma unroll
    for (int n = 0; n < 5; ++n) accA[n] = MFMA16(wf[j * 5 + n], of[5 + j], accA[n]);
    SBAR();
#pragma unroll
    for (int n = 0; n < 5; ++n) wf[j * 5 + n] = ldsr128(wl + (j * 5 + n) * 1024);
    if (j == OB0) { OBL1(0, ob0, 0); OBL1(1, ob0, 1024); OBL1(2, ob0, 2048); }
    if (j == OB1) { OBL1(3, ob0, 3072); OBL2(0, ot0, 0); OBL2(1, ot0, 512); }
    if (j == OB2) { OBL1(4, ob1, 0); OBL1(5, ob1, 1024); OBL1(6, ob1, 2048); }
    if (j == OB3) { OBL1(7, ob1, 3072); OBL2(2, ot1, 0); OBL2(3, ot1, 512); }
    SBAR();
  }
  STAMP(2, 0);
  waitv_bar<12>();
  SBAR();
  const lds_cptr wh = (lds_cptr)lds + L_RING + kh * AT_STAGE + lane * 16;
#pragma unroll
  for (int j = 0; j < 10; ++j) {
    if (j % 5 == 0) {
      unsigned seen = 0;
#pragma unroll
      for (int k = j; k < j + 5; ++k) { const u32x4 v = OFR(k); seen = max(max(seen, v[0]), v[1]); seen = max(max(seen, v[2]), v[3]); }
      if (__any(seen == ~0u)) {
#pragma unroll 1
        for (unsigned spins = 0; spins < (1u << 16); ++spins) {
          unsigned pend = 0;
#pragma unroll
          for (int k = j; k < 10; ++k) { const u32x4 v = OFR(k); pend |= __any(max(max(v[0], v[1]), max(v[2], v[3])) == ~0u) ? (1u << k) : 0u; }
          if (!(pend & (0x1fu << j))) break;
          asm volatile(OB_ALL "s_waitcnt vmcnt(0)" : OG("+v") : "v"(ob0), "v"(ob1), "v"(ot0), "v"(ot1), "s"(ws + WS_OB), "s"(pend) : "memory", "scc");
        }
      }
      if (j == 0) STAMP(2, 1);
    }
    if (j == 0) TPRIO(3); if (j == 3) TPRIO(2); if (j == 6) TPRIO(1); if (j == 8) TPRIO(0);
#pragma unroll
    for (int n = 0; n < 5; ++n) accB[n] = MFMA16(wf[(j % 5) * 5 + n], OFR(j), j == 0 ? f32x4{} : accB[n]);
    if (j < 5) {
#pragma unroll
      for (int n = 0; n < 5; ++n) wf[j * 5 + n] = ldsr128(wh + (j * 5 + n) * 1024);
    }
    SBAR();
  }
  STAMP(2, 2);
#pragma unroll
  for (int n = 0; n < 5; ++n) accB[n] = accA[n] * gate + accB[n] * (1.f - gate);
  {
    char* img = lds + L_R + (2 * kh + 1) * WQ + (tq * 16 + (lane & 15)) * 336 + (lane >> 4) * 16;
#pragma unroll
    for (int n = 0; n < 5; ++n) *(f32x4*)(img + n * 64) = accB[n];
    wait_bar<63>();
#pragma unroll
    for (int it = 0; it < 2; ++it) {
      const int u = tid + 512 * it, row = u / 10, pc = u - 10 * row;
      if (u < 640) {
        const char* a = lds + L_R + WQ + row * 336 + pc * 32;
        const f32x4 v0 = *(const f32x4*)a + *(const f32x4*)(a + 2 * WQ), v1 = *(const f32x4*)(a + 16) + *(const f32x4*)(a + 2 * WQ + 16);
        const u32x4 r4 = u32x4{cvtpk(v0[0], v0[1]), cvtpk(v0[2], v0[3]), cvtpk(v1[0], v1[1]), cvtpk(v1[2], v1[3])};
        char* op = out + (size_t)(rt * 64 + row) * (D_ * 2) + ct * 160 + pc * 16;
        if (FIN == 1) *(u32x4*)op = r4; else __builtin_nontemporal_store(r4, (u32x4*)op);
      }
    }
  }
  STAMP(2, 3);
  DBG_OUT();
  asm volatile("GM_CODE_END:");
  if (blockIdx.x == 0 && w == 7) {
#pragma unroll 1
    for (int i = 0; i < 4; ++i) wait_flags(flg, FOA + i * 64 + lane, tag);
    if (lane == 0) __hip_atomic_store(flg, tag, RLX_AGENT);
  }
}

extern "C" void kernel_launch(void* const* d_in, const int* in_sizes, int n_in, void* d_out, int out_size, void* d_ws,
                              size_t ws_size, hipStream_t stream) {
  (void)hipFuncSetAttribute((const void*)fused_kernel, hipFuncAttributeMaxDynamicSharedMemorySize, LDS_BYTES);
  fused_kernel<<<256, 512, LDS_BYTES, stream>>>((const char*)d_in[0], (const char*)d_in[1], (const char*)d_in[4], (const char*)d_in[2],
                                                (const char*)d_in[5], (const char*)d_in[6], (char*)d_ws, (char*)d_out);
}
```

```cpp
#include <hip/hip_runtime.h>
#include <hip/hip_bf16.h>
#include <stdint.h>

typedef short bf16x8 __attribute__((ext_vector_type(8)));
typedef short s16x4 __attribute__((ext_vector_type(4)));
typedef __bf16 bfv8 __attribute__((ext_vector_type(8)));
typedef __bf16 bfv2 __attribute__((ext_vector_type(2)));
typedef __bf16 bfv16 __attribute__((ext_vector_type(16)));
typedef float f32x16 __attribute__((ext_vector_type(16)));
typedef float f32x8 __attribute__((ext_vector_type(8)));
typedef float f32x4 __attribute__((ext_vector_type(4)));
typedef unsigned u32x4 __attribute__((ext_vector_type(4)));
typedef unsigned u32x2 __attribute__((ext_vector_type(2)));
typedef unsigned long long u64;
#define LAS __attribute__((address_space(3)))
#define GAS __attribute__((address_space(1)))
#define AINL __attribute__((always_inline))
typedef LAS const char* lds_cptr;
typedef LAS char* lds_ptr;
typedef GAS u64 gu64;

constexpr int D_ = 640, N3_ = 1920;
constexpr u64 TAG_STEP = 0x9E3779B97F4A7C15ull;
constexpr float QSCALE = 0.22360679774997896f * 1.4426950408889634f;
constexpr unsigned OOB = 0x70000000u;

constexpr unsigned UNIT = 2048 * 16;
constexpr unsigned F1A = 512, FG = 768, FOA = 1024, F1B = 2048, NREP = 16;
#define REPSTEP 256
constexpr unsigned WS_QKB = (2048 + NREP * 256) * 128;
constexpr unsigned WS_VB = WS_QKB + 160 * UNIT;
constexpr unsigned BR_A = 160 * UNIT + 32 * 64 * 2048;
constexpr unsigned WS_OA = WS_QKB + 2 * BR_A;
constexpr unsigned WS_OB = WS_OA + 2048 * 640 * 2;
constexpr unsigned WS_WO = WS_OB + 2048 * 640 * 2;
constexpr unsigned WS_G = WS_WO + 2 * 640 * 640 * 2;
constexpr unsigned WS_C = WS_G + 2048 * 4;
constexpr unsigned WS_END = WS_C + 256 * 4096;

constexpr int L_R = 0, WQ = 25600;
constexpr int L_RING = 106496, AT_HALF = 14336, AT_STAGE = 2 * AT_HALF;
constexpr int L_FL = 104448, L_EP = 98304, L_PF = 81920, L_ALL = 102400, L_Q = 0;
constexpr int L_LK = 51200, LA_UNIT = 4608, L_LV = L_LK + 3 * LA_UNIT;
constexpr int LDS_BYTES = 163840;

#define STAMP(kid, i) do {} while (0)
#define DBG_DECL
#define DBG_ARG
#define DBG_PASS
#define DBG_OUT() do {} while (0)
#define BST 2
#define OB0 2
#define OB1 3
#define OB2 4
#define OB3 4
#define TPRIO(x) __builtin_amdgcn_s_setprio(x)
#define SNAP 3
#define FIN 1
#define SBAR() __builtin_amdgcn_sched_barrier(0)
#define PIN(x) asm volatile("" : "+v"(x))
#define MFMA32(a, b, c) __builtin_amdgcn_mfma_f32_32x32x16_bf16(__builtin_bit_cast(bfv8, a), __builtin_bit_cast(bfv8, b), c, 0, 0, 0)
#define MFMA8(a, b, c) __builtin_amdgcn_mfma_f32_32x32x8bf16_1k(a, b, c, 0, 0, 0)
#define MFMA16(a, b, c) __builtin_amdgcn_mfma_f32_16x16x32_bf16(__builtin_bit_cast(bfv8, a), __builtin_bit_cast(bfv8, b), c, 0, 0, 0)
#define RLX_AGENT __ATOMIC_RELAXED, __HIP_MEMORY_SCOPE_AGENT
#define SC1 16

template <int V> struct IC { static constexpr int value = V; };
template <int I, int N, class F> __device__ __forceinline__ void sfor(F&& f) { if constexpr (I < N) { f(IC<I>{}); sfor<I + 1, N>(f); } }
template <int N> __device__ __forceinline__ void wait_bar() { asm volatile("s_waitcnt vmcnt(%0) lgkmcnt(0)\n\ts_barrier" :: "i"(N) : "memory"); }
template <int N> __device__ __forceinline__ void waitv_bar() { asm volatile("s_waitcnt vmcnt(%0)\n\ts_barrier" :: "i"(N) : "memory"); }

__device__ __forceinline__ unsigned cvtpk(float lo, float hi) {
  bfv2 r; r[0] = (__bf16)lo; r[1] = (__bf16)hi;
  return __builtin_bit_cast(unsigned, r);
}
__device__ __forceinline__ float dot2(unsigned a, unsigned b, float c) {
  return __builtin_amdgcn_fdot2_f32_bf16(__builtin_bit_cast(bfv2, a), __builtin_bit_cast(bfv2, b), c, false);
}
template <int OFF> __device__ __forceinline__ void dma(const char* base, unsigned voff, unsigned lds_base) {
  asm volatile("s_add_u32 m0, %2, %3\n\ts_nop 0\n\tglobal_load_lds_dwordx4 %0, %1" :: "v"(voff), "s"(base), "s"(lds_base), "i"(OFF) : "memory", "scc");
}
template <int OFF> __device__ __forceinline__ void dma_sc1(const char* base, unsigned voff, unsigned lds_base) {
  asm volatile("s_add_u32 m0, %2, %3\n\ts_nop 0\n\tglobal_load_lds_dwordx4 %0, %1 sc1" :: "v"(voff), "s"(base), "s"(lds_base), "i"(OFF) : "memory", "scc");
}
template <int OFF> __device__ __forceinline__ void dma2_sc1(const char* base, unsigned voff, unsigned lds_base) {
  asm volatile("s_add_u32 m0, %2, %3\n\ts_nop 0\n\tglobal_load_lds_dwordx4 %0, %1 sc1\n\tglobal_load_lds_dwordx4 %0, %1 offset:1024 sc1"
               :: "v"(voff), "s"(base), "s"(lds_base), "i"(OFF) : "memory", "scc");
}
template <int OFF> __device__ __forceinline__ void dma3_sc1(const char* base, unsigned voff, unsigned lds_base) {
  asm volatile("s_add_u32 m0, %2, %3\n\ts_nop 0\n\tglobal_load_lds_dwordx4 %0, %1 sc1\n\tglobal_load_lds_dwordx4 %0, %1 offset:1024 sc1\n\t"
               "global_load_lds_dwordx4 %0, %1 offset:2048 sc1" :: "v"(voff), "s"(base), "s"(lds_base), "i"(OFF) : "memory", "scc");
}
typedef short v4i16_t __attribute__((ext_vector_type(4)));
__device__ __forceinline__ s16x4 trd(lds_cptr p) {
  return __builtin_bit_cast(s16x4, __builtin_amdgcn_ds_read_tr16_b64_v4i16((LAS v4i16_t*)p));
}
__device__ __forceinline__ bf16x8 cat8(s16x4 a, s16x4 b) { return __builtin_shufflevector(a, b, 0, 1, 2, 3, 4, 5, 6, 7); }
__device__ __forceinline__ bf16x8 ldsr128(lds_cptr p) { return *(const LAS bf16x8*)p; }
__device__ __forceinline__ s16x4 ldsr64(lds_cptr p) { return *(const LAS s16x4*)p; }
__device__ __forceinline__ u32x4 pack8(const f32x16& a, int s, float sc) {
  return u32x4{cvtpk(a[8 * s] * sc, a[8 * s + 1] * sc), cvtpk(a[8 * s + 2] * sc, a[8 * s + 3] * sc),
               cvtpk(a[8 * s + 4] * sc, a[8 * s + 5] * sc), cvtpk(a[8 * s + 6] * sc, a[8 * s + 7] * sc)};
}
__device__ __forceinline__ int swap23(int r) { return (r & 0x13) | ((r & 4) << 1) | ((r & 8) >> 1); }
__device__ __forceinline__ u64 uni64(unsigned lo, unsigned hi) {
  return ((u64)(unsigned)__builtin_amdgcn_readfirstlane(hi) << 32) | (unsigned)__builtin_amdgcn_readfirstlane(lo);
}

typedef __amdgpu_buffer_rsrc_t rsrc_t;
__device__ __forceinline__ void st128(rsrc_t rs, unsigned off, u32x4 v) { __builtin_amdgcn_raw_buffer_store_b128(v, rs, off, 0, SC1); }
__device__ __forceinline__ void st64(rsrc_t rs, unsigned off, u32x2 v) { __builtin_amdgcn_raw_buffer_store_b64(v, rs, off, 0, SC1); }
__device__ __forceinline__ u32x4 ld128(rsrc_t rs, unsigned off) { return __builtin_amdgcn_raw_buffer_load_b128(rs, off, 0, SC1); }
__device__ __forceinline__ u32x2 ld64(rsrc_t rs, unsigned off) { return __builtin_amdgcn_raw_buffer_load_b64(rs, off, 0, SC1); }
__device__ __forceinline__ unsigned ld32(rsrc_t rs, unsigned off) { return __builtin_amdgcn_raw_buffer_load_b32(rs, off, 0, SC1); }

template <int SLEEP = 6, int ROT = 0>
__device__ __forceinline__ void wait_flags(gu64* flg, unsigned idx, u64 tag) {
  bool ok = false;
#pragma unroll 1
  for (unsigned spins = 0; spins < (1u << 18); ++spins) {
    if (!ok) ok = __hip_atomic_load(flg + (idx + min(spins, (unsigned)ROT) * 256) * 16, RLX_AGENT) == tag;
    if (__all(ok)) break;
    __builtin_amdgcn_s_sleep(SLEEP);
  }
  asm volatile("" ::: "memory");
}
__device__ __forceinline__ void sample_flags(u64& fa, u64& fb, unsigned ia, unsigned ib, const char* base, u64 miss) {
  asm volatile("s_cmp_eq_u64 %5, 0\n\ts_cbranch_scc1 1f\n\tglobal_load_dwordx2 %0, %2, %4 sc1\n\tglobal_load_dwordx2 %1, %3, %4 sc1\n1:"
               : "+v"(fa), "+v"(fb) : "v"(ia * 128), "v"(ib * 128), "s"(base), "s"(miss) : "memory", "scc");
}

__device__ __forceinline__ unsigned o_main(int tok, int h, int hh) {
  const int m = h >> 1;
  return (unsigned)((((tok >> 4) * 20 + 5 * (m >> 2) + (m & 3)) * 64 + (2 * (h & 1) + hh) * 16 + (tok & 15)) * 16);
}
__device__ __forceinline__ unsigned o_tail(int tok, int h) {
  const int m = h >> 1;
  return (unsigned)(((tok >> 4) * 20 + 5 * (m >> 2) + 4) * 1024 + 512 * (m & 1) + ((2 * ((m >> 1) & 1) + (h & 1)) * 16 + (tok & 15)) * 8);
}

#define G1_ORDER "BBBBBBBBBBAAAAAAAAAA"
#define G1_PUB 14
#define G1_EP 12
#define G1_SAMP 72
#define G1_LOOK 76
#define G1_AGAIN true
struct G1 {
  int isB[24] = {}, kb[24] = {}, uf[25] = {};
  int lastB = 0;
  int iss_lo[20] = {}, iss_hi[20] = {};
  int wait[20] = {};
  constexpr G1() {
    const char* ord = G1_ORDER;
    int nb = 0, na = 0;
    for (int o = 0; o < 24; ++o) {
      isB[o] = o < 20 && ord[o] == 'B'; kb[o] = o < 20 ? (isB[o] ? nb++ : na++) : 0;
      uf[o + 1] = uf[o] + (o < 20 ? 1 + isB[o] : 0);
      if (isB[o]) lastB = o;
    }
    int pos[60] = {}, cnt = 0, lastst = 0, done = 2 * uf[2];
    for (int d = 0; d < done; ++d) pos[d] = cnt++;
    for (int o = 0; o < 20; ++o) {
      int need = pos[2 * uf[o + 1] - 1];
      if (o == G1_PUB && lastst > need) need = lastst;
      wait[o] = o >= 18 ? 0 : cnt - 1 - need;
      if (o == G1_EP) lastst = cnt++;
      iss_lo[o] = done; iss_hi[o] = 2 * uf[o + 4];
      for (int ks = 0; ks < 4; ++ks) {
        const int per = (iss_hi[o] - iss_lo[o] + 3) / 4;
        for (int z = 0; z < per && done < iss_hi[o]; ++z) pos[done++] = cnt++;
        const int n = 4 * o + ks - 2 - (4 * lastB + 3);
        if (n >= 0 && n < 4 * BST && n % BST == 0) lastst = cnt++;
      }
    }
  }
};
constexpr int g1_slot(int u) { return ((u + 6) % 10) * 16384; }
template <bool VT, class FPiece, class FStart, class FSetup>
__device__ __forceinline__ void g1_body(char* ws, rsrc_t rs, char* lds, int ti, int cj, int w, int lane, u64& tag, bool& early, unsigned& fiKV, unsigned& fiQ,
                                        u32x4 (&pk)[4], unsigned (&dstB)[2], FPiece&& piece, FStart&& att_start, FSetup&& setup DBG_ARG) {
  constexpr G1 P;
  static_assert(P.uf[G1_EP - 1] > 20 && G1_EP < G1_PUB && P.uf[4] <= 9, "epoch / prefetch scratch slots");
  const int tg = w & 3, cg = w >> 2;
  const int r = lane & 31, hh = lane >> 5, g16 = (lane >> 4) & 1, q4 = (lane & 15) >> 2, p4 = lane & 3;
  const int tr = VT ? swap23(r) : r;
  unsigned xoff[4];
#pragma unroll
  for (int ks = 0; ks < 4; ++ks) xoff[ks] = (unsigned)((32 * tg + tr) * 128 + (((2 * ks + hh) ^ ((tr >> 1) & 7)) * 16));
  const int psw = ((p4 & 1) << 1) | (p4 >> 1);
  unsigned woff[2];
#pragma unroll
  for (int t2 = 0; t2 < 2; ++t2) woff[t2] = (unsigned)((8 * hh + q4) * 256 + (((2 * cg + t2) ^ q4) * 64) + 32 * g16 + 8 * (VT ? p4 : psw));
  const float sc = (!VT && cj < 5) ? QSCALE : 1.f;
  bf16x8 xf[40], wf[3][2];
  u64 fa, fb;
  f32x16 accB[2], accA[2];
  u64 miss = 1;
  auto look = [&](bool again) AINL {
    asm volatile("s_cmp_eq_u64 %2, 0\n\ts_cbranch_scc1 1f\n\ts_waitcnt vmcnt(0)\n1:" : "+v"(fa), "+v"(fb) : "s"(miss) : "memory", "scc");
    const u64 m = __ballot(fa != tag || fb != tag);
    if (m == 0 && miss != 0) att_start();
    miss = m;
    if (again) sample_flags(fa, fb, fiKV, fiQ, ws + (F1B + REPSTEP) * 128, miss);
  };
  sfor<0, 82>([&](auto ic) AINL {
    constexpr int nn = decltype(ic)::value, n = nn - 2;
    constexpr int o = nn >> 2, ks = nn & 3;
    if constexpr (nn < 80) {
      if constexpr (ks == 0) {
        if constexpr (o < 19) waitv_bar<P.wait[o]>();
        if constexpr (o == G1_EP) dma_sc1<L_EP>(ws, 0, (unsigned)(uintptr_t)lds + w * 1024);
        if constexpr (o == G1_PUB) { const u32x2 ep = *(const LAS u32x2*)((lds_cptr)lds + L_EP + w * 1024); tag = uni64(ep[0], ep[1]) + TAG_STEP; }
        if constexpr (o == G1_PUB) { if (w == 0 && lane < NREP) __hip_atomic_store((gu64*)ws + (F1B + lane * 256 + cj * 16 + ti) * 16, tag, RLX_AGENT); }
        if constexpr (o % 2 == 0) STAMP(0, 1 + o / 2);
      }
      if constexpr (nn >= 62 && nn < 70 && !(nn & 1)) setup(IC<(nn - 62) / 2>{});
      if constexpr (nn == G1_SAMP) { fa = 0; fb = 0; sample_flags(fa, fb, fiKV, fiQ, ws + F1B * 128, miss); }
      if constexpr (nn == G1_LOOK) look(G1_AGAIN);
      if constexpr (P.isB[o]) xf[4 * P.kb[o] + ks] = ldsr128((lds_cptr)lds + g1_slot(P.uf[o] + 1) + xoff[ks]);
#pragma unroll
      for (int t2 = 0; t2 < 2; ++t2) {
        const lds_cptr wp = (lds_cptr)lds + g1_slot(P.uf[o]) + woff[t2] + ks * 4096;
        wf[nn % 3][t2] = cat8(trd(wp), trd(wp + 1024));
      }
      constexpr int per = (P.iss_hi[o] - P.iss_lo[o] + 3) / 4, d0 = P.iss_lo[o] + per * ks;
      sfor<d0, (d0 + per < P.iss_hi[o] ? d0 + per : P.iss_hi[o])>(piece);
    }
    if constexpr (n >= 0) {
      constexpr int om = n >> 2, xi = 4 * P.kb[om] + (n & 3);
#pragma unroll
      for (int t2 = 0; t2 < 2; ++t2) {
        f32x16& ac = P.isB[om] ? accB[t2] : accA[t2];
        const f32x16 cin = xi == 0 ? f32x16{} : ac;
        ac = VT ? MFMA32(xf[xi], wf[n % 3][t2], cin) : MFMA32(wf[n % 3][t2], xf[xi], cin);
      }
    }
    SBAR();
    constexpr int nb = n - (4 * P.lastB + 3);
    if constexpr (nb == 0) {
#pragma unroll
      for (int i = 0; i < 4; ++i) { pk[i] = VT ? pack8(accB[i >> 1], i & 1, 1.f) : pack8(accB[i >> 1], i & 1, sc); PIN(pk[i]); }
      int ln = lane; PIN(ln);
#pragma unroll
      for (int t2 = 0; t2 < 2; ++t2) {
        if (VT) {
          const int F = (cj - 10) * 128 + 64 * cg + 32 * t2 + (ln & 31), hd = F / 20;
          int d = F - 20 * hd - 4 * (hd & 1); if (d < 0) d += 20;
          dstB[t2] = WS_VB + (unsigned)((hd * 64 + 4 * ti + tg) * 2048 + (ln >> 5) * 512 + d * 16);
        } else {
          dstB[t2] = WS_QKB + (unsigned)(cj * 16 + 8 * cg + 4 * t2 + (ln >> 5)) * UNIT + (unsigned)(128 * ti + 32 * tg + (ln & 31)) * 16;
        }
      }
    }
    if constexpr (nb >= 0 && nb < 4 * BST && nb % BST == 0) {
      constexpr int i = nb / BST;
      st128(rs, dstB[i >> 1] + (i & 1) * (VT ? 1024 : 2 * UNIT), pk[i]);
      SBAR();
    }
  });
  STAMP(0, 13);
  look(false);
  early = miss == 0;
#pragma unroll
  for (int i = 0; i < 4; ++i) pk[i] = pack8(accA[i >> 1], i & 1, 1.f);
}

__device__ __forceinline__ f32x16 smf(bf16x8 a, const f32x16& p, int s, f32x16 o, int idx) {
  f32x8 b = s ? __builtin_shufflevector(p, p, 8, 9, 10, 11, 12, 13, 14, 15) : __builtin_shufflevector(p, p, 0, 1, 2, 3, 4, 5, 6, 7);
  return __builtin_amdgcn_smfmac_f32_32x32x32_bf16(__builtin_bit_cast(bfv8, a), __builtin_bit_cast(bfv16, b), o, idx, 0, 0);
}
__device__ __forceinline__ float max16(const f32x16& s) {
  float a = fmaxf(fmaxf(s[0], s[1]), s[2]), b = fmaxf(fmaxf(s[3], s[4]), s[5]);
  a = fmaxf(fmaxf(a, s[6]), s[7]); b = fmaxf(fmaxf(b, s[8]), s[9]);
  a = fmaxf(fmaxf(a, s[10]), s[11]); b = fmaxf(fmaxf(b, s[12]), s[13]);
  a = fmaxf(fmaxf(a, s[14]), s[15]);
  return fmaxf(a, b);
}
__device__ __forceinline__ float swap_max(float m) {
  auto rr = __builtin_amdgcn_permlane32_swap(__float_as_uint(m), __float_as_uint(m), false, false);
  return fmaxf(__uint_as_float(rr[0]), __uint_as_float(rr[1]));
}
__device__ __forceinline__ float swap_add(float m) {
  auto rr = __builtin_amdgcn_permlane32_swap(__float_as_uint(m), __float_as_uint(m), false, false);
  return __uint_as_float(rr[0]) + __uint_as_float(rr[1]);
}
__device__ __forceinline__ void set_ref(s16x4& q1, float m, bool spare) {
  const unsigned a = __float_as_uint(-m) & 0xffff0000u;
  const float r1 = -m - __uint_as_float(a);
  const unsigned b = __float_as_uint(r1) & 0xffff0000u;
  const unsigned c = __float_as_uint(r1 - __uint_as_float(b));
  if (spare) q1 = __builtin_bit_cast(s16x4, u32x2{(a >> 16) | b, c >> 16});
}

__global__ __launch_bounds__(512, 2) void fused_kernel(const char* __restrict__ x, const char* __restrict__ wA, const char* __restrict__ wB,
                                                       const char* __restrict__ woA, const char* __restrict__ woB,
                                                       const char* __restrict__ gw, char* ws, char* __restrict__ out) {
  extern __shared__ __attribute__((aligned(16))) char lds[];
  const int tid = threadIdx.x, lane = tid & 63;
  const int w = __builtin_amdgcn_readfirstlane(tid >> 6);
  const int xcd = blockIdx.x & 7, slot = blockIdx.x >> 3;
  const unsigned lds0 = (unsigned)(uintptr_t)lds;
  gu64* flg = (gu64*)ws;
  const rsrc_t rs = __builtin_amdgcn_make_buffer_rsrc(ws, 0, WS_END, 0x00020000);
  DBG_DECL
  STAMP(0, 0);
  const int ti = 8 * (xcd & 1) + (slot & 7);
  const int cj = 4 * (xcd >> 1) + (slot >> 3);
  const unsigned ldpf = lds0 + (cj < 15 ? L_PF : L_RING);
  if (w == 6) {
    u64 pc; unsigned len;
    asm volatile("s_getpc_b64 %0\nGM_CODE_BEG:\n\ts_mov_b32 %1, GM_CODE_END-GM_CODE_BEG" : "=s"(pc), "=s"(len));
    asm volatile("s_add_u32 m0, %2, %3\n\ts_nop 0\n\tglobal_load_lds_dword %0, %1"
                 :: "v"(min((unsigned)((slot & 7) * 64 + lane) * 128, len)), "s"(pc), "s"(ldpf), "i"(0) : "memory", "scc");
  }
  const int rot = ((slot & 7) * 5 / 4 + (slot >> 3) * 5 / 2 + xcd) % 10;
  unsigned xlo[2], wlo[2];
#pragma unroll
  for (int i = 0; i < 2; ++i) {
    const int row = 16 * w + 8 * i + (lane >> 3);
    xlo[i] = (unsigned)(row * (D_ * 2) + (((lane & 7) ^ ((row >> 1) & 7)) * 16));
    wlo[i] = (unsigned)((8 * w + 4 * i + (lane >> 4)) * (N3_ * 2) + (((lane & 15) ^ (4 * (lane >> 4))) * 16));
  }
  const char* xb = x + (size_t)(128 * ti) * (D_ * 2);
  const char* wBb = wB + cj * 256;
  const char* wAb = wA + cj * 256;
  const unsigned ldw = lds0 + w * 2048;
  auto piece = [&](auto dc) AINL {
    constexpr G1 P;
    constexpr int d = decltype(dc)::value, u = d >> 1, base = g1_slot(u) + (d & 1) * 1024;
    constexpr int o = [&]() { int q = 0; while (P.uf[q + 1] <= u) ++q; return q; }();
    int kb = P.kb[o] + rot; if (kb >= 10) kb -= 10;
    if constexpr (u != P.uf[o]) dma<base>(xb + kb * 128, xlo[d & 1], ldw);
    else dma<base>((P.isB[o] ? wBb : wAb) + (size_t)kb * (64 * N3_ * 2), wlo[d & 1], ldw);
  };
  if (cj < 15) {
    sfor<0, 4>(piece);
    sfor<4, 8>(piece);
  }
  STAMP(0, 14);
  SBAR();
  u64 tag;
  const int h = 4 * xcd + (slot & 3), c = slot >> 2;
  const int e = h & 1, ua = 5 * (h >> 1) + 3 * e, us = 5 * (h >> 1) + 2;
  const int jlo = (5 * (h >> 1) + 2 * e) >> 4, jhi = (5 * (h >> 1) + 2 * e + 2) >> 4;
  const int kh = w >> 2, tq = w & 3;
  const char* kbase = ws + WS_QKB + (size_t)(80 + (tq >= 2 ? us : ua + tq)) * UNIT + 1024 * kh * 16;
  if (w == 5) { const unsigned cv = (lane & 1) ? 0u : 0x3f803f80u; st128(rs, lane < 8 ? WS_C + blockIdx.x * 4096 + (lane >> 1) * 1024 + (lane & 1) * 16 : OOB, u32x4{cv, cv, cv, cv}); }
  const unsigned ldr = lds0 + kh * AT_HALF + tq * 2048;
  unsigned vlo, vstr;
  auto kpiece = [&](auto stc) AINL {
    constexpr int st = decltype(stc)::value;
    if (tq < 3) dma2_sc1<L_RING + (st & 1) * AT_STAGE>(kbase + st * 2048, lane * 16, ldr);
  };
  auto vpiece = [&](auto stc) AINL {
    constexpr int st = decltype(stc)::value;
    dma2_sc1<L_RING + (st & 1) * AT_STAGE + 6144>(ws, vlo + st * vstr, ldr);
  };
  unsigned fiKV, fiQ;
  auto att_start = [&]() AINL {
    kpiece(IC<0>{});
    if (tq == 3) {
      const char* qb = ws + WS_QKB + (256 * c + 128 * kh) * 16;
      dma2_sc1<L_Q>(qb + (size_t)ua * UNIT, lane * 16, lds0 + kh * 2048);
      dma2_sc1<L_Q + 4096>(qb + (size_t)(ua + 1) * UNIT, lane * 16, lds0 + kh * 2048);
      dma2_sc1<L_Q + 8192>(qb + (size_t)us * UNIT, lane * 16, lds0 + kh * 2048);
    }
    vpiece(IC<0>{});
  };

  const int hh = lane >> 5, r = lane & 31;
  const int rt = 4 * xcd + (slot & 3), ct = slot >> 2;
  const int SWP = ct & 1;
  const char* wob = ws + WS_WO + (size_t)ct * 102400 + w * 3072;
  auto woquarter = [&](auto brc, auto qc, auto dc) AINL {
    constexpr int br = decltype(brc)::value, q = decltype(qc)::value, d = decltype(dc)::value;
    const int off = br * 819200 + (q ^ (br & SWP)) * WQ;
    dma3_sc1<d>(wob + off, lane * 16, lds0 + w * 3072);
    if (w == 0) dma_sc1<d + 24576>(wob + off + 24576, lane * 16, lds0);
  };
  const unsigned ladst = lds0 + (w < 3 ? L_LK + w * LA_UNIT : L_LV + min(4 * (w - 3), 13) * 1024);
  unsigned osrc, fiA, obm, obt, lalo; int idx;
  lds_cptr kp0, kp1, vp, qp;
  f32x16 oa, ob;
  auto setup = [&](auto pc) AINL {
    constexpr int p = decltype(pc)::value;
    int ln = lane; PIN(ln);
    const int hh = ln >> 5, r = ln & 31;
    if constexpr (p == 0) {
      const unsigned crow = WS_C + blockIdx.x * 4096 + (r == 20 ? 0 : 16);
      vlo = r < 20 ? WS_VB + (h * 64 + 32 * kh + tq) * 2048 + ln * 16 : crow; vstr = r < 20 ? 8192u : 0u;
      const unsigned lab = w < 3 ? WS_QKB + BR_A + (80 + (w == 2 ? us : ua + w)) * UNIT + (256 * c - 16) * 16
                                 : WS_VB + BR_A + (h * 64 + 8 * c) * 2048 - 1024 + min(4 * (w - 3), 13) * 1024;
      lalo = (w < 3 || r < 20) ? lab + ln * 16 : crow;
      osrc = (unsigned)((rt * 4 + tq) * 20 + 10 * kh) * 1024 + ln * 16;
      PIN(vlo); PIN(vstr); PIN(lalo); PIN(osrc);
    }
    if constexpr (p == 1) {
      fiA = ln < 18 ? F1A + (5 * (ln / 6) + (((ln / 3) & 1) ? jhi : jlo)) * 16 + max(2 * c - 1 + ln % 3, 0) : FG + (ln == 18 ? ct : 8 + ct);
      obm = WS_OB + o_main(256 * c + 64 * tq + 32 * kh + r, h, hh); obt = (hh ? OOB : 0u) + WS_OB + o_tail(256 * c + 64 * tq + 32 * kh + r, h);
      PIN(fiA); PIN(obm); PIN(obt);
    }
    if constexpr (p == 2) {
      const lds_cptr ring = (lds_cptr)lds + L_RING + kh * AT_HALF;
      kp0 = ring + hh * 2048 + r * 16;
      kp1 = (hh == e) ? ring + 2 * 2048 + r * 16 + 8 * hh : ring + 6144 + 20 * 16;
      vp = ring + 6144 + hh * 512 + r * 16;
      qp = (lds_cptr)lds + L_Q + (64 * tq + r) * 16;
      idx = 0xDDDD;
      PIN(kp0); PIN(kp1); PIN(vp); PIN(qp); PIN(idx);
    }
    if constexpr (p == 3) {
      oa = f32x16{}; ob = f32x16{}; PIN(oa); PIN(ob);
      fiKV = (5 + 5 * hh + ((ln & 16) ? jhi : jlo)) * 16 + (ln & 15); fiQ = ((ln & 1) ? jhi : jlo) * 16 + 2 * c + ((ln >> 1) & 1);
      PIN(fiKV); PIN(fiQ);
    }
  };

  bool early = false;
  u32x4 pk[4]; unsigned dstA[2];
  if (cj < 10) g1_body<false>(ws, rs, lds, ti, cj, w, lane, tag, early, fiKV, fiQ, pk, dstA, piece, att_start, setup DBG_PASS);
  else if (cj < 15) g1_body<true>(ws, rs, lds, ti, cj, w, lane, tag, early, fiKV, fiQ, pk, dstA, piece, att_start, setup DBG_PASS);
  else {
    const char* wo = ((ti & 8) ? woB : woA) + (size_t)(80 * w) * (D_ * 2) + (ti & 7) * 160;
#pragma unroll 1
    for (int p = 0; p < 13; ++p) {
      const int s0 = min(64 * p, 736), sl = s0 + lane;
      dma<0>(wo, (unsigned)((sl / 10) * (D_ * 2) + (sl % 10) * 16), lds0 + w * 12800 + s0 * 16);
    }
    const u64 ep = __hip_atomic_load(flg, RLX_AGENT);
    tag = uni64((unsigned)ep, (unsigned)(ep >> 32)) + TAG_STEP;
    wait_bar<0>();
    const int g4 = lane >> 4, rl = 320 * kh + 4 * (g4 & 1) + ((lane & 15) >> 2);
#pragma unroll
    for (int ff = 0; ff < 13; ++ff) {
      const int f = min(tq + 4 * ff, 49), j = f / 5, n = f - 5 * j;
      const bool mn = j % 5 < 4;
      const int row = rl + 160 * (j / 5) + (mn ? 40 * (j % 5) + 24 * (g4 >> 1) : 80 * (g4 >> 1) + 16);
      const lds_cptr p = (lds_cptr)lds + row * 160 + n * 32 + 8 * (lane & 3);
      st128(rs, WS_WO + (unsigned)(ti * 100 + kh * 50 + f) * 1024 + lane * 16, __builtin_bit_cast(u32x4, cat8(trd(p), trd(p + (mn ? 8 : 40) * 160))));
    }
    wait_bar<0>();
    if (tid == 0) __hip_atomic_store(flg + (FG + ti) * 16, tag, RLX_AGENT);
    if (w == 0) { wait_flags<16>(flg, F1B + (NREP - 1) * 256 + jlo * 16 + 2 * c, tag); }
    wait_bar<63>();
    sfor<0, 4>(setup);
    dstA[0] = OOB; dstA[1] = OOB;
#pragma unroll
    for (int i = 0; i < 4; ++i) pk[i] = u32x4{0u, 0u, 0u, 0u};
  }
  const unsigned flag1 = F1A + cj * 16 + ti;

  STAMP(1, 0);
  if (!early) {
    bool oka = false, okb = false;
#pragma unroll 1
    for (unsigned spins = 0; spins < (1u << 18); ++spins) {
      const unsigned rp = F1B + (2 + min(spins, NREP - 4)) * REPSTEP;
      const u64 a = __hip_atomic_load(flg + (rp + (oka ? fiQ : fiKV)) * 16, RLX_AGENT), b = __hip_atomic_load(flg + (rp + fiQ) * 16, RLX_AGENT);
      oka = oka || a == tag; okb = okb || b == tag;
      if (__all(oka && okb)) break;
      __builtin_amdgcn_s_sleep(2);
    }
    asm volatile("" ::: "memory");
    att_start();
  }
  STAMP(1, 1);
  wait_bar<0>();
  STAMP(1, 2);
  float gate;
  const unsigned fiOA = FOA + (rt >> 2) * 32 + r;
  u32x4 lq; u32x2 lqt;
  f32x16 sa, sb;
  bf16x8 q0[2]; s16x4 q1[2];
#pragma unroll
  for (int t = 0; t < 2; ++t) {
    q0[t] = ldsr128(qp + hh * 4096 + ((t ^ kh ^ 1) * 512));
    q1[t] = ldsr64(qp + 8192 + 8 * e + ((t ^ kh ^ 1) * 512));
  }
  const bf16x8 kr0 = ldsr128(kp0 - kh * AT_HALF); const s16x4 kr1 = ldsr64(kp1 - kh * AT_HALF);
  bf16x8 k0 = ldsr128(kp0); s16x4 k1 = ldsr64(kp1);
  bf16x8 v0 = ldsr128(vp), v1 = ldsr128(vp + 1024);
  bf16x8 v1p = v1;
  SBAR();
  {
    set_ref(q1[0], 0.f, hh != e); set_ref(q1[1], 0.f, hh != e);
    f32x16 s = MFMA32(kr0, q0[0], f32x16{}), t = MFMA32(kr0, q0[1], f32x16{});
    s = MFMA8(kr1, q1[0], s); t = MFMA8(kr1, q1[1], t);
    sa = MFMA32(k0, q0[0], f32x16{});
    SBAR();
    kpiece(IC<1>{}); vpiece(IC<1>{});
    SBAR();
    set_ref(q1[0], swap_max(max16(s)), hh != e);
    sa = MFMA8(k1, q1[0], sa);
    SBAR();
    set_ref(q1[1], swap_max(max16(t)), hh != e);
  }
  STAMP(1, 3);
  const bool keeper = blockIdx.x == 0 && w == 7;
  bool advanced = false;
  u32x4 of[10];
  unsigned gx[5], gg[5]; float gs;

  auto local_attention = [&]() AINL {
    const int tok = 256 * c + 32 * w + r;
    const lds_cptr lk = (lds_cptr)lds + L_LK + (32 * w + r) * 16, lv = (lds_cptr)lds + L_LV + (4 * w + hh) * 512 + r * 16;
    const bf16x8 qa = __builtin_bit_cast(bf16x8, lq);
    const s16x4 qb = __builtin_bit_cast(s16x4, (hh == e) ? lqt : u32x2{0u, 0u});
    f32x16 sx = MFMA32(ldsr128(lk + hh * LA_UNIT), qa, f32x16{}), sy = MFMA32(ldsr128(lk + hh * LA_UNIT + 512), qa, f32x16{});
    sx = MFMA8(ldsr64(lk + 2 * LA_UNIT + 8 * e), qb, sx); sy = MFMA8(ldsr64(lk + 2 * LA_UNIT + 8 * e + 512), qb, sy);
    const bf16x8 va = ldsr128(lv), vb = ldsr128(lv + 1024), vc = ldsr128(lv + 2048);
    const u64 edge = (c == 0 && w == 0) ? 0ull : ~0ull;
    const float ninf = -INFINITY;
    sfor<0, 24>([&](auto jc) AINL {
      constexpr int j = decltype(jc)::value;
      constexpr u64 mk = []() { u64 m = 0; for (int l = 0; l < 64; ++l) { const int d = (l & 31) + 16 - ((j & 3) + 8 * (j >> 2) + 4 * (l >> 5)); if (d >= 0 && d <= 14 && !(d & 1)) m |= 1ull << l; } return m; }();
      const bool ok = __builtin_amdgcn_inverse_ballot_w64(j < 8 ? mk & edge : mk);
      if constexpr (j < 16) sx[j] = ok ? sx[j] : ninf; else sy[j - 16] = ok ? sy[j - 16] : ninf;
    });
    float m = fmaxf(fmaxf(max16(sx), sy[0]), sy[1]);
    m = fmaxf(fmaxf(m, sy[2]), sy[3]); m = fmaxf(fmaxf(m, sy[4]), sy[5]); m = -QSCALE * swap_max(fmaxf(fmaxf(m, sy[6]), sy[7]));
#pragma unroll
    for (int j = 0; j < 16; ++j) sx[j] = __builtin_amdgcn_exp2f(__builtin_fmaf(sx[j], QSCALE, m));
#pragma unroll
    for (int j = 0; j < 8; ++j) sy[j] = __builtin_amdgcn_exp2f(__builtin_fmaf(sy[j], QSCALE, m));
    f32x16 o = smf(va, sx, 0, f32x16{}, idx); o = smf(vb, sx, 1, o, idx); o = smf(vc, sy, 0, o, idx);
    auto lr = __builtin_amdgcn_permlane32_swap(__float_as_uint(o[8]), __float_as_uint(o[8]), false, false);
    const float rl = __builtin_amdgcn_rcpf(__uint_as_float(lr[1]));
    st128(rs, WS_OA + o_main(tok, h, hh), pack8(o, 0, rl));
    st64(rs, hh ? OOB : WS_OA + o_tail(tok, h), u32x2{cvtpk(o[8] * rl, o[9] * rl), cvtpk(o[10] * rl, o[11] * rl)});
    gs += __builtin_bit_cast(float, __builtin_amdgcn_update_dpp(0, __builtin_bit_cast(int, gs), 0xB1, 0xf, 0xf, true));
    gs += __builtin_bit_cast(float, __builtin_amdgcn_update_dpp(0, __builtin_bit_cast(int, gs), 0x4E, 0xf, 0xf, true));
    gs += __builtin_bit_cast(float, __builtin_amdgcn_update_dpp(0, __builtin_bit_cast(int, gs), 0x141, 0xf, 0xf, true));
    gs += __builtin_bit_cast(float, __builtin_amdgcn_update_dpp(0, __builtin_bit_cast(int, gs), 0x140, 0xf, 0xf, true));
    { auto g2 = __builtin_amdgcn_permlane16_swap(__float_as_uint(gs), __float_as_uint(gs), false, false); gs = __uint_as_float(g2[0]) + __uint_as_float(g2[1]); }
    gs = swap_add(gs);
    const float gv = __builtin_amdgcn_rcpf(1.f + __builtin_amdgcn_exp2f(-1.4426950408889634f * gs));
    __builtin_amdgcn_raw_buffer_store_b32(__float_as_uint(gv), rs, lane == 0 ? WS_G + (256 * c + 8 * h + w) * 4 : OOB, 0, SC1);
    SBAR();
  };

  const lds_cptr wl = (lds_cptr)lds + L_R + kh * (2 * WQ) + lane * 16;
  f32x4 accA[5];
  bf16x8 wq[2][5], wf[25];
  auto opa = [&](auto hc, auto gc) AINL {
    constexpr int hs = decltype(hc)::value, g = decltype(gc)::value;
    if constexpr (hs >= 0 && hs <= 5) SBAR();
    if constexpr (hs >= 1 && hs <= 5) accA[g] = MFMA16(wq[(hs - 1) & 1][g], of[hs - 1], hs == 1 ? f32x4{} : accA[g]);
    if constexpr (hs >= 0 && hs < 5) wq[hs & 1][g] = ldsr128(wl + (hs * 5 + g) * 1024);
    if constexpr (hs >= 0 && hs <= 5) SBAR();
  };
#define EX(S, j) S[j] = __builtin_amdgcn_exp2f(S[j])
#define EX2(S, j) do { EX(S, j); EX(S, j + 1); } while (0)
#define EX4(S, j) do { EX(S, j); EX(S, j + 1); EX(S, j + 2); EX(S, j + 3); } while (0)
  sfor<0, 8>([&](auto stc) AINL {
    constexpr int st = decltype(stc)::value;
    constexpr int cur = (st & 1) * AT_STAGE, nxt = ((st + 1) & 1) * AT_STAGE;
    sfor<0, 4>([&](auto ic) AINL {
      constexpr int i = decltype(ic)::value;
      constexpr int nk = (i < 3) ? cur + (i + 1) * 512 : nxt, nv = (i < 3) ? cur + (i + 1) * 2048 : nxt;
      constexpr int sl = (i + 1) & 3, sst = (i == 3) ? st + 1 : st;
      constexpr int ha = st == 6 ? 2 * i : -9, hb = st == 6 ? 2 * i + 1 : -9;
      SBAR();
      EX2(sa, 0); PIN(sa); SBAR();
      if constexpr (st + i > 0) ob = smf(v1p, sb, 1, ob, idx);
      SBAR(); EX2(sa, 2); opa(IC<ha>{}, IC<0>{}); EX2(sa, 4); PIN(sa); SBAR();
      sb = MFMA32(k0, q0[1], f32x16{}); SBAR(); EX2(sa, 6); opa(IC<ha>{}, IC<1>{}); EX2(sa, 8); PIN(sa); SBAR();
      sb = MFMA8(k1, q1[1], sb); SBAR();
      if constexpr (i == 3) {
        wait_bar<(st == 4 && SNAP == 2) ? 3 : 0>();
        if constexpr (st == 0) { if (tid == 0) __hip_atomic_store(flg + flag1 * 16, tag, RLX_AGENT); }
        if constexpr (st == 2) {
          PIN(lq); PIN(lqt);
        }
        if constexpr (st == 1) {
          gs = 0.f;
#pragma unroll
          for (int i = 0; i < 5; ++i) gs = dot2(gx[i], gg[i], gs);
          PIN(gs);
        }
        if constexpr (st == 3) { if (tid == 0) __hip_atomic_store(flg + (FOA + c * 32 + h) * 16, tag, RLX_AGENT); }
        if constexpr (st == 5 || st == 7) {
#pragma unroll
          for (int j = 0; j < 5; ++j) PIN(of[(st == 7 ? 5 : 0) + j]);
          if constexpr (st == 7) PIN(gate);
        }
        if constexpr (st == 6) {
          if (keeper) {
            const lds_cptr p = (lds_cptr)lds + L_ALL + lane * 16;
            advanced = __all(*(const LAS u64*)p == tag && *(const LAS u64*)(p + 1024) == tag && *(const LAS u64*)(p + 2048) == tag && *(const LAS u64*)(p + 3072) == tag);
            if (advanced && lane == 0) __hip_atomic_store(flg, tag, RLX_AGENT);
          }
        }
        if constexpr (st == 1 || st == 4) {
          if (!__all(*(const LAS u64*)((lds_cptr)lds + L_FL + lane * 16) == tag)) wait_flags(flg, st == 1 ? fiA : fiOA, tag);
        }
      }
      __builtin_amdgcn_s_setprio(3 - sl);
      k0 = ldsr128(kp0 + nk); k1 = ldsr64(kp1 + nk);
      if constexpr (sst >= 1 && sst + 1 < 8 && sl == 0) kpiece(IC<sst + 1>{});
      if constexpr (sst == 0 && sl < 3) st128(rs, dstA[sl - 1] + BR_A, pk[2 * sl - 2]);
      if constexpr ((sst == 1 || sst == 4) && sl == SNAP) { if (w == 7) dma_sc1<L_FL>(ws, (sst == 1 ? fiA : fiOA) * 128, lds0); }
      if constexpr (sst == 6 && sl == 1) {
        if (keeper) sfor<0, 4>([&](auto kc) AINL { constexpr int k = decltype(kc)::value; dma_sc1<L_ALL + k * 1024>(ws + (FOA + k * 64) * 128, lane * 128, lds0); });
      }
      if constexpr (sst == 2 && sl == 0) dma_sc1<0>(ws, lalo + ((c == 0 && (w == 3 || (w < 3 && lane < 16))) ? 1024 : 0), ladst);
      if constexpr (sst == 2 && sl == 1) { dma3_sc1<1024>(ws + 1024, lalo, ladst); if (w < 3) dma_sc1<3328>(ws + 3328, lalo, ladst); }
      if constexpr ((sst == 4 || sst == 5) && (sl == 1 || sl == 2)) woquarter(IC<0>{}, IC<2 * sl - 2 + sst - 4>{}, IC<L_R + (2 * sl - 2 + sst - 4) * WQ>{});
      if constexpr (sst == 7 && sl == 0) woquarter(IC<1>{}, IC<1>{}, IC<L_RING>{});
      if constexpr (sst == 8) woquarter(IC<1>{}, IC<3>{}, IC<L_RING + AT_STAGE>{});
      if constexpr (sst == 7 && sl > 0) { wf[4 * sl - 4] = ldsr128(wl + WQ + (4 * sl - 4) * 1024); wf[4 * sl - 3] = ldsr128(wl + WQ + (4 * sl - 3) * 1024); }
      if constexpr (sst == 7 && (sl == 1 || sl == 2)) woquarter(IC<1>{}, IC<2 * sl - 2>{}, IC<L_R + (2 * sl - 2) * WQ>{});
      EX2(sa, 10); opa(IC<ha>{}, IC<2>{}); EX2(sa, 12); PIN(sa); SBAR();
      __builtin_amdgcn_s_waitcnt(0xC07F); SBAR();
      oa = smf(v0, sa, 0, oa, idx); SBAR(); EX2(sa, 14); opa(IC<ha>{}, IC<3>{}); PIN(sa); SBAR();
      EX2(sb, 0); opa(IC<ha>{}, IC<4>{}); PIN(sb); SBAR();
      oa = smf(v1, sa, 1, oa, idx); SBAR(); EX2(sb, 2); opa(IC<hb>{}, IC<0>{}); EX2(sb, 4); PIN(sb); SBAR();
      sa = MFMA32(k0, q0[0], f32x16{}); SBAR(); EX2(sb, 6); opa(IC<hb>{}, IC<1>{}); EX2(sb, 8); PIN(sb); SBAR();
      sa = MFMA8(k1, q1[0], sa); SBAR();
      if constexpr (sst >= 1 && sst + 1 < 8 && sl == 0) vpiece(IC<sst + 1>{});
      if constexpr (sst == 0 && sl < 3) st128(rs, dstA[sl - 1] + (BR_A + (cj >= 10 ? 1024 : 2 * UNIT)), pk[2 * sl - 1]);
      if constexpr (sst == 1 && sl == 1) {
        st128(rs, obm, u32x4{~0u, ~0u, ~0u, ~0u}); st64(rs, obt, u32x2{~0u, ~0u});
      }
      if constexpr (sst == 2 && sl == 0) {
        const unsigned to = WS_QKB + BR_A + (unsigned)(256 * c + 32 * w + r) * 16;
        lq = ld128(rs, (ua + hh) * UNIT + to);
        lqt = ld64(rs, us * UNIT + to + 8 * e);
      }
      if constexpr (sst == 1 && sl == 0) {
#pragma unroll
        for (int i = 0; i < 5; ++i) { gx[i] = ((const unsigned*)(x + (size_t)(256 * c + 8 * h + w) * (D_ * 2)))[lane * 5 + i]; gg[i] = ((const unsigned*)gw)[lane * 5 + i]; }
      }
      if constexpr ((sst == 5 || sst == 7) && (sl == 1 || sl == 2)) {
#pragma unroll
        for (int j = (sst == 7 ? 5 : 0) + 3 * sl - 3; j < (sst == 7 ? 5 : 0) + (sl == 1 ? 3 : 5); ++j) {
          if (j % 5 < 4) of[j] = ld128(rs, WS_OA + osrc + j * 1024);
          else { const u32x2 a = ld64(rs, WS_OA + osrc - lane * 8 + j * 1024), b = ld64(rs, WS_OA + osrc - lane * 8 + j * 1024 + 512); of[j] = u32x4{a[0], a[1], b[0], b[1]}; }
        }
        if constexpr (sst == 7 && sl == 2) gate = __uint_as_float(ld32(rs, WS_G + (rt * 64 + tq * 16 + (lane & 15)) * 4));
      }
      if constexpr (sst == 7 && sl > 0) { wf[4 * sl - 2] = ldsr128(wl + WQ + (4 * sl - 2) * 1024); wf[4 * sl - 1] = ldsr128(wl + WQ + (4 * sl - 1) * 1024); }
      v1p = v1;
      const bf16x8 v0n = ldsr128(vp + nv); v1 = ldsr128(vp + nv + 1024);
      EX2(sb, 10); opa(IC<hb>{}, IC<2>{}); EX2(sb, 12); PIN(sb); SBAR();
      ob = smf(v0, sb, 0, ob, idx); SBAR();
      v0 = v0n;
      EX(sb, 14); opa(IC<hb>{}, IC<3>{}); EX(sb, 15); opa(IC<hb>{}, IC<4>{}); PIN(sb); SBAR();
      if constexpr (st == 2 && i == 3) local_attention();
    });
    STAMP(1, 4 + st);
  });
  ob = smf(v1p, sb, 1, ob, idx);
  __builtin_amdgcn_s_setprio(0);
  float ofin[12];
  {
    f32x4* xw = (f32x4*)(lds + L_R + (2 * kh + 1) * WQ + tq * 3072) + lane;
    const f32x4* xr = (const f32x4*)(lds + L_R + (3 - 2 * kh) * WQ + tq * 3072) + lane;
#pragma unroll
    for (int i = 0; i < 3; ++i) xw[i * 64] = f32x4{oa[4 * i], oa[4 * i + 1], oa[4 * i + 2], oa[4 * i + 3]};
    wait_bar<63>();
#pragma unroll
    for (int i = 0; i < 3; ++i) { const f32x4 v = xr[i * 64]; for (int k = 0; k < 4; ++k) ofin[4 * i + k] = ob[4 * i + k] + v[k]; }
  }
  {
    auto rr = __builtin_amdgcn_permlane32_swap(__float_as_uint(ofin[8]), __float_as_uint(ofin[8]), false, false);
    float l = __uint_as_float(rr[1]);
    if (__builtin_expect(__any(!(l > 8.67e-19f && l < 1.15e18f)), 0)) {
      const bf16x8 qa = q0[1]; s16x4 qb = q1[1];
      float m = -INFINITY;
#pragma unroll 1
      for (int pass = 0; pass < 2; ++pass) {
        set_ref(qb, pass ? m : 0.f, hh != e);
        f32x16 o = f32x16{};
#pragma unroll 1
        for (int kt = 0; kt < 64; ++kt) {
          const unsigned to = (unsigned)(32 * kt + r) * 16;
          const bf16x8 a0 = __builtin_bit_cast(bf16x8, ld128(rs, WS_QKB + (80 + ua + hh) * UNIT + to));
          const s16x4 a1 = __builtin_bit_cast(s16x4, ld64(rs, hh == e ? WS_QKB + (80 + us) * UNIT + to + 8 * e : WS_C + blockIdx.x * 4096));
          f32x16 s = MFMA32(a0, qa, f32x16{}); s = MFMA8(a1, qb, s);
          if (pass == 0) { m = fmaxf(m, max16(s)); continue; }
#pragma unroll
          for (int j = 0; j < 16; ++j) EX(s, j);
#pragma unroll
          for (int s2 = 0; s2 < 2; ++s2) {
            const u32x4 vv = ld128(rs, r < 20 ? WS_VB + (h * 64 + kt) * 2048 + (2 * s2 + hh) * 512 + r * 16 : WS_C + blockIdx.x * 4096 + (r == 20 ? 0 : 16));
            o = smf(__builtin_bit_cast(bf16x8, vv), s, s2, o, idx);
          }
        }
        if (pass == 0) m = swap_max(m);
        else {
#pragma unroll
          for (int i = 0; i < 12; ++i) ofin[i] = o[i];
        }
      }
      auto r2 = __builtin_amdgcn_permlane32_swap(__float_as_uint(ofin[8]), __float_as_uint(ofin[8]), false, false);
      l = __uint_as_float(r2[1]);
    }
    const float rl = __builtin_amdgcn_rcpf(l);
    unsigned ow[6];
#pragma unroll
    for (int i = 0; i < 6; ++i) ow[i] = min(cvtpk(ofin[2 * i] * rl, ofin[2 * i + 1] * rl), 0xfffffffeu);
    st128(rs, obm, u32x4{ow[0], ow[1], ow[2], ow[3]});
    st64(rs, obt, u32x2{ow[4], ow[5]});
  }
  SBAR();
  STAMP(1, 12);
#pragma unroll
  for (int f = 12; f < 25; ++f) wf[f] = ldsr128(wl + WQ + f * 1024);
  SBAR();

  f32x4 accB[5];
  u32x4 og[8]; u32x2 ot[4];
  const unsigned ob0 = osrc + SWP * 5120, ob1 = osrc + (SWP ^ 1) * 5120, ot0 = ob0 - lane * 8 + 4096, ot1 = ob1 - lane * 8 + 4096;
#define OBLD(k, r, a, o) "s_bitcmp1_b32 %17, " #k "\n\ts_cbranch_scc0 1f\n\tglobal_load_dwordx4 %" #r ", %" #a ", %16 offset:" #o " sc1\n1:\n\t"
#define OBLT(k, r0, r1, a) "s_bitcmp1_b32 %17, " #k "\n\ts_cbranch_scc0 1f\n\tglobal_load_dwordx2 %" #r0 ", %" #a ", %16 sc1\n\tglobal_load_dwordx2 %" #r1 ", %" #a ", %16 offset:512 sc1\n1:\n\t"
#define OB_ALL OBLD(0, 0, 12, 0) OBLD(1, 1, 12, 1024) OBLD(2, 2, 12, 2048) OBLD(3, 3, 12, 3072) OBLT(4, 8, 9, 14) OBLD(5, 4, 13, 0) OBLD(6, 5, 13, 1024) OBLD(7, 6, 13, 2048) OBLD(8, 7, 13, 3072) OBLT(9, 10, 11, 15)
#define OG(c) c(og[0]), c(og[1]), c(og[2]), c(og[3]), c(og[4]), c(og[5]), c(og[6]), c(og[7]), c(ot[0]), c(ot[1]), c(ot[2]), c(ot[3])
#define OBL1(k, a, o) og[k] = ld128(rs, WS_OB + a + o)
#define OBL2(k, a, o) ot[k] = ld64(rs, WS_OB + a + o)
#define OFR(j) ((j) % 5 < 4 ? og[((j) - (j) / 5) & 7] : u32x4{ot[2 * ((j) / 5)][0], ot[2 * ((j) / 5)][1], ot[2 * ((j) / 5) + 1][0], ot[2 * ((j) / 5) + 1][1]})
#pragma unroll
  for (int j = 0; j < 5; ++j) {
    if (j == 0) TPRIO(3); if (j == 2) TPRIO(2); if (j == 3) TPRIO(1); if (j == 4) TPRIO(0);
#pragma unroll
    for (int n = 0; n < 5; ++n) accA[n] = MFMA16(wf[j * 5 + n], of[5 + j], accA[n]);
    SBAR();
#pragma unroll
    for (int n = 0; n < 5; ++n) wf[j * 5 + n] = ldsr128(wl + (j * 5 + n) * 1024);
    if (j == OB0) { OBL1(0, ob0, 0); OBL1(1, ob0, 1024); OBL1(2, ob0, 2048); }
    if (j == OB1) { OBL1(3, ob0, 3072); OBL2(0, ot0, 0); OBL2(1, ot0, 512); }
    if (j == OB2) { OBL1(4, ob1, 0); OBL1(5, ob1, 1024); OBL1(6, ob1, 2048); }
    if (j == OB3) { OBL1(7, ob1, 3072); OBL2(2, ot1, 0); OBL2(3, ot1, 512); }
    SBAR();
  }
  STAMP(2, 0);
  waitv_bar<12>();
  SBAR();
  const lds_cptr wh = (lds_cptr)lds + L_RING + kh * AT_STAGE + lane * 16;
#pragma unroll
  for (int j = 0; j < 10; ++j) {
    if (j % 5 == 0) {
      unsigned seen = 0;
#pragma unroll
      for (int k = j; k < j + 5; ++k) { const u32x4 v = OFR(k); seen = max(max(seen, v[0]), v[1]); seen = max(max(seen, v[2]), v[3]); }
      if (__any(seen == ~0u)) {
#pragma unroll 1
        for (unsigned spins = 0; spins < (1u << 16); ++spins) {
          unsigned pend = 0;
#pragma unroll
          for (int k = j; k < 10; ++k) { const u32x4 v = OFR(k); pend |= __any(max(max(v[0], v[1]), max(v[2], v[3])) == ~0u) ? (1u << k) : 0u; }
          if (!(pend & (0x1fu << j))) break;
          asm volatile(OB_ALL "s_waitcnt vmcnt(0)" : OG("+v") : "v"(ob0), "v"(ob1), "v"(ot0), "v"(ot1), "s"(ws + WS_OB), "s"(pend) : "memory", "scc");
        }
      }
      if (j == 0) STAMP(2, 1);
    }
    if (j == 0) TPRIO(3); if (j == 3) TPRIO(2); if (j == 6) TPRIO(1); if (j == 8) TPRIO(0);
#pragma unroll
    for (int n = 0; n < 5; ++n) accB[n] = MFMA16(wf[(j % 5) * 5 + n], OFR(j), j == 0 ? f32x4{} : accB[n]);
    if (j < 5) {
#pragma unroll
      for (int n = 0; n < 5; ++n) wf[j * 5 + n] = ldsr128(wh + (j * 5 + n) * 1024);
    }
    SBAR();
  }
  STAMP(2, 2);
#pragma unroll
  for (int n = 0; n < 5; ++n) accB[n] = accA[n] * gate + accB[n] * (1.f - gate);
  {
    char* img = lds + L_R + (2 * kh + 1) * WQ + (tq * 16 + (lane & 15)) * 336 + (lane >> 4) * 16;
#pragma unroll
    for (int n = 0; n < 5; ++n) *(f32x4*)(img + n * 64) = accB[n];
    wait_bar<63>();
#pragma unroll
    for (int it = 0; it < 2; ++it) {
      const int u = tid + 512 * it, row = u / 10, pc = u - 10 * row;
      if (u < 640) {
        const char* a = lds + L_R + WQ + row * 336 + pc * 32;
        const f32x4 v0 = *(const f32x4*)a + *(const f32x4*)(a + 2 * WQ), v1 = *(const f32x4*)(a + 16) + *(const f32x4*)(a + 2 * WQ + 16);
        const u32x4 r4 = u32x4{cvtpk(v0[0], v0[1]), cvtpk(v0[2], v0[3]), cvtpk(v1[0], v1[1]), cvtpk(v1[2], v1[3])};
        char* op = out + (size_t)(rt * 64 + row) * (D_ * 2) + ct * 160 + pc * 16;
        if (FIN == 1) *(u32x4*)op = r4; else __builtin_nontemporal_store(r4, (u32x4*)op);
      }
    }
  }
  STAMP(2, 3);
  DBG_OUT();
  asm volatile("GM_CODE_END:");
  if (keeper && !advanced) {
#pragma unroll 1
    for (int i = 0; i < 4; ++i) wait_flags(flg, FOA + i * 64 + lane, tag);
    if (lane == 0) __hip_atomic_store(flg, tag, RLX_AGENT);
  }
}

extern "C" void kernel_launch(void* const* d_in, const int* in_sizes, int n_in, void* d_out, int out_size, void* d_ws,
                              size_t ws_size, hipStream_t stream) {
  (void)hipFuncSetAttribute((const void*)fused_kernel, hipFuncAttributeMaxDynamicSharedMemorySize, LDS_BYTES);
  fused_kernel<<<256, 512, LDS_BYTES, stream>>>((const char*)d_in[0], (const char*)d_in[1], (const char*)d_in[4], (const char*)d_in[2],
                                                (const char*)d_in[5], (const char*)d_in[6], (char*)d_ws, (char*)d_out);
}
```

```cpp
#include <hip/hip_runtime.h>
#include <hip/hip_bf16.h>
#include <stdint.h>

typedef short bf16x8 __attribute__((ext_vector_type(8)));
typedef short s16x4 __attribute__((ext_vector_type(4)));
typedef __bf16 bfv8 __attribute__((ext_vector_type(8)));
typedef __bf16 bfv2 __attribute__((ext_vector_type(2)));
typedef __bf16 bfv16 __attribute__((ext_vector_type(16)));
typedef float f32x16 __attribute__((ext_vector_type(16)));
typedef float f32x8 __attribute__((ext_vector_type(8)));
typedef float f32x4 __attribute__((ext_vector_type(4)));
typedef unsigned u32x4 __attribute__((ext_vector_type(4)));
typedef unsigned u32x2 __attribute__((ext_vector_type(2)));
typedef unsigned long long u64;
#define LAS __attribute__((address_space(3)))
#define GAS __attribute__((address_space(1)))
#define AINL __attribute__((always_inline))
typedef LAS const char* lds_cptr;
typedef LAS char* lds_ptr;
typedef GAS u64 gu64;

constexpr int D_ = 640, N3_ = 1920;
constexpr u64 TAG_STEP = 0x9E3779B97F4A7C15ull;
constexpr float QSCALE = 0.22360679774997896f * 1.4426950408889634f;
constexpr unsigned OOB = 0x70000000u;

constexpr unsigned UNIT = 2048 * 16;
constexpr unsigned F1A = 512, FG = 768, FOA = 1024, F1B = 2048, NREP = 16;
constexpr unsigned WS_QKB = (2048 + NREP * 256) * 128;
constexpr unsigned WS_VB = WS_QKB + 160 * UNIT;
constexpr unsigned BR_A = 160 * UNIT + 32 * 64 * 2048;
constexpr unsigned WS_OA = WS_QKB + 2 * BR_A;
constexpr unsigned WS_OB = WS_OA + 2048 * 640 * 2;
constexpr unsigned WS_WO = WS_OB + 2048 * 640 * 2;
constexpr unsigned WS_G = WS_WO + 2 * 640 * 640 * 2;
constexpr unsigned WS_C = WS_G + 2048 * 4;
constexpr unsigned WS_END = WS_C + 256 * 4096;

constexpr int L_R = 0, WQ = 25600;
constexpr int L_RING = 106496, AT_HALF = 14336, AT_STAGE = 2 * AT_HALF;
constexpr int L_FL = 104448, L_EP = 98304, L_PF = 81920, L_ALL = 102400, L_Q = 0;
constexpr int L_LK = 51200, LA_UNIT = 4608, L_LV = L_LK + 3 * LA_UNIT;
constexpr int LDS_BYTES = 163840;

constexpr int BST = 2;
#define SBAR() __builtin_amdgcn_sched_barrier(0)
#define PIN(x) asm volatile("" : "+v"(x))
#define MFMA32(a, b, c) __builtin_amdgcn_mfma_f32_32x32x16_bf16(__builtin_bit_cast(bfv8, a), __builtin_bit_cast(bfv8, b), c, 0, 0, 0)
#define MFMA8(a, b, c) __builtin_amdgcn_mfma_f32_32x32x8bf16_1k(a, b, c, 0, 0, 0)
#define MFMA16(a, b, c) __builtin_amdgcn_mfma_f32_16x16x32_bf16(__builtin_bit_cast(bfv8, a), __builtin_bit_cast(bfv8, b), c, 0, 0, 0)
#define RLX_AGENT __ATOMIC_RELAXED, __HIP_MEMORY_SCOPE_AGENT
#define SC1 16

template <int V> struct IC { static constexpr int value = V; };
template <int I, int N, class F> __device__ __forceinline__ void sfor(F&& f) { if constexpr (I < N) { f(IC<I>{}); sfor<I + 1, N>(f); } }
template <int N> __device__ __forceinline__ void wait_bar() { asm volatile("s_waitcnt vmcnt(%0) lgkmcnt(0)\n\ts_barrier" :: "i"(N) : "memory"); }
template <int N> __device__ __forceinline__ void waitv_bar() { asm volatile("s_waitcnt vmcnt(%0)\n\ts_barrier" :: "i"(N) : "memory"); }

__device__ __forceinline__ unsigned cvtpk(float lo, float hi) {
  bfv2 r; r[0] = (__bf16)lo; r[1] = (__bf16)hi;
  return __builtin_bit_cast(unsigned, r);
}
__device__ __forceinline__ float dot2(unsigned a, unsigned b, float c) {
  return __builtin_amdgcn_fdot2_f32_bf16(__builtin_bit_cast(bfv2, a), __builtin_bit_cast(bfv2, b), c, false);
}
template <int OFF> __device__ __forceinline__ void dma(const char* base, unsigned voff, unsigned lds_base) {
  asm volatile("s_add_u32 m0, %2, %3\n\ts_nop 0\n\tglobal_load_lds_dwordx4 %0, %1" :: "v"(voff), "s"(base), "s"(lds_base), "i"(OFF) : "memory", "scc");
}
template <int OFF> __device__ __forceinline__ void dma_sc1(const char* base, unsigned voff, unsigned lds_base) {
  asm volatile("s_add_u32 m0, %2, %3\n\ts_nop 0\n\tglobal_load_lds_dwordx4 %0, %1 sc1" :: "v"(voff), "s"(base), "s"(lds_base), "i"(OFF) : "memory", "scc");
}
template <int OFF> __device__ __forceinline__ void dma2_sc1(const char* base, unsigned voff, unsigned lds_base) {
  asm volatile("s_add_u32 m0, %2, %3\n\ts_nop 0\n\tglobal_load_lds_dwordx4 %0, %1 sc1\n\tglobal_load_lds_dwordx4 %0, %1 offset:1024 sc1"
               :: "v"(voff), "s"(base), "s"(lds_base), "i"(OFF) : "memory", "scc");
}
template <int OFF> __device__ __forceinline__ void dma3_sc1(const char* base, unsigned voff, unsigned lds_base) {
  asm volatile("s_add_u32 m0, %2, %3\n\ts_nop 0\n\tglobal_load_lds_dwordx4 %0, %1 sc1\n\tglobal_load_lds_dwordx4 %0, %1 offset:1024 sc1\n\t"
               "global_load_lds_dwordx4 %0, %1 offset:2048 sc1" :: "v"(voff), "s"(base), "s"(lds_base), "i"(OFF) : "memory", "scc");
}
typedef short v4i16_t __attribute__((ext_vector_type(4)));
__device__ __forceinline__ s16x4 trd(lds_cptr p) {
  return __builtin_bit_cast(s16x4, __builtin_amdgcn_ds_read_tr16_b64_v4i16((LAS v4i16_t*)p));
}
__device__ __forceinline__ bf16x8 cat8(s16x4 a, s16x4 b) { return __builtin_shufflevector(a, b, 0, 1, 2, 3, 4, 5, 6, 7); }
__device__ __forceinline__ bf16x8 ldsr128(lds_cptr p) { return *(const LAS bf16x8*)p; }
__device__ __forceinline__ s16x4 ldsr64(lds_cptr p) { return *(const LAS s16x4*)p; }
__device__ __forceinline__ u32x4 pack8(const f32x16& a, int s, float sc) {
  return u32x4{cvtpk(a[8 * s] * sc, a[8 * s + 1] * sc), cvtpk(a[8 * s + 2] * sc, a[8 * s + 3] * sc),
               cvtpk(a[8 * s + 4] * sc, a[8 * s + 5] * sc), cvtpk(a[8 * s + 6] * sc, a[8 * s + 7] * sc)};
}
__device__ __forceinline__ int swap23(int r) { return (r & 0x13) | ((r & 4) << 1) | ((r & 8) >> 1); }
__device__ __forceinline__ u64 uni64(unsigned lo, unsigned hi) {
  return ((u64)(unsigned)__builtin_amdgcn_readfirstlane(hi) << 32) | (unsigned)__builtin_amdgcn_readfirstlane(lo);
}

typedef __amdgpu_buffer_rsrc_t rsrc_t;
__device__ __forceinline__ void st128(rsrc_t rs, unsigned off, u32x4 v) { __builtin_amdgcn_raw_buffer_store_b128(v, rs, off, 0, SC1); }
__device__ __forceinline__ void st64(rsrc_t rs, unsigned off, u32x2 v) { __builtin_amdgcn_raw_buffer_store_b64(v, rs, off, 0, SC1); }
__device__ __forceinline__ u32x4 ld128(rsrc_t rs, unsigned off) { return __builtin_amdgcn_raw_buffer_load_b128(rs, off, 0, SC1); }
__device__ __forceinline__ u32x2 ld64(rsrc_t rs, unsigned off) { return __builtin_amdgcn_raw_buffer_load_b64(rs, off, 0, SC1); }
__device__ __forceinline__ unsigned ld32(rsrc_t rs, unsigned off) { return __builtin_amdgcn_raw_buffer_load_b32(rs, off, 0, SC1); }

template <int SLEEP = 6>
__device__ __forceinline__ void wait_flags(gu64* flg, unsigned idx, u64 tag) {
  bool ok = false;
#pragma unroll 1
  for (unsigned spins = 0; spins < (1u << 18); ++spins) {
    if (!ok) ok = __hip_atomic_load(flg + idx * 16, RLX_AGENT) == tag;
    if (__all(ok)) break;
    __builtin_amdgcn_s_sleep(SLEEP);
  }
  asm volatile("" ::: "memory");
}
__device__ __forceinline__ void sample_flags(u64& fa, u64& fb, unsigned ia, unsigned ib, const char* base, u64 miss) {
  asm volatile("s_cmp_eq_u64 %5, 0\n\ts_cbranch_scc1 1f\n\tglobal_load_dwordx2 %0, %2, %4 sc1\n\tglobal_load_dwordx2 %1, %3, %4 sc1\n1:"
               : "+v"(fa), "+v"(fb) : "v"(ia * 128), "v"(ib * 128), "s"(base), "s"(miss) : "memory", "scc");
}

__device__ __forceinline__ unsigned o_main(int tok, int h, int hh) {
  const int m = h >> 1;
  return (unsigned)((((tok >> 4) * 20 + 5 * (m >> 2) + (m & 3)) * 64 + (2 * (h & 1) + hh) * 16 + (tok & 15)) * 16);
}
__device__ __forceinline__ unsigned o_tail(int tok, int h) {
  const int m = h >> 1;
  return (unsigned)(((tok >> 4) * 20 + 5 * (m >> 2) + 4) * 1024 + 512 * (m & 1) + ((2 * ((m >> 1) & 1) + (h & 1)) * 16 + (tok & 15)) * 8);
}

#define G1_ORDER "BBBBBBBBBBAAAAAAAAAA"
constexpr int G1_PUB = 14;
constexpr int G1_EP = 12;
constexpr int G1_SAMP = 72, G1_LOOK = 76;
struct G1 {
  int isB[24] = {}, kb[24] = {}, uf[25] = {};
  int lastB = 0;
  int iss_lo[20] = {}, iss_hi[20] = {};
  int wait[20] = {};
  constexpr G1() {
    const char* ord = G1_ORDER;
    int nb = 0, na = 0;
    for (int o = 0; o < 24; ++o) {
      isB[o] = o < 20 && ord[o] == 'B'; kb[o] = o < 20 ? (isB[o] ? nb++ : na++) : 0;
      uf[o + 1] = uf[o] + (o < 20 ? 1 + isB[o] : 0);
      if (isB[o]) lastB = o;
    }
    int pos[60] = {}, cnt = 0, lastst = 0, done = 2 * uf[2];
    for (int d = 0; d < done; ++d) pos[d] = cnt++;
    for (int o = 0; o < 20; ++o) {
      int need = pos[2 * uf[o + 1] - 1];
      if (o == G1_PUB && lastst > need) need = lastst;
      wait[o] = o >= 18 ? 0 : cnt - 1 - need;
      if (o == G1_EP) lastst = cnt++;
      iss_lo[o] = done; iss_hi[o] = 2 * uf[o + 4];
      for (int ks = 0; ks < 4; ++ks) {
        const int per = (iss_hi[o] - iss_lo[o] + 3) / 4;
        for (int z = 0; z < per && done < iss_hi[o]; ++z) pos[done++] = cnt++;
        const int n = 4 * o + ks - 2 - (4 * lastB + 3);
        if (n >= 0 && n < 4 * BST && n % BST == 0) lastst = cnt++;
      }
    }
  }
};
constexpr int g1_slot(int u) { return ((u + 6) % 10) * 16384; }
template <bool VT, class FPiece, class FStart, class FSetup>
__device__ __forceinline__ void g1_body(char* ws, rsrc_t rs, char* lds, int ti, int cj, int w, int lane, u64& tag, bool& early, unsigned& fiKV, unsigned& fiQ,
                                        u32x4 (&pk)[4], unsigned (&dstB)[2], FPiece&& piece, FStart&& att_start, FSetup&& setup) {
  constexpr G1 P;
  static_assert(P.uf[G1_EP - 1] > 20 && G1_EP < G1_PUB && P.uf[4] <= 9, "epoch / prefetch scratch slots");
  const int tg = w & 3, cg = w >> 2;
  const int r = lane & 31, hh = lane >> 5, g16 = (lane >> 4) & 1, q4 = (lane & 15) >> 2, p4 = lane & 3;
  const int tr = VT ? swap23(r) : r;
  unsigned xoff[4];
#pragma unroll
  for (int ks = 0; ks < 4; ++ks) xoff[ks] = (unsigned)((32 * tg + tr) * 128 + (((2 * ks + hh) ^ ((tr >> 1) & 7)) * 16));
  const int psw = ((p4 & 1) << 1) | (p4 >> 1);
  unsigned woff[2];
#pragma unroll
  for (int t2 = 0; t2 < 2; ++t2) woff[t2] = (unsigned)((8 * hh + q4) * 256 + (((2 * cg + t2) ^ q4) * 64) + 32 * g16 + 8 * (VT ? p4 : psw));
  const float sc = (!VT && cj < 5) ? QSCALE : 1.f;
  bf16x8 xf[40], wf[3][2];
  u64 fa, fb;
  f32x16 accB[2], accA[2];
  u64 miss = 1;
  auto look = [&](bool again) AINL {
    asm volatile("s_cmp_eq_u64 %2, 0\n\ts_cbranch_scc1 1f\n\ts_waitcnt vmcnt(0)\n1:" : "+v"(fa), "+v"(fb) : "s"(miss) : "memory", "scc");
    const u64 m = __ballot(fa != tag || fb != tag);
    if (m == 0 && miss != 0) att_start();
    miss = m;
    if (again) sample_flags(fa, fb, fiKV, fiQ, ws + (F1B + 256) * 128, miss);
  };
  sfor<0, 82>([&](auto ic) AINL {
    constexpr int nn = decltype(ic)::value, n = nn - 2;
    constexpr int o = nn >> 2, ks = nn & 3;
    if constexpr (nn < 80) {
      if constexpr (ks == 0) {
        if constexpr (o < 19) waitv_bar<P.wait[o]>();
        if constexpr (o == G1_EP) dma_sc1<L_EP>(ws, 0, (unsigned)(uintptr_t)lds + w * 1024);
        if constexpr (o == G1_PUB) { const u32x2 ep = *(const LAS u32x2*)((lds_cptr)lds + L_EP + w * 1024); tag = uni64(ep[0], ep[1]) + TAG_STEP; }
        if constexpr (o == G1_PUB) { if (w == 0 && lane < NREP) __hip_atomic_store((gu64*)ws + (F1B + lane * 256 + cj * 16 + ti) * 16, tag, RLX_AGENT); }
      }
      if constexpr (nn >= 62 && nn < 70 && !(nn & 1)) setup(IC<(nn - 62) / 2>{});
      if constexpr (nn == G1_SAMP) { fa = 0; fb = 0; sample_flags(fa, fb, fiKV, fiQ, ws + F1B * 128, miss); }
      if constexpr (nn == G1_LOOK) look(true);
      if constexpr (P.isB[o]) xf[4 * P.kb[o] + ks] = ldsr128((lds_cptr)lds + g1_slot(P.uf[o] + 1) + xoff[ks]);
#pragma unroll
      for (int t2 = 0; t2 < 2; ++t2) {
        const lds_cptr wp = (lds_cptr)lds + g1_slot(P.uf[o]) + woff[t2] + ks * 4096;
        wf[nn % 3][t2] = cat8(trd(wp), trd(wp + 1024));
      }
      constexpr int per = (P.iss_hi[o] - P.iss_lo[o] + 3) / 4, d0 = P.iss_lo[o] + per * ks;
      sfor<d0, (d0 + per < P.iss_hi[o] ? d0 + per : P.iss_hi[o])>(piece);
    }
    if constexpr (n >= 0) {
      constexpr int om = n >> 2, xi = 4 * P.kb[om] + (n & 3);
#pragma unroll
      for (int t2 = 0; t2 < 2; ++t2) {
        f32x16& ac = P.isB[om] ? accB[t2] : accA[t2];
        const f32x16 cin = xi == 0 ? f32x16{} : ac;
        ac = VT ? MFMA32(xf[xi], wf[n % 3][t2], cin) : MFMA32(wf[n % 3][t2], xf[xi], cin);
      }
    }
    SBAR();
    constexpr int nb = n - (4 * P.lastB + 3);
    if constexpr (nb == 0) {
#pragma unroll
      for (int i = 0; i < 4; ++i) { pk[i] = VT ? pack8(accB[i >> 1], i & 1, 1.f) : pack8(accB[i >> 1], i & 1, sc); PIN(pk[i]); }
      int ln = lane; PIN(ln);
#pragma unroll
      for (int t2 = 0; t2 < 2; ++t2) {
        if (VT) {
          const int F = (cj - 10) * 128 + 64 * cg + 32 * t2 + (ln & 31), hd = F / 20;
          int d = F - 20 * hd - 4 * (hd & 1); if (d < 0) d += 20;
          dstB[t2] = WS_VB + (unsigned)((hd * 64 + 4 * ti + tg) * 2048 + (ln >> 5) * 512 + d * 16);
        } else {
          dstB[t2] = WS_QKB + (unsigned)(cj * 16 + 8 * cg + 4 * t2 + (ln >> 5)) * UNIT + (unsigned)(128 * ti + 32 * tg + (ln & 31)) * 16;
        }
      }
    }
    if constexpr (nb >= 0 && nb < 4 * BST && nb % BST == 0) {
      constexpr int i = nb / BST;
      st128(rs, dstB[i >> 1] + (i & 1) * (VT ? 1024 : 2 * UNIT), pk[i]);
      SBAR();
    }
  });
  look(false);
  early = miss == 0;
#pragma unroll
  for (int i = 0; i < 4; ++i) pk[i] = pack8(accA[i >> 1], i & 1, 1.f);
}

__device__ __forceinline__ f32x16 smf(bf16x8 a, const f32x16& p, int s, f32x16 o, int idx) {
  f32x8 b = s ? __builtin_shufflevector(p, p, 8, 9, 10, 11, 12, 13, 14, 15) : __builtin_shufflevector(p, p, 0, 1, 2, 3, 4, 5, 6, 7);
  return __builtin_amdgcn_smfmac_f32_32x32x32_bf16(__builtin_bit_cast(bfv8, a), __builtin_bit_cast(bfv16, b), o, idx, 0, 0);
}
__device__ __forceinline__ float max16(const f32x16& s) {
  float a = fmaxf(fmaxf(s[0], s[1]), s[2]), b = fmaxf(fmaxf(s[3], s[4]), s[5]);
  a = fmaxf(fmaxf(a, s[6]), s[7]); b = fmaxf(fmaxf(b, s[8]), s[9]);
  a = fmaxf(fmaxf(a, s[10]), s[11]); b = fmaxf(fmaxf(b, s[12]), s[13]);
  a = fmaxf(fmaxf(a, s[14]), s[15]);
  return fmaxf(a, b);
}
__device__ __forceinline__ float swap_max(float m) {
  auto rr = __builtin_amdgcn_permlane32_swap(__float_as_uint(m), __float_as_uint(m), false, false);
  return fmaxf(__uint_as_float(rr[0]), __uint_as_float(rr[1]));
}
__device__ __forceinline__ float swap_add(float m) {
  auto rr = __builtin_amdgcn_permlane32_swap(__float_as_uint(m), __float_as_uint(m), false, false);
  return __uint_as_float(rr[0]) + __uint_as_float(rr[1]);
}
__device__ __forceinline__ void set_ref(s16x4& q1, float m, bool spare) {
  const unsigned a = __float_as_uint(-m) & 0xffff0000u;
  const float r1 = -m - __uint_as_float(a);
  const unsigned b = __float_as_uint(r1) & 0xffff0000u;
  const unsigned c = __float_as_uint(r1 - __uint_as_float(b));
  if (spare) q1 = __builtin_bit_cast(s16x4, u32x2{(a >> 16) | b, c >> 16});
}

__global__ __launch_bounds__(512, 2) void fused_kernel(const char* __restrict__ x, const char* __restrict__ wA, const char* __restrict__ wB,
                                                       const char* __restrict__ woA, const char* __restrict__ woB,
                                                       const char* __restrict__ gw, char* ws, char* __restrict__ out) {
  extern __shared__ __attribute__((aligned(16))) char lds[];
  const int tid = threadIdx.x, lane = tid & 63;
  const int w = __builtin_amdgcn_readfirstlane(tid >> 6);
  const int xcd = blockIdx.x & 7, slot = blockIdx.x >> 3;
  const unsigned lds0 = (unsigned)(uintptr_t)lds;
  gu64* flg = (gu64*)ws;
  const rsrc_t rs = __builtin_amdgcn_make_buffer_rsrc(ws, 0, WS_END, 0x00020000);
  const int ti = 8 * (xcd & 1) + (slot & 7);
  const int cj = 4 * (xcd >> 1) + (slot >> 3);
  const unsigned ldpf = lds0 + (cj < 15 ? L_PF : L_RING);
  if (w == 6) {
    u64 pc; unsigned len;
    asm volatile("s_getpc_b64 %0\nGM_CODE_BEG:\n\ts_mov_b32 %1, GM_CODE_END-GM_CODE_BEG" : "=s"(pc), "=s"(len));
    asm volatile("s_add_u32 m0, %2, %3\n\ts_nop 0\n\tglobal_load_lds_dword %0, %1"
                 :: "v"(min((unsigned)((slot & 7) * 64 + lane) * 128, len)), "s"(pc), "s"(ldpf), "i"(0) : "memory", "scc");
  }
  const int rot = ((slot & 7) * 5 / 4 + (slot >> 3) * 5 / 2 + xcd) % 10;
  unsigned xlo[2], wlo[2];
#pragma unroll
  for (int i = 0; i < 2; ++i) {
    const int row = 16 * w + 8 * i + (lane >> 3);
    xlo[i] = (unsigned)(row * (D_ * 2) + (((lane & 7) ^ ((row >> 1) & 7)) * 16));
    wlo[i] = (unsigned)((8 * w + 4 * i + (lane >> 4)) * (N3_ * 2) + (((lane & 15) ^ (4 * (lane >> 4))) * 16));
  }
  const char* xb = x + (size_t)(128 * ti) * (D_ * 2);
  const char* wBb = wB + cj * 256;
  const char* wAb = wA + cj * 256;
  const unsigned ldw = lds0 + w * 2048;
  auto piece = [&](auto dc) AINL {
    constexpr G1 P;
    constexpr int d = decltype(dc)::value, u = d >> 1, base = g1_slot(u) + (d & 1) * 1024;
    constexpr int o = [&]() { int q = 0; while (P.uf[q + 1] <= u) ++q; return q; }();
    int kb = P.kb[o] + rot; if (kb >= 10) kb -= 10;
    if constexpr (u != P.uf[o]) dma<base>(xb + kb * 128, xlo[d & 1], ldw);
    else dma<base>((P.isB[o] ? wBb : wAb) + (size_t)kb * (64 * N3_ * 2), wlo[d & 1], ldw);
  };
  if (cj < 15) {
    sfor<0, 8>(piece);
  }
  SBAR();
  u64 tag;
  const int h = 4 * xcd + (slot & 3), c = slot >> 2;
  const int e = h & 1, ua = 5 * (h >> 1) + 3 * e, us = 5 * (h >> 1) + 2;
  const int jlo = (5 * (h >> 1) + 2 * e) >> 4, jhi = (5 * (h >> 1) + 2 * e + 2) >> 4;
  const int kh = w >> 2, tq = w & 3;
  const char* kbase = ws + WS_QKB + (size_t)(80 + (tq >= 2 ? us : ua + tq)) * UNIT + 1024 * kh * 16;
  if (w == 5) { const unsigned cv = (lane & 1) ? 0u : 0x3f803f80u; st128(rs, lane < 8 ? WS_C + blockIdx.x * 4096 + (lane >> 1) * 1024 + (lane & 1) * 16 : OOB, u32x4{cv, cv, cv, cv}); }
  const unsigned ldr = lds0 + kh * AT_HALF + tq * 2048;
  unsigned vlo, vstr;
  auto kpiece = [&](auto stc) AINL {
    constexpr int st = decltype(stc)::value;
    if (tq < 3) dma2_sc1<L_RING + (st & 1) * AT_STAGE>(kbase + st * 2048, lane * 16, ldr);
  };
  auto vpiece = [&](auto stc) AINL {
    constexpr int st = decltype(stc)::value;
    dma2_sc1<L_RING + (st & 1) * AT_STAGE + 6144>(ws, vlo + st * vstr, ldr);
  };
  unsigned fiKV, fiQ;
  auto att_start = [&]() AINL {
    kpiece(IC<0>{});
    if (tq == 3) {
      const char* qb = ws + WS_QKB + (256 * c + 128 * kh) * 16;
      dma2_sc1<L_Q>(qb + (size_t)ua * UNIT, lane * 16, lds0 + kh * 2048);
      dma2_sc1<L_Q + 4096>(qb + (size_t)(ua + 1) * UNIT, lane * 16, lds0 + kh * 2048);
      dma2_sc1<L_Q + 8192>(qb + (size_t)us * UNIT, lane * 16, lds0 + kh * 2048);
    }
    vpiece(IC<0>{});
  };

  const int hh = lane >> 5, r = lane & 31;
  const int rt = 4 * xcd + (slot & 3), ct = slot >> 2;
  const int SWP = ct & 1;
  const char* wob = ws + WS_WO + (size_t)ct * 102400 + w * 3072;
  auto woquarter = [&](auto brc, auto qc, auto dc) AINL {
    constexpr int br = decltype(brc)::value, q = decltype(qc)::value, d = decltype(dc)::value;
    const int off = br * 819200 + (q ^ (br & SWP)) * WQ;
    dma3_sc1<d>(wob + off, lane * 16, lds0 + w * 3072);
    if (w == 0) dma_sc1<d + 24576>(wob + off + 24576, lane * 16, lds0);
  };
  const unsigned ladst = lds0 + (w < 3 ? L_LK + w * LA_UNIT : L_LV + min(4 * (w - 3), 13) * 1024);
  unsigned osrc, fiA, obm, obt, lalo; int idx;
  lds_cptr kp0, kp1, vp, qp;
  f32x16 oa, ob;
  auto setup = [&](auto pc) AINL {
    constexpr int p = decltype(pc)::value;
    int ln = lane; PIN(ln);
    const int hh = ln >> 5, r = ln & 31;
    if constexpr (p == 0) {
      const unsigned crow = WS_C + blockIdx.x * 4096 + (r == 20 ? 0 : 16);
      vlo = r < 20 ? WS_VB + (h * 64 + 32 * kh + tq) * 2048 + ln * 16 : crow; vstr = r < 20 ? 8192u : 0u;
      const unsigned lab = w < 3 ? WS_QKB + BR_A + (80 + (w == 2 ? us : ua + w)) * UNIT + (256 * c - 16) * 16
                                 : WS_VB + BR_A + (h * 64 + 8 * c) * 2048 - 1024 + min(4 * (w - 3), 13) * 1024;
      lalo = (w < 3 || r < 20) ? lab + ln * 16 : crow;
      osrc = (unsigned)((rt * 4 + tq) * 20 + 10 * kh) * 1024 + ln * 16;
      PIN(vlo); PIN(vstr); PIN(lalo); PIN(osrc);
    }
    if constexpr (p == 1) {
      fiA = ln < 18 ? F1A + (5 * (ln / 6) + (((ln / 3) & 1) ? jhi : jlo)) * 16 + max(2 * c - 1 + ln % 3, 0) : FG + (ln == 18 ? ct : 8 + ct);
      obm = WS_OB + o_main(256 * c + 64 * tq + 32 * kh + r, h, hh); obt = (hh ? OOB : 0u) + WS_OB + o_tail(256 * c + 64 * tq + 32 * kh + r, h);
      PIN(fiA); PIN(obm); PIN(obt);
    }
    if constexpr (p == 2) {
      const lds_cptr ring = (lds_cptr)lds + L_RING + kh * AT_HALF;
      kp0 = ring + hh * 2048 + r * 16;
      kp1 = (hh == e) ? ring + 2 * 2048 + r * 16 + 8 * hh : ring + 6144 + 20 * 16;
      vp = ring + 6144 + hh * 512 + r * 16;
      qp = (lds_cptr)lds + L_Q + (64 * tq + r) * 16;
      idx = 0xDDDD;
      PIN(kp0); PIN(kp1); PIN(vp); PIN(qp); PIN(idx);
    }
    if constexpr (p == 3) {
      oa = f32x16{}; ob = f32x16{}; PIN(oa); PIN(ob);
      fiKV = (5 + 5 * hh + ((ln & 16) ? jhi : jlo)) * 16 + (ln & 15); fiQ = ((ln & 1) ? jhi : jlo) * 16 + 2 * c + ((ln >> 1) & 1);
      PIN(fiKV); PIN(fiQ);
    }
  };

  bool early = false;
  u32x4 pk[4]; unsigned dstA[2];
  if (cj < 10) g1_body<false>(ws, rs, lds, ti, cj, w, lane, tag, early, fiKV, fiQ, pk, dstA, piece, att_start, setup);
  else if (cj < 15) g1_body<true>(ws, rs, lds, ti, cj, w, lane, tag, early, fiKV, fiQ, pk, dstA, piece, att_start, setup);
  else {
    const char* wo = ((ti & 8) ? woB : woA) + (size_t)(80 * w) * (D_ * 2) + (ti & 7) * 160;
#pragma unroll 1
    for (int p = 0; p < 13; ++p) {
      const int s0 = min(64 * p, 736), sl = s0 + lane;
      dma<0>(wo, (unsigned)((sl / 10) * (D_ * 2) + (sl % 10) * 16), lds0 + w * 12800 + s0 * 16);
    }
    const u64 ep = __hip_atomic_load(flg, RLX_AGENT);
    tag = uni64((unsigned)ep, (unsigned)(ep >> 32)) + TAG_STEP;
    wait_bar<0>();
    const int g4 = lane >> 4, rl = 320 * kh + 4 * (g4 & 1) + ((lane & 15) >> 2);
#pragma unroll
    for (int ff = 0; ff < 13; ++ff) {
      const int f = min(tq + 4 * ff, 49), j = f / 5, n = f - 5 * j;
      const bool mn = j % 5 < 4;
      const int row = rl + 160 * (j / 5) + (mn ? 40 * (j % 5) + 24 * (g4 >> 1) : 80 * (g4 >> 1) + 16);
      const lds_cptr p = (lds_cptr)lds + row * 160 + n * 32 + 8 * (lane & 3);
      st128(rs, WS_WO + (unsigned)(ti * 100 + kh * 50 + f) * 1024 + lane * 16, __builtin_bit_cast(u32x4, cat8(trd(p), trd(p + (mn ? 8 : 40) * 160))));
    }
    wait_bar<0>();
    if (tid == 0) __hip_atomic_store(flg + (FG + ti) * 16, tag, RLX_AGENT);
    if (w == 0) { wait_flags<16>(flg, F1B + (NREP - 1) * 256 + jlo * 16 + 2 * c, tag); }
    wait_bar<63>();
    sfor<0, 4>(setup);
    dstA[0] = OOB; dstA[1] = OOB;
#pragma unroll
    for (int i = 0; i < 4; ++i) pk[i] = u32x4{0u, 0u, 0u, 0u};
  }
  const unsigned flag1 = F1A + cj * 16 + ti;

  if (!early) {
    bool oka = false, okb = false;
#pragma unroll 1
    for (unsigned spins = 0; spins < (1u << 18); ++spins) {
      const unsigned rp = F1B + (2 + min(spins, NREP - 4)) * 256;
      const u64 a = __hip_atomic_load(flg + (rp + (oka ? fiQ : fiKV)) * 16, RLX_AGENT), b = __hip_atomic_load(flg + (rp + fiQ) * 16, RLX_AGENT);
      oka = oka || a == tag; okb = okb || b == tag;
      if (__all(oka && okb)) break;
      __builtin_amdgcn_s_sleep(2);
    }
    asm volatile("" ::: "memory");
    att_start();
  }
  wait_bar<0>();
  float gate;
  const unsigned fiOA = FOA + (rt >> 2) * 32 + r;
  u32x4 lq; u32x2 lqt;
  f32x16 sa, sb;
  bf16x8 q0[2]; s16x4 q1[2];
#pragma unroll
  for (int t = 0; t < 2; ++t) {
    q0[t] = ldsr128(qp + hh * 4096 + ((t ^ kh ^ 1) * 512));
    q1[t] = ldsr64(qp + 8192 + 8 * e + ((t ^ kh ^ 1) * 512));
  }
  const bf16x8 kr0 = ldsr128(kp0 - kh * AT_HALF); const s16x4 kr1 = ldsr64(kp1 - kh * AT_HALF);
  bf16x8 k0 = ldsr128(kp0); s16x4 k1 = ldsr64(kp1);
  bf16x8 v0 = ldsr128(vp), v1 = ldsr128(vp + 1024);
  bf16x8 v1p = v1;
  SBAR();
  {
    set_ref(q1[0], 0.f, hh != e); set_ref(q1[1], 0.f, hh != e);
    f32x16 s = MFMA32(kr0, q0[0], f32x16{}), t = MFMA32(kr0, q0[1], f32x16{});
    s = MFMA8(kr1, q1[0], s); t = MFMA8(kr1, q1[1], t);
    sa = MFMA32(k0, q0[0], f32x16{});
    SBAR();
    kpiece(IC<1>{}); vpiece(IC<1>{});
    SBAR();
    set_ref(q1[0], swap_max(max16(s)), hh != e);
    sa = MFMA8(k1, q1[0], sa);
    SBAR();
    set_ref(q1[1], swap_max(max16(t)), hh != e);
  }
  const bool keeper = blockIdx.x == 0 && w == 7;
  bool advanced = false;
  u32x4 of[10];
  unsigned gx[5], gg[5]; float gs;

  auto local_attention = [&]() AINL {
    const int tok = 256 * c + 32 * w + r;
    const lds_cptr lk = (lds_cptr)lds + L_LK + (32 * w + r) * 16, lv = (lds_cptr)lds + L_LV + (4 * w + hh) * 512 + r * 16;
    const bf16x8 qa = __builtin_bit_cast(bf16x8, lq);
    const s16x4 qb = __builtin_bit_cast(s16x4, (hh == e) ? lqt : u32x2{0u, 0u});
    f32x16 sx = MFMA32(ldsr128(lk + hh * LA_UNIT), qa, f32x16{}), sy = MFMA32(ldsr128(lk + hh * LA_UNIT + 512), qa, f32x16{});
    sx = MFMA8(ldsr64(lk + 2 * LA_UNIT + 8 * e), qb, sx); sy = MFMA8(ldsr64(lk + 2 * LA_UNIT + 8 * e + 512), qb, sy);
    const bf16x8 va = ldsr128(lv), vb = ldsr128(lv + 1024), vc = ldsr128(lv + 2048);
    const u64 edge = (c == 0 && w == 0) ? 0ull : ~0ull;
    const float ninf = -INFINITY;
    sfor<0, 24>([&](auto jc) AINL {
      constexpr int j = decltype(jc)::value;
      constexpr u64 mk = []() { u64 m = 0; for (int l = 0; l < 64; ++l) { const int d = (l & 31) + 16 - ((j & 3) + 8 * (j >> 2) + 4 * (l >> 5)); if (d >= 0 && d <= 14 && !(d & 1)) m |= 1ull << l; } return m; }();
      const bool ok = __builtin_amdgcn_inverse_ballot_w64(j < 8 ? mk & edge : mk);
      if constexpr (j < 16) sx[j] = ok ? sx[j] : ninf; else sy[j - 16] = ok ? sy[j - 16] : ninf;
    });
    float m = fmaxf(fmaxf(max16(sx), sy[0]), sy[1]);
    m = fmaxf(fmaxf(m, sy[2]), sy[3]); m = fmaxf(fmaxf(m, sy[4]), sy[5]); m = -QSCALE * swap_max(fmaxf(fmaxf(m, sy[6]), sy[7]));
#pragma unroll
    for (int j = 0; j < 16; ++j) sx[j] = __builtin_amdgcn_exp2f(__builtin_fmaf(sx[j], QSCALE, m));
#pragma unroll
    for (int j = 0; j < 8; ++j) sy[j] = __builtin_amdgcn_exp2f(__builtin_fmaf(sy[j], QSCALE, m));
    f32x16 o = smf(va, sx, 0, f32x16{}, idx); o = smf(vb, sx, 1, o, idx); o = smf(vc, sy, 0, o, idx);
    auto lr = __builtin_amdgcn_permlane32_swap(__float_as_uint(o[8]), __float_as_uint(o[8]), false, false);
    const float rl = __builtin_amdgcn_rcpf(__uint_as_float(lr[1]));
    st128(rs, WS_OA + o_main(tok, h, hh), pack8(o, 0, rl));
    st64(rs, hh ? OOB : WS_OA + o_tail(tok, h), u32x2{cvtpk(o[8] * rl, o[9] * rl), cvtpk(o[10] * rl, o[11] * rl)});
    gs += __builtin_bit_cast(float, __builtin_amdgcn_update_dpp(0, __builtin_bit_cast(int, gs), 0xB1, 0xf, 0xf, true));
    gs += __builtin_bit_cast(float, __builtin_amdgcn_update_dpp(0, __builtin_bit_cast(int, gs), 0x4E, 0xf, 0xf, true));
    gs += __builtin_bit_cast(float, __builtin_amdgcn_update_dpp(0, __builtin_bit_cast(int, gs), 0x141, 0xf, 0xf, true));
    gs += __builtin_bit_cast(float, __builtin_amdgcn_update_dpp(0, __builtin_bit_cast(int, gs), 0x140, 0xf, 0xf, true));
    { auto g2 = __builtin_amdgcn_permlane16_swap(__float_as_uint(gs), __float_as_uint(gs), false, false); gs = __uint_as_float(g2[0]) + __uint_as_float(g2[1]); }
    gs = swap_add(gs);
    const float gv = __builtin_amdgcn_rcpf(1.f + __builtin_amdgcn_exp2f(-1.4426950408889634f * gs));
    __builtin_amdgcn_raw_buffer_store_b32(__float_as_uint(gv), rs, lane == 0 ? WS_G + (256 * c + 8 * h + w) * 4 : OOB, 0, SC1);
    SBAR();
  };

  const lds_cptr wl = (lds_cptr)lds + L_R + kh * (2 * WQ) + lane * 16;
  f32x4 accA[5];
  bf16x8 wq[2][5], wf[25];
  auto opa = [&](auto hc, auto gc) AINL {
    constexpr int hs = decltype(hc)::value, g = decltype(gc)::value;
    if constexpr (hs >= 0 && hs <= 5) SBAR();
    if constexpr (hs >= 1 && hs <= 5) accA[g] = MFMA16(wq[(hs - 1) & 1][g], of[hs - 1], hs == 1 ? f32x4{} : accA[g]);
    if constexpr (hs >= 0 && hs < 5) wq[hs & 1][g] = ldsr128(wl + (hs * 5 + g) * 1024);
    if constexpr (hs >= 0 && hs <= 5) SBAR();
  };
#define EX(S, j) S[j] = __builtin_amdgcn_exp2f(S[j])
#define EX2(S, j) do { EX(S, j); EX(S, j + 1); } while (0)
  sfor<0, 8>([&](auto stc) AINL {
    constexpr int st = decltype(stc)::value;
    constexpr int cur = (st & 1) * AT_STAGE, nxt = ((st + 1) & 1) * AT_STAGE;
    sfor<0, 4>([&](auto ic) AINL {
      constexpr int i = decltype(ic)::value;
      constexpr int nk = (i < 3) ? cur + (i + 1) * 512 : nxt, nv = (i < 3) ? cur + (i + 1) * 2048 : nxt;
      constexpr int sl = (i + 1) & 3, sst = (i == 3) ? st + 1 : st;
      constexpr int ha = st == 6 ? 2 * i : -9, hb = st == 6 ? 2 * i + 1 : -9;
      SBAR();
      EX2(sa, 0); PIN(sa); SBAR();
      if constexpr (st + i > 0) ob = smf(v1p, sb, 1, ob, idx);
      SBAR(); EX2(sa, 2); opa(IC<ha>{}, IC<0>{}); EX2(sa, 4); PIN(sa); SBAR();
      sb = MFMA32(k0, q0[1], f32x16{}); SBAR(); EX2(sa, 6); opa(IC<ha>{}, IC<1>{}); EX2(sa, 8); PIN(sa); SBAR();
      sb = MFMA8(k1, q1[1], sb); SBAR();
      if constexpr (i == 3) {
        wait_bar<0>();
        if constexpr (st == 0) { if (tid == 0) __hip_atomic_store(flg + flag1 * 16, tag, RLX_AGENT); }
        if constexpr (st == 2) {
          PIN(lq); PIN(lqt);
        }
        if constexpr (st == 1) {
          gs = 0.f;
#pragma unroll
          for (int i = 0; i < 5; ++i) gs = dot2(gx[i], gg[i], gs);
          PIN(gs);
        }
        if constexpr (st == 3) { if (tid == 0) __hip_atomic_store(flg + (FOA + c * 32 + h) * 16, tag, RLX_AGENT); }
        if constexpr (st == 5 || st == 7) {
#pragma unroll
          for (int j = 0; j < 5; ++j) PIN(of[(st == 7 ? 5 : 0) + j]);
          if constexpr (st == 7) PIN(gate);
        }
        if constexpr (st == 6) {
          if (keeper) {
            const lds_cptr p = (lds_cptr)lds + L_ALL + lane * 16;
            advanced = __all(*(const LAS u64*)p == tag && *(const LAS u64*)(p + 1024) == tag && *(const LAS u64*)(p + 2048) == tag && *(const LAS u64*)(p + 3072) == tag);
            if (advanced && lane == 0) __hip_atomic_store(flg, tag, RLX_AGENT);
          }
        }
        if constexpr (st == 1 || st == 4) {
          if (!__all(*(const LAS u64*)((lds_cptr)lds + L_FL + lane * 16) == tag)) wait_flags(flg, st == 1 ? fiA : fiOA, tag);
        }
      }
      __builtin_amdgcn_s_setprio(3 - sl);
      k0 = ldsr128(kp0 + nk); k1 = ldsr64(kp1 + nk);
      if constexpr (sst >= 1 && sst + 1 < 8 && sl == 0) kpiece(IC<sst + 1>{});
      if constexpr (sst == 0 && sl < 3) st128(rs, dstA[sl - 1] + BR_A, pk[2 * sl - 2]);
      if constexpr ((sst == 1 || sst == 4) && sl == 3) { if (w == 7) dma_sc1<L_FL>(ws, (sst == 1 ? fiA : fiOA) * 128, lds0); }
      if constexpr (sst == 6 && sl == 1) {
        if (keeper) sfor<0, 4>([&](auto kc) AINL { constexpr int k = decltype(kc)::value; dma_sc1<L_ALL + k * 1024>(ws + (FOA + k * 64) * 128, lane * 128, lds0); });
      }
      if constexpr (sst == 2 && sl == 0) dma_sc1<0>(ws, lalo + ((c == 0 && (w == 3 || (w < 3 && lane < 16))) ? 1024 : 0), ladst);
      if constexpr (sst == 2 && sl == 1) { dma3_sc1<1024>(ws + 1024, lalo, ladst); if (w < 3) dma_sc1<3328>(ws + 3328, lalo, ladst); }
      if constexpr ((sst == 4 || sst == 5) && (sl == 1 || sl == 2)) woquarter(IC<0>{}, IC<2 * sl - 2 + sst - 4>{}, IC<L_R + (2 * sl - 2 + sst - 4) * WQ>{});
      if constexpr (sst == 7 && sl == 0) woquarter(IC<1>{}, IC<1>{}, IC<L_RING>{});
      if constexpr (sst == 8) woquarter(IC<1>{}, IC<3>{}, IC<L_RING + AT_STAGE>{});
      if constexpr (sst == 7 && sl > 0) { wf[4 * sl - 4] = ldsr128(wl + WQ + (4 * sl - 4) * 1024); wf[4 * sl - 3] = ldsr128(wl + WQ + (4 * sl - 3) * 1024); }
      if constexpr (sst == 7 && (sl == 1 || sl == 2)) woquarter(IC<1>{}, IC<2 * sl - 2>{}, IC<L_R + (2 * sl - 2) * WQ>{});
      EX2(sa, 10); opa(IC<ha>{}, IC<2>{}); EX2(sa, 12); PIN(sa); SBAR();
      __builtin_amdgcn_s_waitcnt(0xC07F); SBAR();
      oa = smf(v0, sa, 0, oa, idx); SBAR(); EX2(sa, 14); opa(IC<ha>{}, IC<3>{}); PIN(sa); SBAR();
      EX2(sb, 0); opa(IC<ha>{}, IC<4>{}); PIN(sb); SBAR();
      oa = smf(v1, sa, 1, oa, idx); SBAR(); EX2(sb, 2); opa(IC<hb>{}, IC<0>{}); EX2(sb, 4); PIN(sb); SBAR();
      sa = MFMA32(k0, q0[0], f32x16{}); SBAR(); EX2(sb, 6); opa(IC<hb>{}, IC<1>{}); EX2(sb, 8); PIN(sb); SBAR();
      sa = MFMA8(k1, q1[0], sa); SBAR();
      if constexpr (sst >= 1 && sst + 1 < 8 && sl == 0) vpiece(IC<sst + 1>{});
      if constexpr (sst == 0 && sl < 3) st128(rs, dstA[sl - 1] + (BR_A + (cj >= 10 ? 1024 : 2 * UNIT)), pk[2 * sl - 1]);
      if constexpr (sst == 1 && sl == 1) {
        st128(rs, obm, u32x4{~0u, ~0u, ~0u, ~0u}); st64(rs, obt, u32x2{~0u, ~0u});
      }
      if constexpr (sst == 2 && sl == 0) {
        const unsigned to = WS_QKB + BR_A + (unsigned)(256 * c + 32 * w + r) * 16;
        lq = ld128(rs, (ua + hh) * UNIT + to);
        lqt = ld64(rs, us * UNIT + to + 8 * e);
      }
      if constexpr (sst == 1 && sl == 0) {
#pragma unroll
        for (int i = 0; i < 5; ++i) { gx[i] = ((const unsigned*)(x + (size_t)(256 * c + 8 * h + w) * (D_ * 2)))[lane * 5 + i]; gg[i] = ((const unsigned*)gw)[lane * 5 + i]; }
      }
      if constexpr ((sst == 5 || sst == 7) && (sl == 1 || sl == 2)) {
#pragma unroll
        for (int j = (sst == 7 ? 5 : 0) + 3 * sl - 3; j < (sst == 7 ? 5 : 0) + (sl == 1 ? 3 : 5); ++j) {
          if (j % 5 < 4) of[j] = ld128(rs, WS_OA + osrc + j * 1024);
          else { const u32x2 a = ld64(rs, WS_OA + osrc - lane * 8 + j * 1024), b = ld64(rs, WS_OA + osrc - lane * 8 + j * 1024 + 512); of[j] = u32x4{a[0], a[1], b[0], b[1]}; }
        }
        if constexpr (sst == 7 && sl == 2) gate = __uint_as_float(ld32(rs, WS_G + (rt * 64 + tq * 16 + (lane & 15)) * 4));
      }
      if constexpr (sst == 7 && sl > 0) { wf[4 * sl - 2] = ldsr128(wl + WQ + (4 * sl - 2) * 1024); wf[4 * sl - 1] = ldsr128(wl + WQ + (4 * sl - 1) * 1024); }
      v1p = v1;
      const bf16x8 v0n = ldsr128(vp + nv); v1 = ldsr128(vp + nv + 1024);
      EX2(sb, 10); opa(IC<hb>{}, IC<2>{}); EX2(sb, 12); PIN(sb); SBAR();
      ob = smf(v0, sb, 0, ob, idx); SBAR();
      v0 = v0n;
      EX(sb, 14); opa(IC<hb>{}, IC<3>{}); EX(sb, 15); opa(IC<hb>{}, IC<4>{}); PIN(sb); SBAR();
      if constexpr (st == 2 && i == 3) local_attention();
    });
  });
  ob = smf(v1p, sb, 1, ob, idx);
  __builtin_amdgcn_s_setprio(0);
  float ofin[12];
  {
    f32x4* xw = (f32x4*)(lds + L_R + (2 * kh + 1) * WQ + tq * 3072) + lane;
    const f32x4* xr = (const f32x4*)(lds + L_R + (3 - 2 * kh) * WQ + tq * 3072) + lane;
#pragma unroll
    for (int i = 0; i < 3; ++i) xw[i * 64] = f32x4{oa[4 * i], oa[4 * i + 1], oa[4 * i + 2], oa[4 * i + 3]};
    wait_bar<63>();
#pragma unroll
    for (int i = 0; i < 3; ++i) { const f32x4 v = xr[i * 64]; for (int k = 0; k < 4; ++k) ofin[4 * i + k] = ob[4 * i + k] + v[k]; }
  }
  {
    auto rr = __builtin_amdgcn_permlane32_swap(__float_as_uint(ofin[8]), __float_as_uint(ofin[8]), false, false);
    float l = __uint_as_float(rr[1]);
    if (__builtin_expect(__any(!(l > 8.67e-19f && l < 1.15e18f)), 0)) {
      const bf16x8 qa = q0[1]; s16x4 qb = q1[1];
      float m = -INFINITY;
#pragma unroll 1
      for (int pass = 0; pass < 2; ++pass) {
        set_ref(qb, pass ? m : 0.f, hh != e);
        f32x16 o = f32x16{};
#pragma unroll 1
        for (int kt = 0; kt < 64; ++kt) {
          const unsigned to = (unsigned)(32 * kt + r) * 16;
          const bf16x8 a0 = __builtin_bit_cast(bf16x8, ld128(rs, WS_QKB + (80 + ua + hh) * UNIT + to));
          const s16x4 a1 = __builtin_bit_cast(s16x4, ld64(rs, hh == e ? WS_QKB + (80 + us) * UNIT + to + 8 * e : WS_C + blockIdx.x * 4096));
          f32x16 s = MFMA32(a0, qa, f32x16{}); s = MFMA8(a1, qb, s);
          if (pass == 0) { m = fmaxf(m, max16(s)); continue; }
#pragma unroll
          for (int j = 0; j < 16; ++j) EX(s, j);
#pragma unroll
          for (int s2 = 0; s2 < 2; ++s2) {
            const u32x4 vv = ld128(rs, r < 20 ? WS_VB + (h * 64 + kt) * 2048 + (2 * s2 + hh) * 512 + r * 16 : WS_C + blockIdx.x * 4096 + (r == 20 ? 0 : 16));
            o = smf(__builtin_bit_cast(bf16x8, vv), s, s2, o, idx);
          }
        }
        if (pass == 0) m = swap_max(m);
        else {
#pragma unroll
          for (int i = 0; i < 12; ++i) ofin[i] = o[i];
        }
      }
      auto r2 = __builtin_amdgcn_permlane32_swap(__float_as_uint(ofin[8]), __float_as_uint(ofin[8]), false, false);
      l = __uint_as_float(r2[1]);
    }
    const float rl = __builtin_amdgcn_rcpf(l);
    unsigned ow[6];
#pragma unroll
    for (int i = 0; i < 6; ++i) ow[i] = min(cvtpk(ofin[2 * i] * rl, ofin[2 * i + 1] * rl), 0xfffffffeu);
    st128(rs, obm, u32x4{ow[0], ow[1], ow[2], ow[3]});
    st64(rs, obt, u32x2{ow[4], ow[5]});
  }
  SBAR();
#pragma unroll
  for (int f = 12; f < 25; ++f) wf[f] = ldsr128(wl + WQ + f * 1024);
  SBAR();

  f32x4 accB[5];
  u32x4 og[8]; u32x2 ot[4];
  const unsigned ob0 = osrc + SWP * 5120, ob1 = osrc + (SWP ^ 1) * 5120, ot0 = ob0 - lane * 8 + 4096, ot1 = ob1 - lane * 8 + 4096;
#define OBLD(k, r, a, o) "s_bitcmp1_b32 %17, " #k "\n\ts_cbranch_scc0 1f\n\tglobal_load_dwordx4 %" #r ", %" #a ", %16 offset:" #o " sc1\n1:\n\t"
#define OBLT(k, r0, r1, a) "s_bitcmp1_b32 %17, " #k "\n\ts_cbranch_scc0 1f\n\tglobal_load_dwordx2 %" #r0 ", %" #a ", %16 sc1\n\tglobal_load_dwordx2 %" #r1 ", %" #a ", %16 offset:512 sc1\n1:\n\t"
#define OB_ALL OBLD(0, 0, 12, 0) OBLD(1, 1, 12, 1024) OBLD(2, 2, 12, 2048) OBLD(3, 3, 12, 3072) OBLT(4, 8, 9, 14) OBLD(5, 4, 13, 0) OBLD(6, 5, 13, 1024) OBLD(7, 6, 13, 2048) OBLD(8, 7, 13, 3072) OBLT(9, 10, 11, 15)
#define OG(c) c(og[0]), c(og[1]), c(og[2]), c(og[3]), c(og[4]), c(og[5]), c(og[6]), c(og[7]), c(ot[0]), c(ot[1]), c(ot[2]), c(ot[3])
#define OBL1(k, a, o) og[k] = ld128(rs, WS_OB + a + o)
#define OBL2(k, a, o) ot[k] = ld64(rs, WS_OB + a + o)
#define OFR(j) ((j) % 5 < 4 ? og[((j) - (j) / 5) & 7] : u32x4{ot[2 * ((j) / 5)][0], ot[2 * ((j) / 5)][1], ot[2 * ((j) / 5) + 1][0], ot[2 * ((j) / 5) + 1][1]})
#pragma unroll
  for (int j = 0; j < 5; ++j) {
    if (j == 0) __builtin_amdgcn_s_setprio(3); if (j == 2) __builtin_amdgcn_s_setprio(2); if (j == 3) __builtin_amdgcn_s_setprio(1); if (j == 4) __builtin_amdgcn_s_setprio(0);
#pragma unroll
    for (int n = 0; n < 5; ++n) accA[n] = MFMA16(wf[j * 5 + n], of[5 + j], accA[n]);
    SBAR();
#pragma unroll
    for (int n = 0; n < 5; ++n) wf[j * 5 + n] = ldsr128(wl + (j * 5 + n) * 1024);
    if (j == 2) { OBL1(0, ob0, 0); OBL1(1, ob0, 1024); OBL1(2, ob0, 2048); }
    if (j == 3) { OBL1(3, ob0, 3072); OBL2(0, ot0, 0); OBL2(1, ot0, 512); }
    if (j == 4) { OBL1(4, ob1, 0); OBL1(5, ob1, 1024); OBL1(6, ob1, 2048); }
    if (j == 4) { OBL1(7, ob1, 3072); OBL2(2, ot1, 0); OBL2(3, ot1, 512); }
    SBAR();
  }
  waitv_bar<12>();
  SBAR();
  const lds_cptr wh = (lds_cptr)lds + L_RING + kh * AT_STAGE + lane * 16;
#pragma unroll
  for (int j = 0; j < 10; ++j) {
    if (j % 5 == 0) {
      unsigned seen = 0;
#pragma unroll
      for (int k = j; k < j + 5; ++k) { const u32x4 v = OFR(k); seen = max(max(seen, v[0]), v[1]); seen = max(max(seen, v[2]), v[3]); }
      if (__any(seen == ~0u)) {
#pragma unroll 1
        for (unsigned spins = 0; spins < (1u << 16); ++spins) {
          unsigned pend = 0;
#pragma unroll
          for (int k = j; k < 10; ++k) { const u32x4 v = OFR(k); pend |= __any(max(max(v[0], v[1]), max(v[2], v[3])) == ~0u) ? (1u << k) : 0u; }
          if (!(pend & (0x1fu << j))) break;
          asm volatile(OB_ALL "s_waitcnt vmcnt(0)" : OG("+v") : "v"(ob0), "v"(ob1), "v"(ot0), "v"(ot1), "s"(ws + WS_OB), "s"(pend) : "memory", "scc");
        }
      }
    }
    if (j == 0) __builtin_amdgcn_s_setprio(3); if (j == 3) __builtin_amdgcn_s_setprio(2); if (j == 6) __builtin_amdgcn_s_setprio(1); if (j == 8) __builtin_amdgcn_s_setprio(0);
#pragma unroll
    for (int n = 0; n < 5; ++n) accB[n] = MFMA16(wf[(j % 5) * 5 + n], OFR(j), j == 0 ? f32x4{} : accB[n]);
    if (j < 5) {
#pragma unroll
      for (int n = 0; n < 5; ++n) wf[j * 5 + n] = ldsr128(wh + (j * 5 + n) * 1024);
    }
    SBAR();
  }
#pragma unroll
  for (int n = 0; n < 5; ++n) accB[n] = accA[n] * gate + accB[n] * (1.f - gate);
  {
    char* img = lds + L_R + (2 * kh + 1) * WQ + (tq * 16 + (lane & 15)) * 336 + (lane >> 4) * 16;
#pragma unroll
    for (int n = 0; n < 5; ++n) *(f32x4*)(img + n * 64) = accB[n];
    wait_bar<63>();
#pragma unroll
    for (int it = 0; it < 2; ++it) {
      const int u = tid + 512 * it, row = u / 10, pc = u - 10 * row;
      if (u < 640) {
        const char* a = lds + L_R + WQ + row * 336 + pc * 32;
        const f32x4 v0 = *(const f32x4*)a + *(const f32x4*)(a + 2 * WQ), v1 = *(const f32x4*)(a + 16) + *(const f32x4*)(a + 2 * WQ + 16);
        const u32x4 r4 = u32x4{cvtpk(v0[0], v0[1]), cvtpk(v0[2], v0[3]), cvtpk(v1[0], v1[1]), cvtpk(v1[2], v1[3])};
        char* op = out + (size_t)(rt * 64 + row) * (D_ * 2) + ct * 160 + pc * 16;
        *(u32x4*)op = r4;
      }
    }
  }
  asm volatile("GM_CODE_END:");
  if (keeper && !advanced) {
#pragma unroll 1
    for (int i = 0; i < 4; ++i) wait_flags(flg, FOA + i * 64 + lane, tag);
    if (lane == 0) __hip_atomic_store(flg, tag, RLX_AGENT);
  }
}

extern "C" void kernel_launch(void* const* d_in, const int* in_sizes, int n_in, void* d_out, int out_size, void* d_ws,
                              size_t ws_size, hipStream_t stream) {
  (void)hipFuncSetAttribute((const void*)fused_kernel, hipFuncAttributeMaxDynamicSharedMemorySize, LDS_BYTES);
  fused_kernel<<<256, 512, LDS_BYTES, stream>>>((const char*)d_in[0], (const char*)d_in[1], (const char*)d_in[4], (const char*)d_in[2],
                                                (const char*)d_in[5], (const char*)d_in[6], (char*)d_ws, (char*)d_out);
}
```

```cpp
#include <hip/hip_runtime.h>
#include <hip/hip_bf16.h>
#include <stdint.h>

typedef short bf16x8 __attribute__((ext_vector_type(8)));
typedef short s16x4 __attribute__((ext_vector_type(4)));
typedef __bf16 bfv8 __attribute__((ext_vector_type(8)));
typedef __bf16 bfv2 __attribute__((ext_vector_type(2)));
typedef __bf16 bfv16 __attribute__((ext_vector_type(16)));
typedef float f32x16 __attribute__((ext_vector_type(16)));
typedef float f32x8 __attribute__((ext_vector_type(8)));
typedef float f32x4 __attribute__((ext_vector_type(4)));
typedef unsigned u32x4 __attribute__((ext_vector_type(4)));
typedef unsigned u32x2 __attribute__((ext_vector_type(2)));
typedef unsigned long long u64;
#define LAS __attribute__((address_space(3)))
#define GAS __attribute__((address_space(1)))
#define AINL __attribute__((always_inline))
typedef LAS const char* lds_cptr;
typedef LAS char* lds_ptr;
typedef GAS u64 gu64;

constexpr int D_ = 640, N3_ = 1920;
constexpr u64 TAG_STEP = 0x9E3779B97F4A7C15ull;
constexpr float QSCALE = 0.22360679774997896f * 1.4426950408889634f;
constexpr unsigned OOB = 0x70000000u;

constexpr unsigned UNIT = 2048 * 16;
constexpr unsigned F1A = 512, FG = 768, FOA = 1024, F1B = 2048, NREP = 16;
constexpr unsigned WS_QKB = (2048 + NREP * 256) * 128;
constexpr unsigned WS_VB = WS_QKB + 160 * UNIT;
constexpr unsigned BR_A = 160 * UNIT + 32 * 64 * 2048;
constexpr unsigned WS_OA = WS_QKB + 2 * BR_A;
constexpr unsigned WS_OB = WS_OA + 2048 * 640 * 2;
constexpr unsigned WS_WO = WS_OB + 2048 * 640 * 2;
constexpr unsigned WS_G = WS_WO + 2 * 640 * 640 * 2;
constexpr unsigned WS_C = WS_G + 2048 * 4;
constexpr unsigned WS_END = WS_C + 256 * 4096;

constexpr int L_R = 0, WQ = 25600;
constexpr int L_RING = 106496, AT_HALF = 14336, AT_STAGE = 2 * AT_HALF;
constexpr int L_FL = 104448, L_EP = 98304, L_PF = 81920, L_ALL = 102400, L_Q = 0;
constexpr int L_LK = 51200, LA_UNIT = 4608, L_LV = L_LK + 3 * LA_UNIT;
constexpr int LDS_BYTES = 163840;

constexpr int BST = 2;
#define SBAR() __builtin_amdgcn_sched_barrier(0)
#define PIN(x) asm volatile("" : "+v"(x))
#define MFMA32(a, b, c) __builtin_amdgcn_mfma_f32_32x32x16_bf16(__builtin_bit_cast(bfv8, a), __builtin_bit_cast(bfv8, b), c, 0, 0, 0)
#define MFMA8(a, b, c) __builtin_amdgcn_mfma_f32_32x32x8bf16_1k(a, b, c, 0, 0, 0)
#define MFMA16(a, b, c) __builtin_amdgcn_mfma_f32_16x16x32_bf16(__builtin_bit_cast(bfv8, a), __builtin_bit_cast(bfv8, b), c, 0, 0, 0)
#define RLX_AGENT __ATOMIC_RELAXED, __HIP_MEMORY_SCOPE_AGENT
#define SC1 16

template <int V> struct IC { static constexpr int value = V; };
template <int I, int N, class F> __device__ __forceinline__ void sfor(F&& f) { if constexpr (I < N) { f(IC<I>{}); sfor<I + 1, N>(f); } }
template <int N> __device__ __forceinline__ void wait_bar() { asm volatile("s_waitcnt vmcnt(%0) lgkmcnt(0)\n\ts_barrier" :: "i"(N) : "memory"); }
template <int N> __device__ __forceinline__ void waitv_bar() { asm volatile("s_waitcnt vmcnt(%0)\n\ts_barrier" :: "i"(N) : "memory"); }

__device__ __forceinline__ unsigned cvtpk(float lo, float hi) {
  bfv2 r; r[0] = (__bf16)lo; r[1] = (__bf16)hi;
  return __builtin_bit_cast(unsigned, r);
}
__device__ __forceinline__ float dot2(unsigned a, unsigned b, float c) {
  return __builtin_amdgcn_fdot2_f32_bf16(__builtin_bit_cast(bfv2, a), __builtin_bit_cast(bfv2, b), c, false);
}
template <int OFF> __device__ __forceinline__ void dma(const char* base, unsigned voff, unsigned lds_base) {
  asm volatile("s_add_u32 m0, %2, %3\n\ts_nop 0\n\tglobal_load_lds_dwordx4 %0, %1" :: "v"(voff), "s"(base), "s"(lds_base), "i"(OFF) : "memory", "scc");
}
template <int OFF> __device__ __forceinline__ void dma_sc1(const char* base, unsigned voff, unsigned lds_base) {
  asm volatile("s_add_u32 m0, %2, %3\n\ts_nop 0\n\tglobal_load_lds_dwordx4 %0, %1 sc1" :: "v"(voff), "s"(base), "s"(lds_base), "i"(OFF) : "memory", "scc");
}
template <int OFF> __device__ __forceinline__ void dma2_sc1(const char* base, unsigned voff, unsigned lds_base) {
  asm volatile("s_add_u32 m0, %2, %3\n\ts_nop 0\n\tglobal_load_lds_dwordx4 %0, %1 sc1\n\tglobal_load_lds_dwordx4 %0, %1 offset:1024 sc1"
               :: "v"(voff), "s"(base), "s"(lds_base), "i"(OFF) : "memory", "scc");
}
template <int OFF> __device__ __forceinline__ void dma3_sc1(const char* base, unsigned voff, unsigned lds_base) {
  asm volatile("s_add_u32 m0, %2, %3\n\ts_nop 0\n\tglobal_load_lds_dwordx4 %0, %1 sc1\n\tglobal_load_lds_dwordx4 %0, %1 offset:1024 sc1\n\t"
               "global_load_lds_dwordx4 %0, %1 offset:2048 sc1" :: "v"(voff), "s"(base), "s"(lds_base), "i"(OFF) : "memory", "scc");
}
typedef short v4i16_t __attribute__((ext_vector_type(4)));
__device__ __forceinline__ s16x4 trd(lds_cptr p) {
  return __builtin_bit_cast(s16x4, __builtin_amdgcn_ds_read_tr16_b64_v4i16((LAS v4i16_t*)p));
}
__device__ __forceinline__ bf16x8 cat8(s16x4 a, s16x4 b) { return __builtin_shufflevector(a, b, 0, 1, 2, 3, 4, 5, 6, 7); }
__device__ __forceinline__ bf16x8 ldsr128(lds_cptr p) { return *(const LAS bf16x8*)p; }
__device__ __forceinline__ s16x4 ldsr64(lds_cptr p) { return *(const LAS s16x4*)p; }
__device__ __forceinline__ u32x4 pack8(const f32x16& a, int s, float sc) {
  return u32x4{cvtpk(a[8 * s] * sc, a[8 * s + 1] * sc), cvtpk(a[8 * s + 2] * sc, a[8 * s + 3] * sc),
               cvtpk(a[8 * s + 4] * sc, a[8 * s + 5] * sc), cvtpk(a[8 * s + 6] * sc, a[8 * s + 7] * sc)};
}
__device__ __forceinline__ int swap23(int r) { return (r & 0x13) | ((r & 4) << 1) | ((r & 8) >> 1); }
__device__ __forceinline__ u64 uni64(unsigned lo, unsigned hi) {
  return ((u64)(unsigned)__builtin_amdgcn_readfirstlane(hi) << 32) | (unsigned)__builtin_amdgcn_readfirstlane(lo);
}

typedef __amdgpu_buffer_rsrc_t rsrc_t;
__device__ __forceinline__ void st128(rsrc_t rs, unsigned off, u32x4 v) { __builtin_amdgcn_raw_buffer_store_b128(v, rs, off, 0, SC1); }
__device__ __forceinline__ void st64(rsrc_t rs, unsigned off, u32x2 v) { __builtin_amdgcn_raw_buffer_store_b64(v, rs, off, 0, SC1); }
__device__ __forceinline__ u32x4 ld128(rsrc_t rs, unsigned off) { return __builtin_amdgcn_raw_buffer_load_b128(rs, off, 0, SC1); }
__device__ __forceinline__ u32x2 ld64(rsrc_t rs, unsigned off) { return __builtin_amdgcn_raw_buffer_load_b64(rs, off, 0, SC1); }
__device__ __forceinline__ unsigned ld32(rsrc_t rs, unsigned off) { return __builtin_amdgcn_raw_buffer_load_b32(rs, off, 0, SC1); }

template <int SLEEP = 6>
__device__ __forceinline__ void wait_flags(gu64* flg, unsigned idx, u64 tag) {
  bool ok = false;
#pragma unroll 1
  for (unsigned spins = 0; spins < (1u << 18); ++spins) {
    if (!ok) ok = __hip_atomic_load(flg + idx * 16, RLX_AGENT) == tag;
    if (__all(ok)) break;
    __builtin_amdgcn_s_sleep(SLEEP);
  }
  asm volatile("" ::: "memory");
}
__device__ __forceinline__ void sample_flags(u64& fa, u64& fb, unsigned ia, unsigned ib, const char* base, u64 miss) {
  asm volatile("s_cmp_eq_u64 %5, 0\n\ts_cbranch_scc1 1f\n\tglobal_load_dwordx2 %0, %2, %4 sc1\n\tglobal_load_dwordx2 %1, %3, %4 sc1\n1:"
               : "+v"(fa), "+v"(fb) : "v"(ia * 128), "v"(ib * 128), "s"(base), "s"(miss) : "memory", "scc");
}

__device__ __forceinline__ unsigned o_main(int tok, int h, int hh) {
  const int m = h >> 1;
  return (unsigned)((((tok >> 4) * 20 + 5 * (m >> 2) + (m & 3)) * 64 + (2 * (h & 1) + hh) * 16 + (tok & 15)) * 16);
}
__device__ __forceinline__ unsigned o_tail(int tok, int h) {
  const int m = h >> 1;
  return (unsigned)(((tok >> 4) * 20 + 5 * (m >> 2) + 4) * 1024 + 512 * (m & 1) + ((2 * ((m >> 1) & 1) + (h & 1)) * 16 + (tok & 15)) * 8);
}

#define G1_ORDER "BBBBBBBBBBAAAAAAAAAA"
constexpr int G1_PUB = 14;
constexpr int G1_EP = 12;
constexpr int G1_SAMP = 72, G1_LOOK = 76;
struct G1 {
  int isB[24] = {}, kb[24] = {}, uf[25] = {};
  int lastB = 0;
  int iss_lo[20] = {}, iss_hi[20] = {};
  int wait[20] = {};
  constexpr G1() {
    const char* ord = G1_ORDER;
    int nb = 0, na = 0;
    for (int o = 0; o < 24; ++o) {
      isB[o] = o < 20 && ord[o] == 'B'; kb[o] = o < 20 ? (isB[o] ? nb++ : na++) : 0;
      uf[o + 1] = uf[o] + (o < 20 ? 1 + isB[o] : 0);
      if (isB[o]) lastB = o;
    }
    int pos[60] = {}, cnt = 0, lastst = 0, done = 2 * uf[2];
    for (int d = 0; d < done; ++d) pos[d] = cnt++;
    for (int o = 0; o < 20; ++o) {
      int need = pos[2 * uf[o + 1] - 1];
      if (o == G1_PUB && lastst > need) need = lastst;
      wait[o] = o >= 18 ? 0 : cnt - 1 - need;
      if (o == G1_EP) lastst = cnt++;
      iss_lo[o] = done; iss_hi[o] = 2 * uf[o + 4];
      for (int ks = 0; ks < 4; ++ks) {
        const int per = (iss_hi[o] - iss_lo[o] + 3) / 4;
        for (int z = 0; z < per && done < iss_hi[o]; ++z) pos[done++] = cnt++;
        const int n = 4 * o + ks - 2 - (4 * lastB + 3);
        if (n >= 0 && n < 4 * BST && n % BST == 0) lastst = cnt++;
      }
    }
  }
};
constexpr int g1_slot(int u) { return ((u + 6) % 10) * 16384; }
template <bool VT, class FPiece, class FStart, class FSetup>
__device__ __forceinline__ void g1_body(char* ws, rsrc_t rs, char* lds, int ti, int cj, int w, int lane, u64& tag, bool& early, unsigned& fiKV, unsigned& fiQ,
                                        u32x4 (&pk)[4], unsigned (&dstB)[2], FPiece&& piece, FStart&& att_start, FSetup&& setup) {
  constexpr G1 P;
  static_assert(P.uf[G1_EP - 1] > 20 && G1_EP < G1_PUB && P.uf[4] <= 9, "epoch / prefetch scratch slots");
  const int tg = w & 3, cg = w >> 2;
  const int r = lane & 31, hh = lane >> 5, g16 = (lane >> 4) & 1, q4 = (lane & 15) >> 2, p4 = lane & 3;
  const int tr = VT ? swap23(r) : r;
  unsigned xoff[4];
#pragma unroll
  for (int ks = 0; ks < 4; ++ks) xoff[ks] = (unsigned)((32 * tg + tr) * 128 + (((2 * ks + hh) ^ ((tr >> 1) & 7)) * 16));
  const int psw = ((p4 & 1) << 1) | (p4 >> 1);
  unsigned woff[2];
#pragma unroll
  for (int t2 = 0; t2 < 2; ++t2) woff[t2] = (unsigned)((8 * hh + q4) * 256 + (((2 * cg + t2) ^ q4) * 64) + 32 * g16 + 8 * (VT ? p4 : psw));
  const float sc = (!VT && cj < 5) ? QSCALE : 1.f;
  bf16x8 xf[40], wf[3][2];
  u64 fa, fb;
  f32x16 accB[2], accA[2];
  u64 miss = 1;
  auto look = [&](bool again) AINL {
    asm volatile("s_cmp_eq_u64 %2, 0\n\ts_cbranch_scc1 1f\n\ts_waitcnt vmcnt(0)\n1:" : "+v"(fa), "+v"(fb) : "s"(miss) : "memory", "scc");
    const u64 m = __ballot(fa != tag || fb != tag);
    if (m == 0 && miss != 0) att_start();
    miss = m;
    if (again) sample_flags(fa, fb, fiKV, fiQ, ws + (F1B + 256) * 128, miss);
  };
  sfor<0, 82>([&](auto ic) AINL {
    constexpr int nn = decltype(ic)::value, n = nn - 2;
    constexpr int o = nn >> 2, ks = nn & 3;
    if constexpr (nn < 80) {
      if constexpr (ks == 0) {
        if constexpr (o < 19) waitv_bar<P.wait[o]>();
        if constexpr (o == G1_EP) dma_sc1<L_EP>(ws, 0, (unsigned)(uintptr_t)lds + w * 1024);
        if constexpr (o == G1_PUB) { const u32x2 ep = *(const LAS u32x2*)((lds_cptr)lds + L_EP + w * 1024); tag = uni64(ep[0], ep[1]) + TAG_STEP; }
        if constexpr (o == G1_PUB) { if (w == 0 && lane < NREP) __hip_atomic_store((gu64*)ws + (F1B + lane * 256 + cj * 16 + ti) * 16, tag, RLX_AGENT); }
      }
      if constexpr (nn >= 62 && nn < 70 && !(nn & 1)) setup(IC<(nn - 62) / 2>{});
      if constexpr (nn == G1_SAMP) { fa = 0; fb = 0; sample_flags(fa, fb, fiKV, fiQ, ws + F1B * 128, miss); }
      if constexpr (nn == G1_LOOK) look(true);
      if constexpr (P.isB[o]) xf[4 * P.kb[o] + ks] = ldsr128((lds_cptr)lds + g1_slot(P.uf[o] + 1) + xoff[ks]);
#pragma unroll
      for (int t2 = 0; t2 < 2; ++t2) {
        const lds_cptr wp = (lds_cptr)lds + g1_slot(P.uf[o]) + woff[t2] + ks * 4096;
        wf[nn % 3][t2] = cat8(trd(wp), trd(wp + 1024));
      }
      constexpr int per = (P.iss_hi[o] - P.iss_lo[o] + 3) / 4, d0 = P.iss_lo[o] + per * ks;
      sfor<d0, (d0 + per < P.iss_hi[o] ? d0 + per : P.iss_hi[o])>(piece);
    }
    if constexpr (n >= 0) {
      constexpr int om = n >> 2, xi = 4 * P.kb[om] + (n & 3);
#pragma unroll
      for (int t2 = 0; t2 < 2; ++t2) {
        f32x16& ac = P.isB[om] ? accB[t2] : accA[t2];
        const f32x16 cin = xi == 0 ? f32x16{} : ac;
        ac = VT ? MFMA32(xf[xi], wf[n % 3][t2], cin) : MFMA32(wf[n % 3][t2], xf[xi], cin);
      }
    }
    SBAR();
    constexpr int nb = n - (4 * P.lastB + 3);
    if constexpr (nb == 0) {
#pragma unroll
      for (int i = 0; i < 4; ++i) { pk[i] = VT ? pack8(accB[i >> 1], i & 1, 1.f) : pack8(accB[i >> 1], i & 1, sc); PIN(pk[i]); }
      int ln = lane; PIN(ln);
#pragma unroll
      for (int t2 = 0; t2 < 2; ++t2) {
        if (VT) {
          const int F = (cj - 10) * 128 + 64 * cg + 32 * t2 + (ln & 31), hd = F / 20;
          int d = F - 20 * hd - 4 * (hd & 1); if (d < 0) d += 20;
          dstB[t2] = WS_VB + (unsigned)((hd * 64 + 4 * ti + tg) * 2048 + (ln >> 5) * 512 + d * 16);
        } else {
          dstB[t2] = WS_QKB + (unsigned)(cj * 16 + 8 * cg + 4 * t2 + (ln >> 5)) * UNIT + (unsigned)(128 * ti + 32 * tg + (ln & 31)) * 16;
        }
      }
    }
    if constexpr (nb >= 0 && nb < 4 * BST && nb % BST == 0) {
      constexpr int i = nb / BST;
      st128(rs, dstB[i >> 1] + (i & 1) * (VT ? 1024 : 2 * UNIT), pk[i]);
      SBAR();
    }
  });
  look(false);
  early = miss == 0;
#pragma unroll
  for (int i = 0; i < 4; ++i) pk[i] = pack8(accA[i >> 1], i & 1, 1.f);
}

__device__ __forceinline__ f32x16 smf(bf16x8 a, const f32x16& p, int s, f32x16 o, int idx) {
  f32x8 b = s ? __builtin_shufflevector(p, p, 8, 9, 10, 11, 12, 13, 14, 15) : __builtin_shufflevector(p, p, 0, 1, 2, 3, 4, 5, 6, 7);
  return __builtin_amdgcn_smfmac_f32_32x32x32_bf16(__builtin_bit_cast(bfv8, a), __builtin_bit_cast(bfv16, b), o, idx, 0, 0);
}
__device__ __forceinline__ float max16(const f32x16& s) {
  float a = fmaxf(fmaxf(s[0], s[1]), s[2]), b = fmaxf(fmaxf(s[3], s[4]), s[5]);
  a = fmaxf(fmaxf(a, s[6]), s[7]); b = fmaxf(fmaxf(b, s[8]), s[9]);
  a = fmaxf(fmaxf(a, s[10]), s[11]); b = fmaxf(fmaxf(b, s[12]), s[13]);
  a = fmaxf(fmaxf(a, s[14]), s[15]);
  return fmaxf(a, b);
}
__device__ __forceinline__ float swap_max(float m) {
  auto rr = __builtin_amdgcn_permlane32_swap(__float_as_uint(m), __float_as_uint(m), false, false);
  return fmaxf(__uint_as_float(rr[0]), __uint_as_float(rr[1]));
}
__device__ __forceinline__ float swap_add(float m) {
  auto rr = __builtin_amdgcn_permlane32_swap(__float_as_uint(m), __float_as_uint(m), false, false);
  return __uint_as_float(rr[0]) + __uint_as_float(rr[1]);
}
__device__ __forceinline__ void set_ref(s16x4& q1, float m, bool spare) {
  const unsigned a = __float_as_uint(-m) & 0xffff0000u;
  const float r1 = -m - __uint_as_float(a);
  const unsigned b = __float_as_uint(r1) & 0xffff0000u;
  const unsigned c = __float_as_uint(r1 - __uint_as_float(b));
  if (spare) q1 = __builtin_bit_cast(s16x4, u32x2{(a >> 16) | b, c >> 16});
}

__global__ __launch_bounds__(512, 2) void fused_kernel(const char* __restrict__ x, const char* __restrict__ wA, const char* __restrict__ wB,
                                                       const char* __restrict__ woA, const char* __restrict__ woB,
                                                       const char* __restrict__ gw, char* ws, char* __restrict__ out) {
  extern __shared__ __attribute__((aligned(16))) char lds[];
  const int tid = threadIdx.x, lane = tid & 63;
  const int w = __builtin_amdgcn_readfirstlane(tid >> 6);
  const int xcd = blockIdx.x & 7, slot = blockIdx.x >> 3;
  const unsigned lds0 = (unsigned)(uintptr_t)lds;
  gu64* flg = (gu64*)ws;
  const rsrc_t rs = __builtin_amdgcn_make_buffer_rsrc(ws, 0, WS_END, 0x00020000);
  const int ti = 8 * (xcd & 1) + (slot & 7);
  const int cj = 4 * (xcd >> 1) + (slot >> 3);
  const unsigned ldpf = lds0 + (cj < 15 ? L_PF : L_RING);
  if (w == 6) {
    u64 pc; unsigned len;
    asm volatile("s_getpc_b64 %0\nGM_CODE_BEG:\n\ts_mov_b32 %1, GM_CODE_END-GM_CODE_BEG" : "=s"(pc), "=s"(len));
    asm volatile("s_add_u32 m0, %2, %3\n\ts_nop 0\n\tglobal_load_lds_dword %0, %1"
                 :: "v"(min((unsigned)((slot & 7) * 64 + lane) * 128, len)), "s"(pc), "s"(ldpf), "i"(0) : "memory", "scc");
  }
  const int rot = ((slot & 7) * 5 / 4 + (slot >> 3) * 5 / 2 + xcd) % 10;
  unsigned xlo[2], wlo[2];
#pragma unroll
  for (int i = 0; i < 2; ++i) {
    const int row = 16 * w + 8 * i + (lane >> 3);
    xlo[i] = (unsigned)(row * (D_ * 2) + (((lane & 7) ^ ((row >> 1) & 7)) * 16));
    wlo[i] = (unsigned)((8 * w + 4 * i + (lane >> 4)) * (N3_ * 2) + (((lane & 15) ^ (4 * (lane >> 4))) * 16));
  }
  const char* xb = x + (size_t)(128 * ti) * (D_ * 2);
  const char* wBb = wB + cj * 256;
  const char* wAb = wA + cj * 256;
  const unsigned ldw = lds0 + w * 2048;
  auto piece = [&](auto dc) AINL {
    constexpr G1 P;
    constexpr int d = decltype(dc)::value, u = d >> 1, base = g1_slot(u) + (d & 1) * 1024;
    constexpr int o = [&]() { int q = 0; while (P.uf[q + 1] <= u) ++q; return q; }();
    int kb = P.kb[o] + rot; if (kb >= 10) kb -= 10;
    if constexpr (u != P.uf[o]) dma<base>(xb + kb * 128, xlo[d & 1], ldw);
    else dma<base>((P.isB[o] ? wBb : wAb) + (size_t)kb * (64 * N3_ * 2), wlo[d & 1], ldw);
  };
  if (cj < 15) {
    sfor<0, 8>(piece);
  }
  SBAR();
  u64 tag;
  const int h = 4 * xcd + (slot & 3), c = slot >> 2;
  const int e = h & 1, ua = 5 * (h >> 1) + 3 * e, us = 5 * (h >> 1) + 2;
  const int jlo = (5 * (h >> 1) + 2 * e) >> 4, jhi = (5 * (h >> 1) + 2 * e + 2) >> 4;
  const int kh = w >> 2, tq = w & 3;
  const char* kbase = ws + WS_QKB + (size_t)(80 + (tq >= 2 ? us : ua + tq)) * UNIT + 1024 * kh * 16;
  if (w == 5) { const unsigned cv = (lane & 1) ? 0u : 0x3f803f80u; st128(rs, lane < 8 ? WS_C + blockIdx.x * 4096 + (lane >> 1) * 1024 + (lane & 1) * 16 : OOB, u32x4{cv, cv, cv, cv}); }
  const unsigned ldr = lds0 + kh * AT_HALF + tq * 2048;
  unsigned vlo, vstr;
  auto kpiece = [&](auto stc) AINL {
    constexpr int st = decltype(stc)::value;
    if (tq < 3) dma2_sc1<L_RING + (st & 1) * AT_STAGE>(kbase + st * 2048, lane * 16, ldr);
  };
  auto vpiece = [&](auto stc) AINL {
    constexpr int st = decltype(stc)::value;
    dma2_sc1<L_RING + (st & 1) * AT_STAGE + 6144>(ws, vlo + st * vstr, ldr);
  };
  unsigned fiKV, fiQ;
  auto att_start = [&]() AINL {
    kpiece(IC<0>{});
    if (tq == 3) {
      const char* qb = ws + WS_QKB + (256 * c + 128 * kh) * 16;
      dma2_sc1<L_Q>(qb + (size_t)ua * UNIT, lane * 16, lds0 + kh * 2048);
      dma2_sc1<L_Q + 4096>(qb + (size_t)(ua + 1) * UNIT, lane * 16, lds0 + kh * 2048);
      dma2_sc1<L_Q + 8192>(qb + (size_t)us * UNIT, lane * 16, lds0 + kh * 2048);
    }
    vpiece(IC<0>{});
  };

  const int hh = lane >> 5, r = lane & 31;
  const int rt = 4 * xcd + (slot & 3), ct = slot >> 2;
  const int SWP = ct & 1;
  const char* wob = ws + WS_WO + (size_t)ct * 102400 + w * 3072;
  auto woquarter = [&](auto brc, auto qc, auto dc) AINL {
    constexpr int br = decltype(brc)::value, q = decltype(qc)::value, d = decltype(dc)::value;
    const int off = br * 819200 + (q ^ (br & SWP)) * WQ;
    dma3_sc1<d>(wob + off, lane * 16, lds0 + w * 3072);
    if (w == 0) dma_sc1<d + 24576>(wob + off + 24576, lane * 16, lds0);
  };
  const unsigned ladst = lds0 + (w < 3 ? L_LK + w * LA_UNIT : L_LV + min(4 * (w - 3), 13) * 1024);
  unsigned osrc, fiA, obm, obt, lalo; int idx;
  lds_cptr kp0, kp1, vp, qp;
  f32x16 oa, ob;
  auto setup = [&](auto pc) AINL {
    constexpr int p = decltype(pc)::value;
    int ln = lane; PIN(ln);
    const int hh = ln >> 5, r = ln & 31;
    if constexpr (p == 0) {
      const unsigned crow = WS_C + blockIdx.x * 4096 + (r == 20 ? 0 : 16);
      vlo = r < 20 ? WS_VB + (h * 64 + 32 * kh + tq) * 2048 + ln * 16 : crow; vstr = r < 20 ? 8192u : 0u;
      const unsigned lab = w < 3 ? WS_QKB + BR_A + (80 + (w == 2 ? us : ua + w)) * UNIT + (256 * c - 16) * 16
                                 : WS_VB + BR_A + (h * 64 + 8 * c) * 2048 - 1024 + min(4 * (w - 3), 13) * 1024;
      lalo = (w < 3 || r < 20) ? lab + ln * 16 : crow;
      osrc = (unsigned)((rt * 4 + tq) * 20 + 10 * kh) * 1024 + ln * 16;
      PIN(vlo); PIN(vstr); PIN(lalo); PIN(osrc);
    }
    if constexpr (p == 1) {
      fiA = ln < 18 ? F1A + (5 * (ln / 6) + (((ln / 3) & 1) ? jhi : jlo)) * 16 + max(2 * c - 1 + ln % 3, 0) : FG + (ln == 18 ? ct : 8 + ct);
      obm = WS_OB + o_main(256 * c + 64 * tq + 32 * kh + r, h, hh); obt = (hh ? OOB : 0u) + WS_OB + o_tail(256 * c + 64 * tq + 32 * kh + r, h);
      PIN(fiA); PIN(obm); PIN(obt);
    }
    if constexpr (p == 2) {
      const lds_cptr ring = (lds_cptr)lds + L_RING + kh * AT_HALF;
      kp0 = ring + hh * 2048 + r * 16;
      kp1 = (hh == e) ? ring + 2 * 2048 + r * 16 + 8 * hh : ring + 6144 + 20 * 16;
      vp = ring + 6144 + hh * 512 + r * 16;
      qp = (lds_cptr)lds + L_Q + (64 * tq + r) * 16;
      idx = 0xDDDD;
      PIN(kp0); PIN(kp1); PIN(vp); PIN(qp); PIN(idx);
    }
    if constexpr (p == 3) {
      oa = f32x16{}; ob = f32x16{}; PIN(oa); PIN(ob);
      fiKV = (5 + 5 * hh + ((ln & 16) ? jhi : jlo)) * 16 + (ln & 15); fiQ = ((ln & 1) ? jhi : jlo) * 16 + 2 * c + ((ln >> 1) & 1);
      PIN(fiKV); PIN(fiQ);
    }
  };

  bool early = false;
  u32x4 pk[4]; unsigned dstA[2];
  if (cj < 10) g1_body<false>(ws, rs, lds, ti, cj, w, lane, tag, early, fiKV, fiQ, pk, dstA, piece, att_start, setup);
  else if (cj < 15) g1_body<true>(ws, rs, lds, ti, cj, w, lane, tag, early, fiKV, fiQ, pk, dstA, piece, att_start, setup);
  else {
    const char* wo = ((ti & 8) ? woB : woA) + (size_t)(80 * w) * (D_ * 2) + (ti & 7) * 160;
#pragma unroll 1
    for (int p = 0; p < 13; ++p) {
      const int s0 = min(64 * p, 736), sl = s0 + lane;
      dma<0>(wo, (unsigned)((sl / 10) * (D_ * 2) + (sl % 10) * 16), lds0 + w * 12800 + s0 * 16);
    }
    const u64 ep = __hip_atomic_load(flg, RLX_AGENT);
    tag = uni64((unsigned)ep, (unsigned)(ep >> 32)) + TAG_STEP;
    wait_bar<0>();
    const int g4 = lane >> 4, rl = 320 * kh + 4 * (g4 & 1) + ((lane & 15) >> 2);
#pragma unroll
    for (int ff = 0; ff < 13; ++ff) {
      const int f = min(tq + 4 * ff, 49), j = f / 5, n = f - 5 * j;
      const bool mn = j % 5 < 4;
      const int row = rl + 160 * (j / 5) + (mn ? 40 * (j % 5) + 24 * (g4 >> 1) : 80 * (g4 >> 1) + 16);
      const lds_cptr p = (lds_cptr)lds + row * 160 + n * 32 + 8 * (lane & 3);
      st128(rs, WS_WO + (unsigned)(ti * 100 + kh * 50 + f) * 1024 + lane * 16, __builtin_bit_cast(u32x4, cat8(trd(p), trd(p + (mn ? 8 : 40) * 160))));
    }
    wait_bar<0>();
    if (tid == 0) __hip_atomic_store(flg + (FG + ti) * 16, tag, RLX_AGENT);
    if (w == 0) { wait_flags<16>(flg, F1B + (NREP - 1) * 256 + jlo * 16 + 2 * c, tag); }
    wait_bar<63>();
    sfor<0, 4>(setup);
    dstA[0] = OOB; dstA[1] = OOB;
#pragma unroll
    for (int i = 0; i < 4; ++i) pk[i] = u32x4{0u, 0u, 0u, 0u};
  }
  const unsigned flag1 = F1A + cj * 16 + ti;

  if (!early) {
    bool oka = false, okb = false;
#pragma unroll 1
    for (unsigned spins = 0; spins < (1u << 18); ++spins) {
      const unsigned rp = F1B + (2 + min(spins, NREP - 4)) * 256;
      const u64 a = __hip_atomic_load(flg + (rp + (oka ? fiQ : fiKV)) * 16, RLX_AGENT), b = __hip_atomic_load(flg + (rp + fiQ) * 16, RLX_AGENT);
      oka = oka || a == tag; okb = okb || b == tag;
      if (__all(oka && okb)) break;
      __builtin_amdgcn_s_sleep(2);
    }
    asm volatile("" ::: "memory");
    att_start();
  }
  wait_bar<0>();
  float gate;
  const unsigned fiOA = FOA + (rt >> 2) * 32 + r;
  u32x4 lq; u32x2 lqt;
  f32x16 sa, sb;
  bf16x8 q0[2]; s16x4 q1[2];
#pragma unroll
  for (int t = 0; t < 2; ++t) {
    q0[t] = ldsr128(qp + hh * 4096 + ((t ^ kh ^ 1) * 512));
    q1[t] = ldsr64(qp + 8192 + 8 * e + ((t ^ kh ^ 1) * 512));
  }
  const bf16x8 kr0 = ldsr128(kp0 - kh * AT_HALF); const s16x4 kr1 = ldsr64(kp1 - kh * AT_HALF);
  bf16x8 k0 = ldsr128(kp0); s16x4 k1 = ldsr64(kp1);
  bf16x8 v0 = ldsr128(vp), v1 = ldsr128(vp + 1024);
  bf16x8 v1p = v1;
  SBAR();
  {
    set_ref(q1[0], 0.f, hh != e); set_ref(q1[1], 0.f, hh != e);
    f32x16 s = MFMA32(kr0, q0[0], f32x16{}), t = MFMA32(kr0, q0[1], f32x16{});
    s = MFMA8(kr1, q1[0], s); t = MFMA8(kr1, q1[1], t);
    sa = MFMA32(k0, q0[0], f32x16{});
    SBAR();
    kpiece(IC<1>{}); vpiece(IC<1>{});
    SBAR();
    set_ref(q1[0], swap_max(max16(s)), hh != e);
    sa = MFMA8(k1, q1[0], sa);
    SBAR();
    set_ref(q1[1], swap_max(max16(t)), hh != e);
  }
  const bool keeper = blockIdx.x == 0 && w == 7;
  bool advanced = false;
  u32x4 of[10];
  unsigned gx[5], gg[5]; float gs;

  auto local_attention = [&]() AINL {
    const int tok = 256 * c + 32 * w + r;
    const lds_cptr lk = (lds_cptr)lds + L_LK + (32 * w + r) * 16, lv = (lds_cptr)lds + L_LV + (4 * w + hh) * 512 + r * 16;
    const bf16x8 qa = __builtin_bit_cast(bf16x8, lq);
    const s16x4 qb = __builtin_bit_cast(s16x4, (hh == e) ? lqt : u32x2{0u, 0u});
    f32x16 sx = MFMA32(ldsr128(lk + hh * LA_UNIT), qa, f32x16{}), sy = MFMA32(ldsr128(lk + hh * LA_UNIT + 512), qa, f32x16{});
    sx = MFMA8(ldsr64(lk + 2 * LA_UNIT + 8 * e), qb, sx); sy = MFMA8(ldsr64(lk + 2 * LA_UNIT + 8 * e + 512), qb, sy);
    const bf16x8 va = ldsr128(lv), vb = ldsr128(lv + 1024), vc = ldsr128(lv + 2048);
    const u64 edge = (c == 0 && w == 0) ? 0ull : ~0ull;
    const float ninf = -INFINITY;
    sfor<0, 24>([&](auto jc) AINL {
      constexpr int j = decltype(jc)::value;
      constexpr u64 mk = []() { u64 m = 0; for (int l = 0; l < 64; ++l) { const int d = (l & 31) + 16 - ((j & 3) + 8 * (j >> 2) + 4 * (l >> 5)); if (d >= 0 && d <= 14 && !(d & 1)) m |= 1ull << l; } return m; }();
      const bool ok = __builtin_amdgcn_inverse_ballot_w64(j < 8 ? mk & edge : mk);
      if constexpr (j < 16) sx[j] = ok ? sx[j] : ninf; else sy[j - 16] = ok ? sy[j - 16] : ninf;
    });
    float m = fmaxf(fmaxf(max16(sx), sy[0]), sy[1]);
    m = fmaxf(fmaxf(m, sy[2]), sy[3]); m = fmaxf(fmaxf(m, sy[4]), sy[5]); m = -QSCALE * swap_max(fmaxf(fmaxf(m, sy[6]), sy[7]));
#pragma unroll
    for (int j = 0; j < 16; ++j) sx[j] = __builtin_amdgcn_exp2f(__builtin_fmaf(sx[j], QSCALE, m));
#pragma unroll
    for (int j = 0; j < 8; ++j) sy[j] = __builtin_amdgcn_exp2f(__builtin_fmaf(sy[j], QSCALE, m));
    f32x16 o = smf(va, sx, 0, f32x16{}, idx); o = smf(vb, sx, 1, o, idx); o = smf(vc, sy, 0, o, idx);
    auto lr = __builtin_amdgcn_permlane32_swap(__float_as_uint(o[8]), __float_as_uint(o[8]), false, false);
    const float rl = __builtin_amdgcn_rcpf(__uint_as_float(lr[1]));
    st128(rs, WS_OA + o_main(tok, h, hh), pack8(o, 0, rl));
    st64(rs, hh ? OOB : WS_OA + o_tail(tok, h), u32x2{cvtpk(o[8] * rl, o[9] * rl), cvtpk(o[10] * rl, o[11] * rl)});
    gs += __builtin_bit_cast(float, __builtin_amdgcn_update_dpp(0, __builtin_bit_cast(int, gs), 0xB1, 0xf, 0xf, true));
    gs += __builtin_bit_cast(float, __builtin_amdgcn_update_dpp(0, __builtin_bit_cast(int, gs), 0x4E, 0xf, 0xf, true));
    gs += __builtin_bit_cast(float, __builtin_amdgcn_update_dpp(0, __builtin_bit_cast(int, gs), 0x141, 0xf, 0xf, true));
    gs += __builtin_bit_cast(float, __builtin_amdgcn_update_dpp(0, __builtin_bit_cast(int, gs), 0x140, 0xf, 0xf, true));
    { auto g2 = __builtin_amdgcn_permlane16_swap(__float_as_uint(gs), __float_as_uint(gs), false, false); gs = __uint_as_float(g2[0]) + __uint_as_float(g2[1]); }
    gs = swap_add(gs);
    const float gv = __builtin_amdgcn_rcpf(1.f + __builtin_amdgcn_exp2f(-1.4426950408889634f * gs));
    __builtin_amdgcn_raw_buffer_store_b32(__float_as_uint(gv), rs, lane == 0 ? WS_G + (256 * c + 8 * h + w) * 4 : OOB, 0, SC1);
    SBAR();
  };

  const lds_cptr wl = (lds_cptr)lds + L_R + kh * (2 * WQ) + lane * 16;
  f32x4 accA[5];
  bf16x8 wq[2][5], wf[25];
  auto opa = [&](auto hc, auto gc) AINL {
    constexpr int hs = decltype(hc)::value, g = decltype(gc)::value;
    if constexpr (hs >= 0 && hs <= 5) SBAR();
    if constexpr (hs >= 1 && hs <= 5) accA[g] = MFMA16(wq[(hs - 1) & 1][g], of[hs - 1], hs == 1 ? f32x4{} : accA[g]);
    if constexpr (hs >= 0 && hs < 5) wq[hs & 1][g] = ldsr128(wl + (hs * 5 + g) * 1024);
    if constexpr (hs >= 0 && hs <= 5) SBAR();
  };
#define EX(S, j) S[j] = __builtin_amdgcn_exp2f(S[j])
#define EX2(S, j) do { EX(S, j); EX(S, j + 1); } while (0)
  sfor<0, 8>([&](auto stc) AINL {
    constexpr int st = decltype(stc)::value;
    constexpr int cur = (st & 1) * AT_STAGE, nxt = ((st + 1) & 1) * AT_STAGE;
    sfor<0, 4>([&](auto ic) AINL {
      constexpr int i = decltype(ic)::value;
      constexpr int nk = (i < 3) ? cur + (i + 1) * 512 : nxt, nv = (i < 3) ? cur + (i + 1) * 2048 : nxt;
      constexpr int sl = (i + 1) & 3, sst = (i == 3) ? st + 1 : st;
      constexpr int ha = st == 6 ? 2 * i : -9, hb = st == 6 ? 2 * i + 1 : -9;
      SBAR();
      EX2(sa, 0); PIN(sa); SBAR();
      if constexpr (st + i > 0) ob = smf(v1p, sb, 1, ob, idx);
      SBAR(); EX2(sa, 2); opa(IC<ha>{}, IC<0>{}); EX2(sa, 4); PIN(sa); SBAR();
      sb = MFMA32(k0, q0[1], f32x16{}); SBAR(); EX2(sa, 6); opa(IC<ha>{}, IC<1>{}); EX2(sa, 8); PIN(sa); SBAR();
      sb = MFMA8(k1, q1[1], sb); SBAR();
      if constexpr (i == 3) {
        wait_bar<0>();
        if constexpr (st == 0) { if (tid == 0) __hip_atomic_store(flg + flag1 * 16, tag, RLX_AGENT); }
        if constexpr (st == 2) {
          PIN(lq); PIN(lqt);
        }
        if constexpr (st == 1) {
          gs = 0.f;
#pragma unroll
          for (int i = 0; i < 5; ++i) gs = dot2(gx[i], gg[i], gs);
          PIN(gs);
        }
        if constexpr (st == 3) { if (tid == 0) __hip_atomic_store(flg + (FOA + c * 32 + h) * 16, tag, RLX_AGENT); }
        if constexpr (st == 5 || st == 7) {
#pragma unroll
          for (int j = 0; j < 5; ++j) PIN(of[(st == 7 ? 5 : 0) + j]);
          if constexpr (st == 7) PIN(gate);
        }
        if constexpr (st == 6) {
          if (keeper) {
            const lds_cptr p = (lds_cptr)lds + L_ALL + lane * 16;
            advanced = __all(*(const LAS u64*)p == tag && *(const LAS u64*)(p + 1024) == tag && *(const LAS u64*)(p + 2048) == tag && *(const LAS u64*)(p + 3072) == tag);
            if (advanced && lane == 0) __hip_atomic_store(flg, tag, RLX_AGENT);
          }
        }
        if constexpr (st == 1 || st == 4) {
          if (!__all(*(const LAS u64*)((lds_cptr)lds + L_FL + lane * 16) == tag)) wait_flags(flg, st == 1 ? fiA : fiOA, tag);
        }
      }
      __builtin_amdgcn_s_setprio(3 - sl);
      k0 = ldsr128(kp0 + nk); k1 = ldsr64(kp1 + nk);
      if constexpr (sst >= 1 && sst + 1 < 8 && sl == 0) kpiece(IC<sst + 1>{});
      if constexpr (sst == 0 && sl < 3) st128(rs, dstA[sl - 1] + BR_A, pk[2 * sl - 2]);
      if constexpr ((sst == 1 || sst == 4) && sl == 3) { if (w == 7) dma_sc1<L_FL>(ws, (sst == 1 ? fiA : fiOA) * 128, lds0); }
      if constexpr (sst == 6 && sl == 1) {
        if (keeper) sfor<0, 4>([&](auto kc) AINL { constexpr int k = decltype(kc)::value; dma_sc1<L_ALL + k * 1024>(ws + (FOA + k * 64) * 128, lane * 128, lds0); });
      }
      if constexpr (sst == 2 && sl == 0) dma_sc1<0>(ws, lalo + ((c == 0 && (w == 3 || (w < 3 && lane < 16))) ? 1024 : 0), ladst);
      if constexpr (sst == 2 && sl == 1) { dma3_sc1<1024>(ws + 1024, lalo, ladst); if (w < 3) dma_sc1<3328>(ws + 3328, lalo, ladst); }
      if constexpr ((sst == 4 || sst == 5) && (sl == 1 || sl == 2)) woquarter(IC<0>{}, IC<2 * sl - 2 + sst - 4>{}, IC<L_R + (2 * sl - 2 + sst - 4) * WQ>{});
      if constexpr (sst == 7 && sl == 0) woquarter(IC<1>{}, IC<1>{}, IC<L_RING>{});
      if constexpr (sst == 8) woquarter(IC<1>{}, IC<3>{}, IC<L_RING + AT_STAGE>{});
      if constexpr (sst == 7 && sl > 0) { wf[4 * sl - 4] = ldsr128(wl + WQ + (4 * sl - 4) * 1024); wf[4 * sl - 3] = ldsr128(wl + WQ + (4 * sl - 3) * 1024); }
      if constexpr (sst == 7 && (sl == 1 || sl == 2)) woquarter(IC<1>{}, IC<2 * sl - 2>{}, IC<L_R + (2 * sl - 2) * WQ>{});
      EX2(sa, 10); opa(IC<ha>{}, IC<2>{}); EX2(sa, 12); PIN(sa); SBAR();
      __builtin_amdgcn_s_waitcnt(0xC07F); SBAR();
      oa = smf(v0, sa, 0, oa, idx); SBAR(); EX2(sa, 14); opa(IC<ha>{}, IC<3>{}); PIN(sa); SBAR();
      EX2(sb, 0); opa(IC<ha>{}, IC<4>{}); PIN(sb); SBAR();
      oa = smf(v1, sa, 1, oa, idx); SBAR(); EX2(sb, 2); opa(IC<hb>{}, IC<0>{}); EX2(sb, 4); PIN(sb); SBAR();
      sa = MFMA32(k0, q0[0], f32x16{}); SBAR(); EX2(sb, 6); opa(IC<hb>{}, IC<1>{}); EX2(sb, 8); PIN(sb); SBAR();
      sa = MFMA8(k1, q1[0], sa); SBAR();
      if constexpr (sst >= 1 && sst + 1 < 8 && sl == 0) vpiece(IC<sst + 1>{});
      if constexpr (sst == 0 && sl < 3) st128(rs, dstA[sl - 1] + (BR_A + (cj >= 10 ? 1024 : 2 * UNIT)), pk[2 * sl - 1]);
      if constexpr (sst == 1 && sl == 1) {
        st128(rs, obm, u32x4{~0u, ~0u, ~0u, ~0u}); st64(rs, obt, u32x2{~0u, ~0u});
      }
      if constexpr (sst == 2 && sl == 0) {
        const unsigned to = WS_QKB + BR_A + (unsigned)(256 * c + 32 * w + r) * 16;
        lq = ld128(rs, (ua + hh) * UNIT + to);
        lqt = ld64(rs, us * UNIT + to + 8 * e);
      }
      if constexpr (sst == 1 && sl == 0) {
#pragma unroll
        for (int i = 0; i < 5; ++i) { gx[i] = ((const unsigned*)(x + (size_t)(256 * c + 8 * h + w) * (D_ * 2)))[lane * 5 + i]; gg[i] = ((const unsigned*)gw)[lane * 5 + i]; }
      }
      if constexpr ((sst == 5 || sst == 7) && (sl == 1 || sl == 2)) {
#pragma unroll
        for (int j = (sst == 7 ? 5 : 0) + 3 * sl - 3; j < (sst == 7 ? 5 : 0) + (sl == 1 ? 3 : 5); ++j) {
          if (j % 5 < 4) of[j] = ld128(rs, WS_OA + osrc + j * 1024);
          else { const u32x2 a = ld64(rs, WS_OA + osrc - lane * 8 + j * 1024), b = ld64(rs, WS_OA + osrc - lane * 8 + j * 1024 + 512); of[j] = u32x4{a[0], a[1], b[0], b[1]}; }
        }
        if constexpr (sst == 7 && sl == 2) gate = __uint_as_float(ld32(rs, WS_G + (rt * 64 + tq * 16 + (lane & 15)) * 4));
      }
      if constexpr (sst == 7 && sl > 0) { wf[4 * sl - 2] = ldsr128(wl + WQ + (4 * sl - 2) * 1024); wf[4 * sl - 1] = ldsr128(wl + WQ + (4 * sl - 1) * 1024); }
      v1p = v1;
      const bf16x8 v0n = ldsr128(vp + nv); v1 = ldsr128(vp + nv + 1024);
      if constexpr (sst == 8) {
        f32x4* xw = (f32x4*)(lds + L_R + (2 * kh + 1) * WQ + tq * 3072) + lane;
#pragma unroll
        for (int k = 0; k < 3; ++k) xw[k * 64] = f32x4{oa[4 * k], oa[4 * k + 1], oa[4 * k + 2], oa[4 * k + 3]};
        SBAR();
      }
      EX2(sb, 10); opa(IC<hb>{}, IC<2>{}); EX2(sb, 12); PIN(sb); SBAR();
      ob = smf(v0, sb, 0, ob, idx);
      if constexpr (sst == 8) PIN(ob);
      SBAR();
      v0 = v0n;
      EX(sb, 14); opa(IC<hb>{}, IC<3>{}); EX(sb, 15); opa(IC<hb>{}, IC<4>{}); PIN(sb); SBAR();
      if constexpr (st == 2 && i == 3) local_attention();
    });
  });
  ob = smf(v1p, sb, 1, ob, idx); PIN(ob);
  __builtin_amdgcn_s_setprio(0);
  float ofin[12];
  {
    const f32x4* xr = (const f32x4*)(lds + L_R + (3 - 2 * kh) * WQ + tq * 3072) + lane;
    wait_bar<63>();
#pragma unroll
    for (int i = 0; i < 3; ++i) { const f32x4 v = xr[i * 64]; for (int k = 0; k < 4; ++k) ofin[4 * i + k] = ob[4 * i + k] + v[k]; }
  }
  {
    auto rr = __builtin_amdgcn_permlane32_swap(__float_as_uint(ofin[8]), __float_as_uint(ofin[8]), false, false);
    float l = __uint_as_float(rr[1]);
    if (__builtin_expect(__any(!(l > 8.67e-19f && l < 1.15e18f)), 0)) {
      const bf16x8 qa = q0[1]; s16x4 qb = q1[1];
      float m = -INFINITY;
#pragma unroll 1
      for (int pass = 0; pass < 2; ++pass) {
        set_ref(qb, pass ? m : 0.f, hh != e);
        f32x16 o = f32x16{};
#pragma unroll 1
        for (int kt = 0; kt < 64; ++kt) {
          const unsigned to = (unsigned)(32 * kt + r) * 16;
          const bf16x8 a0 = __builtin_bit_cast(bf16x8, ld128(rs, WS_QKB + (80 + ua + hh) * UNIT + to));
          const s16x4 a1 = __builtin_bit_cast(s16x4, ld64(rs, hh == e ? WS_QKB + (80 + us) * UNIT + to + 8 * e : WS_C + blockIdx.x * 4096));
          f32x16 s = MFMA32(a0, qa, f32x16{}); s = MFMA8(a1, qb, s);
          if (pass == 0) { m = fmaxf(m, max16(s)); continue; }
#pragma unroll
          for (int j = 0; j < 16; ++j) EX(s, j);
#pragma unroll
          for (int s2 = 0; s2 < 2; ++s2) {
            const u32x4 vv = ld128(rs, r < 20 ? WS_VB + (h * 64 + kt) * 2048 + (2 * s2 + hh) * 512 + r * 16 : WS_C + blockIdx.x * 4096 + (r == 20 ? 0 : 16));
            o = smf(__builtin_bit_cast(bf16x8, vv), s, s2, o, idx);
          }
        }
        if (pass == 0) m = swap_max(m);
        else {
#pragma unroll
          for (int i = 0; i < 12; ++i) ofin[i] = o[i];
        }
      }
      auto r2 = __builtin_amdgcn_permlane32_swap(__float_as_uint(ofin[8]), __float_as_uint(ofin[8]), false, false);
      l = __uint_as_float(r2[1]);
    }
    const float rl = __builtin_amdgcn_rcpf(l);
    unsigned ow[6];
#pragma unroll
    for (int i = 0; i < 6; ++i) ow[i] = min(cvtpk(ofin[2 * i] * rl, ofin[2 * i + 1] * rl), 0xfffffffeu);
    st128(rs, obm, u32x4{ow[0], ow[1], ow[2], ow[3]});
    st64(rs, obt, u32x2{ow[4], ow[5]});
  }
  SBAR();
#pragma unroll
  for (int f = 12; f < 25; ++f) wf[f] = ldsr128(wl + WQ + f * 1024);
  SBAR();

  f32x4 accB[5];
  u32x4 og[8]; u32x2 ot[4];
  const unsigned ob0 = osrc + SWP * 5120, ob1 = osrc + (SWP ^ 1) * 5120, ot0 = ob0 - lane * 8 + 4096, ot1 = ob1 - lane * 8 + 4096;
#define OBLD(k, r, a, o) "s_bitcmp1_b32 %17, " #k "\n\ts_cbranch_scc0 1f\n\tglobal_load_dwordx4 %" #r ", %" #a ", %16 offset:" #o " sc1\n1:\n\t"
#define OBLT(k, r0, r1, a) "s_bitcmp1_b32 %17, " #k "\n\ts_cbranch_scc0 1f\n\tglobal_load_dwordx2 %" #r0 ", %" #a ", %16 sc1\n\tglobal_load_dwordx2 %" #r1 ", %" #a ", %16 offset:512 sc1\n1:\n\t"
#define OB_ALL OBLD(0, 0, 12, 0) OBLD(1, 1, 12, 1024) OBLD(2, 2, 12, 2048) OBLD(3, 3, 12, 3072) OBLT(4, 8, 9, 14) OBLD(5, 4, 13, 0) OBLD(6, 5, 13, 1024) OBLD(7, 6, 13, 2048) OBLD(8, 7, 13, 3072) OBLT(9, 10, 11, 15)
#define OG(c) c(og[0]), c(og[1]), c(og[2]), c(og[3]), c(og[4]), c(og[5]), c(og[6]), c(og[7]), c(ot[0]), c(ot[1]), c(ot[2]), c(ot[3])
#define OBL1(k, a, o) og[k] = ld128(rs, WS_OB + a + o)
#define OBL2(k, a, o) ot[k] = ld64(rs, WS_OB + a + o)
#define OFR(j) ((j) % 5 < 4 ? og[((j) - (j) / 5) & 7] : u32x4{ot[2 * ((j) / 5)][0], ot[2 * ((j) / 5)][1], ot[2 * ((j) / 5) + 1][0], ot[2 * ((j) / 5) + 1][1]})
#pragma unroll
  for (int j = 0; j < 5; ++j) {
    if (j == 0) __builtin_amdgcn_s_setprio(3); if (j == 2) __builtin_amdgcn_s_setprio(2); if (j == 3) __builtin_amdgcn_s_setprio(1); if (j == 4) __builtin_amdgcn_s_setprio(0);
#pragma unroll
    for (int n = 0; n < 5; ++n) accA[n] = MFMA16(wf[j * 5 + n], of[5 + j], accA[n]);
    SBAR();
#pragma unroll
    for (int n = 0; n < 5; ++n) wf[j * 5 + n] = ldsr128(wl + (j * 5 + n) * 1024);
    if (j == 4) {
      OBL1(0, ob0, 0); OBL1(1, ob0, 1024); OBL1(2, ob0, 2048); OBL1(3, ob0, 3072); OBL2(0, ot0, 0); OBL2(1, ot0, 512);
      OBL1(4, ob1, 0); OBL1(5, ob1, 1024); OBL1(6, ob1, 2048); OBL1(7, ob1, 3072); OBL2(2, ot1, 0); OBL2(3, ot1, 512);
    }
    SBAR();
  }
  waitv_bar<12>();
  SBAR();
  const lds_cptr wh = (lds_cptr)lds + L_RING + kh * AT_STAGE + lane * 16;
#pragma unroll
  for (int j = 0; j < 10; ++j) {
    if (j % 5 == 0) {
      unsigned seen = 0;
#pragma unroll
      for (int k = j; k < j + 5; ++k) { const u32x4 v = OFR(k); seen = max(max(seen, v[0]), v[1]); seen = max(max(seen, v[2]), v[3]); }
      if (__any(seen == ~0u)) {
#pragma unroll 1
        for (unsigned spins = 0; spins < (1u << 16); ++spins) {
          unsigned pend = 0;
#pragma unroll
          for (int k = j; k < 10; ++k) { const u32x4 v = OFR(k); pend |= __any(max(max(v[0], v[1]), max(v[2], v[3])) == ~0u) ? (1u << k) : 0u; }
          if (!(pend & (0x1fu << j))) break;
          asm volatile(OB_ALL "s_waitcnt vmcnt(0)" : OG("+v") : "v"(ob0), "v"(ob1), "v"(ot0), "v"(ot1), "s"(ws + WS_OB), "s"(pend) : "memory", "scc");
        }
      }
    }
    if (j == 0) __builtin_amdgcn_s_setprio(3); if (j == 3) __builtin_amdgcn_s_setprio(2); if (j == 6) __builtin_amdgcn_s_setprio(1); if (j == 8) __builtin_amdgcn_s_setprio(0);
#pragma unroll
    for (int n = 0; n < 5; ++n) accB[n] = MFMA16(wf[(j % 5) * 5 + n], OFR(j), j == 0 ? f32x4{} : accB[n]);
    if (j < 5) {
#pragma unroll
      for (int n = 0; n < 5; ++n) wf[j * 5 + n] = ldsr128(wh + (j * 5 + n) * 1024);
    }
    SBAR();
  }
#pragma unroll
  for (int n = 0; n < 5; ++n) accB[n] = accA[n] * gate + accB[n] * (1.f - gate);
  {
    char* img = lds + L_R + (2 * kh + 1) * WQ + (tq * 16 + (lane & 15)) * 336 + (lane >> 4) * 16;
#pragma unroll
    for (int n = 0; n < 5; ++n) *(f32x4*)(img + n * 64) = accB[n];
    wait_bar<63>();
#pragma unroll
    for (int it = 0; it < 2; ++it) {
      const int u = tid + 512 * it, row = u / 10, pc = u - 10 * row;
      if (u < 640) {
        const char* a = lds + L_R + WQ + row * 336 + pc * 32;
        const f32x4 v0 = *(const f32x4*)a + *(const f32x4*)(a + 2 * WQ), v1 = *(const f32x4*)(a + 16) + *(const f32x4*)(a + 2 * WQ + 16);
        const u32x4 r4 = u32x4{cvtpk(v0[0], v0[1]), cvtpk(v0[2], v0[3]), cvtpk(v1[0], v1[1]), cvtpk(v1[2], v1[3])};
        char* op = out + (size_t)(rt * 64 + row) * (D_ * 2) + ct * 160 + pc * 16;
        *(u32x4*)op = r4;
      }
    }
  }
  asm volatile("GM_CODE_END:");
  if (keeper && !advanced) {
#pragma unroll 1
    for (int i = 0; i < 4; ++i) wait_flags(flg, FOA + i * 64 + lane, tag);
    if (lane == 0) __hip_atomic_store(flg, tag, RLX_AGENT);
  }
}

extern "C" void kernel_launch(void* const* d_in, const int* in_sizes, int n_in, void* d_out, int out_size, void* d_ws,
                              size_t ws_size, hipStream_t stream) {
  (void)hipFuncSetAttribute((const void*)fused_kernel, hipFuncAttributeMaxDynamicSharedMemorySize, LDS_BYTES);
  fused_kernel<<<256, 512, LDS_BYTES, stream>>>((const char*)d_in[0], (const char*)d_in[1], (const char*)d_in[4], (const char*)d_in[2],
                                                (const char*)d_in[5], (const char*)d_in[6], (char*)d_ws, (char*)d_out);
}
```

```cpp
#include <hip/hip_runtime.h>
#include <hip/hip_bf16.h>
#include <stdint.h>

typedef short bf16x8 __attribute__((ext_vector_type(8)));
typedef short s16x4 __attribute__((ext_vector_type(4)));
typedef __bf16 bfv8 __attribute__((ext_vector_type(8)));
typedef __bf16 bfv2 __attribute__((ext_vector_type(2)));
typedef __bf16 bfv16 __attribute__((ext_vector_type(16)));
typedef float f32x16 __attribute__((ext_vector_type(16)));
typedef float f32x8 __attribute__((ext_vector_type(8)));
typedef float f32x4 __attribute__((ext_vector_type(4)));
typedef unsigned u32x4 __attribute__((ext_vector_type(4)));
typedef unsigned u32x2 __attribute__((ext_vector_type(2)));
typedef unsigned long long u64;
#define LAS __attribute__((address_space(3)))
#define GAS __attribute__((address_space(1)))
#define AINL __attribute__((always_inline))
typedef LAS const char* lds_cptr;
typedef LAS char* lds_ptr;
typedef GAS u64 gu64;

constexpr int D_ = 640, N3_ = 1920;
constexpr u64 TAG_STEP = 0x9E3779B97F4A7C15ull;
constexpr float QSCALE = 0.22360679774997896f * 1.4426950408889634f;
constexpr unsigned OOB = 0x70000000u;

constexpr unsigned UNIT = 2048 * 16;
constexpr unsigned F1A = 512, FG = 768, FOA = 1024, F1B = 2048, NREP = 16;
constexpr unsigned WS_QKB = (2048 + NREP * 256) * 128;
constexpr unsigned WS_VB = WS_QKB + 160 * UNIT;
constexpr unsigned BR_A = 160 * UNIT + 32 * 64 * 2048;
constexpr unsigned WS_OA = WS_QKB + 2 * BR_A;
constexpr unsigned WS_OB = WS_OA + 2048 * 640 * 2;
constexpr unsigned WS_WO = WS_OB + 2048 * 640 * 2;
constexpr unsigned WS_G = WS_WO + 2 * 640 * 640 * 2;
constexpr unsigned WS_C = WS_G + 2048 * 4;
constexpr unsigned WS_END = WS_C + 256 * 4096;

constexpr int L_R = 0, WQ = 25600;
constexpr int L_RING = 106496, AT_HALF = 14336, AT_STAGE = 2 * AT_HALF;
constexpr int L_FL = 104448, L_EP = 98304, L_PF = 81920, L_ALL = 102400, L_Q = 0;
constexpr int L_LK = 51200, LA_UNIT = 4608, L_LV = L_LK + 3 * LA_UNIT;
constexpr int LDS_BYTES = 163840;

constexpr int BST = 2;
#define SBAR() __builtin_amdgcn_sched_barrier(0)
#define PIN(x) asm volatile("" : "+v"(x))
#define MFMA32(a, b, c) __builtin_amdgcn_mfma_f32_32x32x16_bf16(__builtin_bit_cast(bfv8, a), __builtin_bit_cast(bfv8, b), c, 0, 0, 0)
#define MFMA8(a, b, c) __builtin_amdgcn_mfma_f32_32x32x8bf16_1k(a, b, c, 0, 0, 0)
#define MFMA16(a, b, c) __builtin_amdgcn_mfma_f32_16x16x32_bf16(__builtin_bit_cast(bfv8, a), __builtin_bit_cast(bfv8, b), c, 0, 0, 0)
#define RLX_AGENT __ATOMIC_RELAXED, __HIP_MEMORY_SCOPE_AGENT
#define SC1 16

template <int V> struct IC { static constexpr int value = V; };
template <int I, int N, class F> __device__ __forceinline__ void sfor(F&& f) { if constexpr (I < N) { f(IC<I>{}); sfor<I + 1, N>(f); } }
template <int N> __device__ __forceinline__ void wait_bar() { asm volatile("s_waitcnt vmcnt(%0) lgkmcnt(0)\n\ts_barrier" :: "i"(N) : "memory"); }
template <int N> __device__ __forceinline__ void waitv_bar() { asm volatile("s_waitcnt vmcnt(%0)\n\ts_barrier" :: "i"(N) : "memory"); }

__device__ __forceinline__ unsigned cvtpk(float lo, float hi) {
  bfv2 r; r[0] = (__bf16)lo; r[1] = (__bf16)hi;
  return __builtin_bit_cast(unsigned, r);
}
__device__ __forceinline__ float dot2(unsigned a, unsigned b, float c) {
  return __builtin_amdgcn_fdot2_f32_bf16(__builtin_bit_cast(bfv2, a), __builtin_bit_cast(bfv2, b), c, false);
}
template <int OFF> __device__ __forceinline__ void dma(const char* base, unsigned voff, unsigned lds_base) {
  asm volatile("s_add_u32 m0, %2, %3\n\ts_nop 0\n\tglobal_load_lds_dwordx4 %0, %1" :: "v"(voff), "s"(base), "s"(lds_base), "i"(OFF) : "memory", "scc");
}
template <int OFF> __device__ __forceinline__ void dma_sc1(const char* base, unsigned voff, unsigned lds_base) {
  asm volatile("s_add_u32 m0, %2, %3\n\ts_nop 0\n\tglobal_load_lds_dwordx4 %0, %1 sc1" :: "v"(voff), "s"(base), "s"(lds_base), "i"(OFF) : "memory", "scc");
}
template <int OFF> __device__ __forceinline__ void dma2_sc1(const char* base, unsigned voff, unsigned lds_base) {
  asm volatile("s_add_u32 m0, %2, %3\n\ts_nop 0\n\tglobal_load_lds_dwordx4 %0, %1 sc1\n\tglobal_load_lds_dwordx4 %0, %1 offset:1024 sc1"
               :: "v"(voff), "s"(base), "s"(lds_base), "i"(OFF) : "memory", "scc");
}
template <int OFF> __device__ __forceinline__ void dma3_sc1(const char* base, unsigned voff, unsigned lds_base) {
  asm volatile("s_add_u32 m0, %2, %3\n\ts_nop 0\n\tglobal_load_lds_dwordx4 %0, %1 sc1\n\tglobal_load_lds_dwordx4 %0, %1 offset:1024 sc1\n\t"
               "global_load_lds_dwordx4 %0, %1 offset:2048 sc1" :: "v"(voff), "s"(base), "s"(lds_base), "i"(OFF) : "memory", "scc");
}
typedef short v4i16_t __attribute__((ext_vector_type(4)));
__device__ __forceinline__ s16x4 trd(lds_cptr p) {
  return __builtin_bit_cast(s16x4, __builtin_amdgcn_ds_read_tr16_b64_v4i16((LAS v4i16_t*)p));
}
__device__ __forceinline__ bf16x8 cat8(s16x4 a, s16x4 b) { return __builtin_shufflevector(a, b, 0, 1, 2, 3, 4, 5, 6, 7); }
__device__ __forceinline__ bf16x8 ldsr128(lds_cptr p) { return *(const LAS bf16x8*)p; }
__device__ __forceinline__ s16x4 ldsr64(lds_cptr p) { return *(const LAS s16x4*)p; }
__device__ __forceinline__ u32x4 pack8(const f32x16& a, int s, float sc) {
  return u32x4{cvtpk(a[8 * s] * sc, a[8 * s + 1] * sc), cvtpk(a[8 * s + 2] * sc, a[8 * s + 3] * sc),
               cvtpk(a[8 * s + 4] * sc, a[8 * s + 5] * sc), cvtpk(a[8 * s + 6] * sc, a[8 * s + 7] * sc)};
}
__device__ __forceinline__ int swap23(int r) { return (r & 0x13) | ((r & 4) << 1) | ((r & 8) >> 1); }
__device__ __forceinline__ u64 uni64(unsigned lo, unsigned hi) {
  return ((u64)(unsigned)__builtin_amdgcn_readfirstlane(hi) << 32) | (unsigned)__builtin_amdgcn_readfirstlane(lo);
}

typedef __amdgpu_buffer_rsrc_t rsrc_t;
__device__ __forceinline__ void st128(rsrc_t rs, unsigned off, u32x4 v) { __builtin_amdgcn_raw_buffer_store_b128(v, rs, off, 0, SC1); }
__device__ __forceinline__ void st64(rsrc_t rs, unsigned off, u32x2 v) { __builtin_amdgcn_raw_buffer_store_b64(v, rs, off, 0, SC1); }
__device__ __forceinline__ u32x4 ld128(rsrc_t rs, unsigned off) { return __builtin_amdgcn_raw_buffer_load_b128(rs, off, 0, SC1); }
__device__ __forceinline__ u32x2 ld64(rsrc_t rs, unsigned off) { return __builtin_amdgcn_raw_buffer_load_b64(rs, off, 0, SC1); }
__device__ __forceinline__ unsigned ld32(rsrc_t rs, unsigned off) { return __builtin_amdgcn_raw_buffer_load_b32(rs, off, 0, SC1); }

template <int SLEEP = 6>
__device__ __forceinline__ void wait_flags(gu64* flg, unsigned idx, u64 tag) {
  bool ok = false;
#pragma unroll 1
  for (unsigned spins = 0; spins < (1u << 18); ++spins) {
    if (!ok) ok = __hip_atomic_load(flg + idx * 16, RLX_AGENT) == tag;
    if (__all(ok)) break;
    __builtin_amdgcn_s_sleep(SLEEP);
  }
  asm volatile("" ::: "memory");
}
__device__ __forceinline__ void sample_flags(u64& fa, u64& fb, unsigned ia, unsigned ib, const char* base, u64 miss) {
  asm volatile("s_cmp_eq_u64 %5, 0\n\ts_cbranch_scc1 1f\n\tglobal_load_dwordx2 %0, %2, %4 sc1\n\tglobal_load_dwordx2 %1, %3, %4 sc1\n1:"
               : "+v"(fa), "+v"(fb) : "v"(ia * 128), "v"(ib * 128), "s"(base), "s"(miss) : "memory", "scc");
}

__device__ __forceinline__ unsigned o_main(int tok, int h, int hh) {
  const int m = h >> 1;
  return (unsigned)((((tok >> 4) * 20 + 5 * (m >> 2) + (m & 3)) * 64 + (2 * (h & 1) + hh) * 16 + (tok & 15)) * 16);
}
__device__ __forceinline__ unsigned o_tail(int tok, int h) {
  const int m = h >> 1;
  return (unsigned)(((tok >> 4) * 20 + 5 * (m >> 2) + 4) * 1024 + 512 * (m & 1) + ((2 * ((m >> 1) & 1) + (h & 1)) * 16 + (tok & 15)) * 8);
}

#define G1_ORDER "BBBBBBBBBBAAAAAAAAAA"
constexpr int G1_PUB = 14;
constexpr int G1_EP = 12;
constexpr int G1_SAMP = 72, G1_LOOK = 76;
struct G1 {
  int isB[24] = {}, kb[24] = {}, uf[25] = {};
  int lastB = 0;
  int iss_lo[20] = {}, iss_hi[20] = {};
  int wait[20] = {};
  constexpr G1() {
    const char* ord = G1_ORDER;
    int nb = 0, na = 0;
    for (int o = 0; o < 24; ++o) {
      isB[o] = o < 20 && ord[o] == 'B'; kb[o] = o < 20 ? (isB[o] ? nb++ : na++) : 0;
      uf[o + 1] = uf[o] + (o < 20 ? 1 + isB[o] : 0);
      if (isB[o]) lastB = o;
    }
    int pos[60] = {}, cnt = 0, done = 2 * uf[2];
    for (int d = 0; d < done; ++d) pos[d] = cnt++;
    for (int o = 0; o < 20; ++o) {
      int need = pos[2 * uf[o + 1] - 1];
      wait[o] = (o >= 18 || o == G1_PUB) ? 0 : cnt - 1 - need;
      if (o == G1_EP) cnt++;
      iss_lo[o] = done; iss_hi[o] = 2 * uf[o == G1_PUB - 4 ? o + 5 : (o >= G1_PUB - 3 && o < G1_PUB) ? G1_PUB + 3 : o + 4];
      for (int ks = 0; ks < 4; ++ks) {
        const int per = (iss_hi[o] - iss_lo[o] + 3) / 4;
        for (int z = 0; z < per && done < iss_hi[o]; ++z) pos[done++] = cnt++;
        const int n = 4 * o + ks - 2 - (4 * lastB + 3);
        if (n >= 0 && n < 4 * BST && n % BST == 0) cnt++;
      }
    }
  }
};
constexpr int g1_slot(int u) { return ((u + 6) % 10) * 16384; }
template <bool VT, class FPiece, class FStart, class FSetup>
__device__ __forceinline__ void g1_body(char* ws, rsrc_t rs, char* lds, int ti, int cj, int w, int lane, u64& tag, bool& early, unsigned& fiKV, unsigned& fiQ,
                                        u32x4 (&pk)[4], unsigned (&dstB)[2], FPiece&& piece, FStart&& att_start, FSetup&& setup) {
  constexpr G1 P;
  static_assert(P.uf[G1_EP - 1] > 20 && G1_EP < G1_PUB && P.uf[4] <= 9, "epoch / prefetch scratch slots");
  const int tg = w & 3, cg = w >> 2;
  const int r = lane & 31, hh = lane >> 5, g16 = (lane >> 4) & 1, q4 = (lane & 15) >> 2, p4 = lane & 3;
  const int tr = VT ? swap23(r) : r;
  unsigned xoff[4];
#pragma unroll
  for (int ks = 0; ks < 4; ++ks) xoff[ks] = (unsigned)((32 * tg + tr) * 128 + (((2 * ks + hh) ^ ((tr >> 1) & 7)) * 16));
  const int psw = ((p4 & 1) << 1) | (p4 >> 1);
  unsigned woff[2];
#pragma unroll
  for (int t2 = 0; t2 < 2; ++t2) woff[t2] = (unsigned)((8 * hh + q4) * 256 + (((2 * cg + t2) ^ q4) * 64) + 32 * g16 + 8 * (VT ? p4 : psw));
  const float sc = (!VT && cj < 5) ? QSCALE : 1.f;
  bf16x8 xf[40], wf[3][2];
  u64 fa, fb;
  f32x16 accB[2], accA[2];
  u64 miss = 1;
  auto look = [&](bool again) AINL {
    asm volatile("s_cmp_eq_u64 %2, 0\n\ts_cbranch_scc1 1f\n\ts_waitcnt vmcnt(0)\n1:" : "+v"(fa), "+v"(fb) : "s"(miss) : "memory", "scc");
    const u64 m = __ballot(fa != tag || fb != tag);
    if (m == 0 && miss != 0) att_start();
    miss = m;
    if (again) sample_flags(fa, fb, fiKV, fiQ, ws + (F1B + 256) * 128, miss);
  };
  sfor<0, 82>([&](auto ic) AINL {
    constexpr int nn = decltype(ic)::value, n = nn - 2;
    constexpr int o = nn >> 2, ks = nn & 3;
    if constexpr (nn < 80) {
      if constexpr (ks == 0) {
        if constexpr (o < 19) waitv_bar<P.wait[o]>();
        if constexpr (o == G1_EP) dma_sc1<L_EP>(ws, 0, (unsigned)(uintptr_t)lds + w * 1024);
        if constexpr (o == G1_PUB) { const u32x2 ep = *(const LAS u32x2*)((lds_cptr)lds + L_EP + w * 1024); tag = uni64(ep[0], ep[1]) + TAG_STEP; }
        if constexpr (o == G1_PUB) { if (w == 0 && lane < NREP) __hip_atomic_store((gu64*)ws + (F1B + lane * 256 + cj * 16 + ti) * 16, tag, RLX_AGENT); }
      }
      if constexpr (nn >= 62 && nn < 70 && !(nn & 1)) setup(IC<(nn - 62) / 2>{});
      if constexpr (nn == G1_SAMP) { fa = 0; fb = 0; sample_flags(fa, fb, fiKV, fiQ, ws + F1B * 128, miss); }
      if constexpr (nn == G1_LOOK) look(true);
      if constexpr (P.isB[o]) xf[4 * P.kb[o] + ks] = ldsr128((lds_cptr)lds + g1_slot(P.uf[o] + 1) + xoff[ks]);
#pragma unroll
      for (int t2 = 0; t2 < 2; ++t2) {
        const lds_cptr wp = (lds_cptr)lds + g1_slot(P.uf[o]) + woff[t2] + ks * 4096;
        wf[nn % 3][t2] = cat8(trd(wp), trd(wp + 1024));
      }
      constexpr int per = (P.iss_hi[o] - P.iss_lo[o] + 3) / 4, d0 = P.iss_lo[o] + per * ks;
      sfor<d0, (d0 + per < P.iss_hi[o] ? d0 + per : P.iss_hi[o])>(piece);
    }
    if constexpr (n >= 0) {
      constexpr int om = n >> 2, xi = 4 * P.kb[om] + (n & 3);
#pragma unroll
      for (int t2 = 0; t2 < 2; ++t2) {
        f32x16& ac = P.isB[om] ? accB[t2] : accA[t2];
        const f32x16 cin = xi == 0 ? f32x16{} : ac;
        ac = VT ? MFMA32(xf[xi], wf[n % 3][t2], cin) : MFMA32(wf[n % 3][t2], xf[xi], cin);
      }
    }
    SBAR();
    constexpr int nb = n - (4 * P.lastB + 3);
    if constexpr (nb == 0) {
#pragma unroll
      for (int i = 0; i < 4; ++i) { pk[i] = VT ? pack8(accB[i >> 1], i & 1, 1.f) : pack8(accB[i >> 1], i & 1, sc); PIN(pk[i]); }
      int ln = lane; PIN(ln);
#pragma unroll
      for (int t2 = 0; t2 < 2; ++t2) {
        if (VT) {
          const int F = (cj - 10) * 128 + 64 * cg + 32 * t2 + (ln & 31), hd = F / 20;
          int d = F - 20 * hd - 4 * (hd & 1); if (d < 0) d += 20;
          dstB[t2] = WS_VB + (unsigned)((hd * 64 + 4 * ti + tg) * 2048 + (ln >> 5) * 512 + d * 16);
        } else {
          dstB[t2] = WS_QKB + (unsigned)(cj * 16 + 8 * cg + 4 * t2 + (ln >> 5)) * UNIT + (unsigned)(128 * ti + 32 * tg + (ln & 31)) * 16;
        }
      }
    }
    if constexpr (nb >= 0 && nb < 4 * BST && nb % BST == 0) {
      constexpr int i = nb / BST;
      st128(rs, dstB[i >> 1] + (i & 1) * (VT ? 1024 : 2 * UNIT), pk[i]);
      SBAR();
    }
  });
  look(false);
  early = miss == 0;
#pragma unroll
  for (int i = 0; i < 4; ++i) pk[i] = pack8(accA[i >> 1], i & 1, 1.f);
}

__device__ __forceinline__ f32x16 smf(bf16x8 a, const f32x16& p, int s, f32x16 o, int idx) {
  f32x8 b = s ? __builtin_shufflevector(p, p, 8, 9, 10, 11, 12, 13, 14, 15) : __builtin_shufflevector(p, p, 0, 1, 2, 3, 4, 5, 6, 7);
  return __builtin_amdgcn_smfmac_f32_32x32x32_bf16(__builtin_bit_cast(bfv8, a), __builtin_bit_cast(bfv16, b), o, idx, 0, 0);
}
__device__ __forceinline__ float max16(const f32x16& s) {
  float a = fmaxf(fmaxf(s[0], s[1]), s[2]), b = fmaxf(fmaxf(s[3], s[4]), s[5]);
  a = fmaxf(fmaxf(a, s[6]), s[7]); b = fmaxf(fmaxf(b, s[8]), s[9]);
  a = fmaxf(fmaxf(a, s[10]), s[11]); b = fmaxf(fmaxf(b, s[12]), s[13]);
  a = fmaxf(fmaxf(a, s[14]), s[15]);
  return fmaxf(a, b);
}
__device__ __forceinline__ float swap_max(float m) {
  auto rr = __builtin_amdgcn_permlane32_swap(__float_as_uint(m), __float_as_uint(m), false, false);
  return fmaxf(__uint_as_float(rr[0]), __uint_as_float(rr[1]));
}
__device__ __forceinline__ float swap_add(float m) {
  auto rr = __builtin_amdgcn_permlane32_swap(__float_as_uint(m), __float_as_uint(m), false, false);
  return __uint_as_float(rr[0]) + __uint_as_float(rr[1]);
}
__device__ __forceinline__ void set_ref(s16x4& q1, float m, bool spare) {
  const unsigned a = __float_as_uint(-m) & 0xffff0000u;
  const float r1 = -m - __uint_as_float(a);
  const unsigned b = __float_as_uint(r1) & 0xffff0000u;
  const unsigned c = __float_as_uint(r1 - __uint_as_float(b));
  if (spare) q1 = __builtin_bit_cast(s16x4, u32x2{(a >> 16) | b, c >> 16});
}

__global__ __launch_bounds__(512, 2) void fused_kernel(const char* __restrict__ x, const char* __restrict__ wA, const char* __restrict__ wB,
                                                       const char* __restrict__ woA, const char* __restrict__ woB,
                                                       const char* __restrict__ gw, char* ws, char* __restrict__ out) {
  extern __shared__ __attribute__((aligned(16))) char lds[];
  const int tid = threadIdx.x, lane = tid & 63;
  const int w = __builtin_amdgcn_readfirstlane(tid >> 6);
  const int xcd = blockIdx.x & 7, slot = blockIdx.x >> 3;
  const unsigned lds0 = (unsigned)(uintptr_t)lds;
  gu64* flg = (gu64*)ws;
  const rsrc_t rs = __builtin_amdgcn_make_buffer_rsrc(ws, 0, WS_END, 0x00020000);
  const int ti = 8 * (xcd & 1) + (slot & 7);
  const int cj = 4 * (xcd >> 1) + (slot >> 3);
  const unsigned ldpf = lds0 + (cj < 15 ? L_PF : L_RING);
  if (w == 6) {
    u64 pc; unsigned len;
    asm volatile("s_getpc_b64 %0\nGM_CODE_BEG:\n\ts_mov_b32 %1, GM_CODE_END-GM_CODE_BEG" : "=s"(pc), "=s"(len));
    asm volatile("s_add_u32 m0, %2, %3\n\ts_nop 0\n\tglobal_load_lds_dword %0, %1"
                 :: "v"(min((unsigned)((slot & 7) * 64 + lane) * 128, len)), "s"(pc), "s"(ldpf), "i"(0) : "memory", "scc");
  }
  const int rot = ((slot & 7) * 5 / 4 + (slot >> 3) * 5 / 2 + xcd) % 10;
  unsigned xlo[2], wlo[2];
#pragma unroll
  for (int i = 0; i < 2; ++i) {
    const int row = 16 * w + 8 * i + (lane >> 3);
    xlo[i] = (unsigned)(row * (D_ * 2) + (((lane & 7) ^ ((row >> 1) & 7)) * 16));
    wlo[i] = (unsigned)((8 * w + 4 * i + (lane >> 4)) * (N3_ * 2) + (((lane & 15) ^ (4 * (lane >> 4))) * 16));
  }
  const char* xb = x + (size_t)(128 * ti) * (D_ * 2);
  const char* wBb = wB + cj * 256;
  const char* wAb = wA + cj * 256;
  const unsigned ldw = lds0 + w * 2048;
  auto piece = [&](auto dc) AINL {
    constexpr G1 P;
    constexpr int d = decltype(dc)::value, u = d >> 1, base = g1_slot(u) + (d & 1) * 1024;
    constexpr int o = [&]() { int q = 0; while (P.uf[q + 1] <= u) ++q; return q; }();
    int kb = P.kb[o] + rot; if (kb >= 10) kb -= 10;
    if constexpr (u != P.uf[o]) dma<base>(xb + kb * 128, xlo[d & 1], ldw);
    else dma<base>((P.isB[o] ? wBb : wAb) + (size_t)kb * (64 * N3_ * 2), wlo[d & 1], ldw);
  };
  if (cj < 15) {
    sfor<0, 8>(piece);
  }
  SBAR();
  u64 tag;
  const int h = 4 * xcd + (slot & 3), c = slot >> 2;
  const int e = h & 1, ua = 5 * (h >> 1) + 3 * e, us = 5 * (h >> 1) + 2;
  const int jlo = (5 * (h >> 1) + 2 * e) >> 4, jhi = (5 * (h >> 1) + 2 * e + 2) >> 4;
  const int kh = w >> 2, tq = w & 3;
  const char* kbase = ws + WS_QKB + (size_t)(80 + (tq >= 2 ? us : ua + tq)) * UNIT + 1024 * kh * 16;
  if (w == 5) { const unsigned cv = (lane & 1) ? 0u : 0x3f803f80u; st128(rs, lane < 8 ? WS_C + blockIdx.x * 4096 + (lane >> 1) * 1024 + (lane & 1) * 16 : OOB, u32x4{cv, cv, cv, cv}); }
  const unsigned ldr = lds0 + kh * AT_HALF + tq * 2048;
  unsigned vlo, vstr;
  auto kpiece = [&](auto stc) AINL {
    constexpr int st = decltype(stc)::value;
    if (tq < 3) dma2_sc1<L_RING + (st & 1) * AT_STAGE>(kbase + st * 2048, lane * 16, ldr);
  };
  auto vpiece = [&](auto stc) AINL {
    constexpr int st = decltype(stc)::value;
    dma2_sc1<L_RING + (st & 1) * AT_STAGE + 6144>(ws, vlo + st * vstr, ldr);
  };
  unsigned fiKV, fiQ;
  auto att_start = [&]() AINL {
    kpiece(IC<0>{});
    if (tq == 3) {
      const char* qb = ws + WS_QKB + (256 * c + 128 * kh) * 16;
      dma2_sc1<L_Q>(qb + (size_t)ua * UNIT, lane * 16, lds0 + kh * 2048);
      dma2_sc1<L_Q + 4096>(qb + (size_t)(ua + 1) * UNIT, lane * 16, lds0 + kh * 2048);
      dma2_sc1<L_Q + 8192>(qb + (size_t)us * UNIT, lane * 16, lds0 + kh * 2048);
    }
    vpiece(IC<0>{});
  };

  const int hh = lane >> 5, r = lane & 31;
  const int rt = 4 * xcd + (slot & 3), ct = slot >> 2;
  const int SWP = ct & 1;
  const char* wob = ws + WS_WO + (size_t)ct * 102400 + w * 3072;
  auto woquarter = [&](auto brc, auto qc, auto dc) AINL {
    constexpr int br = decltype(brc)::value, q = decltype(qc)::value, d = decltype(dc)::value;
    const int off = br * 819200 + (q ^ (br & SWP)) * WQ;
    dma3_sc1<d>(wob + off, lane * 16, lds0 + w * 3072);
    if (w == 0) dma_sc1<d + 24576>(wob + off + 24576, lane * 16, lds0);
  };
  const unsigned ladst = lds0 + (w < 3 ? L_LK + w * LA_UNIT : L_LV + min(4 * (w - 3), 13) * 1024);
  unsigned osrc, fiA, obm, obt, lalo; int idx;
  lds_cptr kp0, kp1, vp, qp;
  f32x16 oa, ob;
  auto setup = [&](auto pc) AINL {
    constexpr int p = decltype(pc)::value;
    int ln = lane; PIN(ln);
    const int hh = ln >> 5, r = ln & 31;
    if constexpr (p == 0) {
      const unsigned crow = WS_C + blockIdx.x * 4096 + (r == 20 ? 0 : 16);
      vlo = r < 20 ? WS_VB + (h * 64 + 32 * kh + tq) * 2048 + ln * 16 : crow; vstr = r < 20 ? 8192u : 0u;
      const unsigned lab = w < 3 ? WS_QKB + BR_A + (80 + (w == 2 ? us : ua + w)) * UNIT + (256 * c - 16) * 16
                                 : WS_VB + BR_A + (h * 64 + 8 * c) * 2048 - 1024 + min(4 * (w - 3), 13) * 1024;
      lalo = (w < 3 || r < 20) ? lab + ln * 16 : crow;
      osrc = (unsigned)((rt * 4 + tq) * 20 + 10 * kh) * 1024 + ln * 16;
      PIN(vlo); PIN(vstr); PIN(lalo); PIN(osrc);
    }
    if constexpr (p == 1) {
      fiA = ln < 18 ? F1A + (5 * (ln / 6) + (((ln / 3) & 1) ? jhi : jlo)) * 16 + max(2 * c - 1 + ln % 3, 0) : FG + (ln == 18 ? ct : 8 + ct);
      obm = WS_OB + o_main(256 * c + 64 * tq + 32 * kh + r, h, hh); obt = (hh ? OOB : 0u) + WS_OB + o_tail(256 * c + 64 * tq + 32 * kh + r, h);
      PIN(fiA); PIN(obm); PIN(obt);
    }
    if constexpr (p == 2) {
      const lds_cptr ring = (lds_cptr)lds + L_RING + kh * AT_HALF;
      kp0 = ring + hh * 2048 + r * 16;
      kp1 = (hh == e) ? ring + 2 * 2048 + r * 16 + 8 * hh : ring + 6144 + 20 * 16;
      vp = ring + 6144 + hh * 512 + r * 16;
      qp = (lds_cptr)lds + L_Q + (64 * tq + r) * 16;
      idx = 0xDDDD;
      PIN(kp0); PIN(kp1); PIN(vp); PIN(qp); PIN(idx);
    }
    if constexpr (p == 3) {
      oa = f32x16{}; ob = f32x16{}; PIN(oa); PIN(ob);
      fiKV = (5 + 5 * hh + ((ln & 16) ? jhi : jlo)) * 16 + (ln & 15); fiQ = ((ln & 1) ? jhi : jlo) * 16 + 2 * c + ((ln >> 1) & 1);
      PIN(fiKV); PIN(fiQ);
    }
  };

  bool early = false;
  u32x4 pk[4]; unsigned dstA[2];
  if (cj < 10) g1_body<false>(ws, rs, lds, ti, cj, w, lane, tag, early, fiKV, fiQ, pk, dstA, piece, att_start, setup);
  else if (cj < 15) g1_body<true>(ws, rs, lds, ti, cj, w, lane, tag, early, fiKV, fiQ, pk, dstA, piece, att_start, setup);
  else {
    const char* wo = ((ti & 8) ? woB : woA) + (size_t)(80 * w) * (D_ * 2) + (ti & 7) * 160;
#pragma unroll 1
    for (int p = 0; p < 13; ++p) {
      const int s0 = min(64 * p, 736), sl = s0 + lane;
      dma<0>(wo, (unsigned)((sl / 10) * (D_ * 2) + (sl % 10) * 16), lds0 + w * 12800 + s0 * 16);
    }
    const u64 ep = __hip_atomic_load(flg, RLX_AGENT);
    tag = uni64((unsigned)ep, (unsigned)(ep >> 32)) + TAG_STEP;
    wait_bar<0>();
    const int g4 = lane >> 4, rl = 320 * kh + 4 * (g4 & 1) + ((lane & 15) >> 2);
#pragma unroll
    for (int ff = 0; ff < 13; ++ff) {
      const int f = min(tq + 4 * ff, 49), j = f / 5, n = f - 5 * j;
      const bool mn = j % 5 < 4;
      const int row = rl + 160 * (j / 5) + (mn ? 40 * (j % 5) + 24 * (g4 >> 1) : 80 * (g4 >> 1) + 16);
      const lds_cptr p = (lds_cptr)lds + row * 160 + n * 32 + 8 * (lane & 3);
      st128(rs, WS_WO + (unsigned)(ti * 100 + kh * 50 + f) * 1024 + lane * 16, __builtin_bit_cast(u32x4, cat8(trd(p), trd(p + (mn ? 8 : 40) * 160))));
    }
    wait_bar<0>();
    if (tid == 0) __hip_atomic_store(flg + (FG + ti) * 16, tag, RLX_AGENT);
    if (w == 0) { wait_flags<16>(flg, F1B + (NREP - 1) * 256 + jlo * 16 + 2 * c, tag); }
    wait_bar<63>();
    sfor<0, 4>(setup);
    dstA[0] = OOB; dstA[1] = OOB;
#pragma unroll
    for (int i = 0; i < 4; ++i) pk[i] = u32x4{0u, 0u, 0u, 0u};
  }
  const unsigned flag1 = F1A + cj * 16 + ti;

  if (!early) {
    bool oka = false, okb = false;
#pragma unroll 1
    for (unsigned spins = 0; spins < (1u << 18); ++spins) {
      const unsigned rp = F1B + (2 + min(spins, NREP - 4)) * 256;
      const u64 a = __hip_atomic_load(flg + (rp + (oka ? fiQ : fiKV)) * 16, RLX_AGENT), b = __hip_atomic_load(flg + (rp + fiQ) * 16, RLX_AGENT);
      oka = oka || a == tag; okb = okb || b == tag;
      if (__all(oka && okb)) break;
      __builtin_amdgcn_s_sleep(2);
    }
    asm volatile("" ::: "memory");
    att_start();
  }
  wait_bar<0>();
  float gate;
  const unsigned fiOA = FOA + (rt >> 2) * 32 + r;
  u32x4 lq; u32x2 lqt;
  f32x16 sa, sb;
  bf16x8 q0[2]; s16x4 q1[2];
#pragma unroll
  for (int t = 0; t < 2; ++t) {
    q0[t] = ldsr128(qp + hh * 4096 + ((t ^ kh ^ 1) * 512));
    q1[t] = ldsr64(qp + 8192 + 8 * e + ((t ^ kh ^ 1) * 512));
  }
  const bf16x8 kr0 = ldsr128(kp0 - kh * AT_HALF); const s16x4 kr1 = ldsr64(kp1 - kh * AT_HALF);
  bf16x8 k0 = ldsr128(kp0); s16x4 k1 = ldsr64(kp1);
  bf16x8 v0 = ldsr128(vp), v1 = ldsr128(vp + 1024);
  bf16x8 v1p = v1;
  SBAR();
  {
    set_ref(q1[0], 0.f, hh != e); set_ref(q1[1], 0.f, hh != e);
    f32x16 s = MFMA32(kr0, q0[0], f32x16{}), t = MFMA32(kr0, q0[1], f32x16{});
    s = MFMA8(kr1, q1[0], s); t = MFMA8(kr1, q1[1], t);
    sa = MFMA32(k0, q0[0], f32x16{});
    SBAR();
    kpiece(IC<1>{}); vpiece(IC<1>{});
    SBAR();
    set_ref(q1[0], swap_max(max16(s)), hh != e);
    sa = MFMA8(k1, q1[0], sa);
    SBAR();
    set_ref(q1[1], swap_max(max16(t)), hh != e);
  }
  const bool keeper = blockIdx.x == 0 && w == 7;
  bool advanced = false;
  u32x4 of[10];
  unsigned gx[5], gg[5]; float gs;

  auto local_attention = [&]() AINL {
    const int tok = 256 * c + 32 * w + r;
    const lds_cptr lk = (lds_cptr)lds + L_LK + (32 * w + r) * 16, lv = (lds_cptr)lds + L_LV + (4 * w + hh) * 512 + r * 16;
    const bf16x8 qa = __builtin_bit_cast(bf16x8, lq);
    const s16x4 qb = __builtin_bit_cast(s16x4, (hh == e) ? lqt : u32x2{0u, 0u});
    f32x16 sx = MFMA32(ldsr128(lk + hh * LA_UNIT), qa, f32x16{}), sy = MFMA32(ldsr128(lk + hh * LA_UNIT + 512), qa, f32x16{});
    sx = MFMA8(ldsr64(lk + 2 * LA_UNIT + 8 * e), qb, sx); sy = MFMA8(ldsr64(lk + 2 * LA_UNIT + 8 * e + 512), qb, sy);
    const bf16x8 va = ldsr128(lv), vb = ldsr128(lv + 1024), vc = ldsr128(lv + 2048);
    const u64 edge = (c == 0 && w == 0) ? 0ull : ~0ull;
    const float ninf = -INFINITY;
    sfor<0, 24>([&](auto jc) AINL {
      constexpr int j = decltype(jc)::value;
      constexpr u64 mk = []() { u64 m = 0; for (int l = 0; l < 64; ++l) { const int d = (l & 31) + 16 - ((j & 3) + 8 * (j >> 2) + 4 * (l >> 5)); if (d >= 0 && d <= 14 && !(d & 1)) m |= 1ull << l; } return m; }();
      const bool ok = __builtin_amdgcn_inverse_ballot_w64(j < 8 ? mk & edge : mk);
      if constexpr (j < 16) sx[j] = ok ? sx[j] : ninf; else sy[j - 16] = ok ? sy[j - 16] : ninf;
    });
    float m = fmaxf(fmaxf(max16(sx), sy[0]), sy[1]);
    m = fmaxf(fmaxf(m, sy[2]), sy[3]); m = fmaxf(fmaxf(m, sy[4]), sy[5]); m = -QSCALE * swap_max(fmaxf(fmaxf(m, sy[6]), sy[7]));
#pragma unroll
    for (int j = 0; j < 16; ++j) sx[j] = __builtin_amdgcn_exp2f(__builtin_fmaf(sx[j], QSCALE, m));
#pragma unroll
    for (int j = 0; j < 8; ++j) sy[j] = __builtin_amdgcn_exp2f(__builtin_fmaf(sy[j], QSCALE, m));
    f32x16 o = smf(va, sx, 0, f32x16{}, idx); o = smf(vb, sx, 1, o, idx); o = smf(vc, sy, 0, o, idx);
    auto lr = __builtin_amdgcn_permlane32_swap(__float_as_uint(o[8]), __float_as_uint(o[8]), false, false);
    const float rl = __builtin_amdgcn_rcpf(__uint_as_float(lr[1]));
    st128(rs, WS_OA + o_main(tok, h, hh), pack8(o, 0, rl));
    st64(rs, hh ? OOB : WS_OA + o_tail(tok, h), u32x2{cvtpk(o[8] * rl, o[9] * rl), cvtpk(o[10] * rl, o[11] * rl)});
    gs += __builtin_bit_cast(float, __builtin_amdgcn_update_dpp(0, __builtin_bit_cast(int, gs), 0xB1, 0xf, 0xf, true));
    gs += __builtin_bit_cast(float, __builtin_amdgcn_update_dpp(0, __builtin_bit_cast(int, gs), 0x4E, 0xf, 0xf, true));
    gs += __builtin_bit_cast(float, __builtin_amdgcn_update_dpp(0, __builtin_bit_cast(int, gs), 0x141, 0xf, 0xf, true));
    gs += __builtin_bit_cast(float, __builtin_amdgcn_update_dpp(0, __builtin_bit_cast(int, gs), 0x140, 0xf, 0xf, true));
    { auto g2 = __builtin_amdgcn_permlane16_swap(__float_as_uint(gs), __float_as_uint(gs), false, false); gs = __uint_as_float(g2[0]) + __uint_as_float(g2[1]); }
    gs = swap_add(gs);
    const float gv = __builtin_amdgcn_rcpf(1.f + __builtin_amdgcn_exp2f(-1.4426950408889634f * gs));
    __builtin_amdgcn_raw_buffer_store_b32(__float_as_uint(gv), rs, lane == 0 ? WS_G + (256 * c + 8 * h + w) * 4 : OOB, 0, SC1);
    SBAR();
  };

  const lds_cptr wl = (lds_cptr)lds + L_R + kh * (2 * WQ) + lane * 16;
  f32x4 accA[5];
  bf16x8 wq[2][5], wf[25];
  auto opa = [&](auto hc, auto gc) AINL {
    constexpr int hs = decltype(hc)::value, g = decltype(gc)::value;
    if constexpr (hs >= 0 && hs <= 5) SBAR();
    if constexpr (hs >= 1 && hs <= 5) accA[g] = MFMA16(wq[(hs - 1) & 1][g], of[hs - 1], hs == 1 ? f32x4{} : accA[g]);
    if constexpr (hs >= 0 && hs < 5) wq[hs & 1][g] = ldsr128(wl + (hs * 5 + g) * 1024);
    if constexpr (hs >= 0 && hs <= 5) SBAR();
  };
#define EX(S, j) S[j] = __builtin_amdgcn_exp2f(S[j])
#define EX2(S, j) do { EX(S, j); EX(S, j + 1); } while (0)
  sfor<0, 8>([&](auto stc) AINL {
    constexpr int st = decltype(stc)::value;
    constexpr int cur = (st & 1) * AT_STAGE, nxt = ((st + 1) & 1) * AT_STAGE;
    sfor<0, 4>([&](auto ic) AINL {
      constexpr int i = decltype(ic)::value;
      constexpr int nk = (i < 3) ? cur + (i + 1) * 512 : nxt, nv = (i < 3) ? cur + (i + 1) * 2048 : nxt;
      constexpr int sl = (i + 1) & 3, sst = (i == 3) ? st + 1 : st;
      constexpr int ha = st == 6 ? 2 * i : -9, hb = st == 6 ? 2 * i + 1 : -9;
      SBAR();
      EX2(sa, 0); PIN(sa); SBAR();
      if constexpr (st + i > 0) ob = smf(v1p, sb, 1, ob, idx);
      SBAR(); EX2(sa, 2); opa(IC<ha>{}, IC<0>{}); EX2(sa, 4); PIN(sa); SBAR();
      sb = MFMA32(k0, q0[1], f32x16{}); SBAR(); EX2(sa, 6); opa(IC<ha>{}, IC<1>{}); EX2(sa, 8); PIN(sa); SBAR();
      sb = MFMA8(k1, q1[1], sb); SBAR();
      if constexpr (i == 3) {
        wait_bar<0>();
        if constexpr (st == 0) { if (tid == 0) __hip_atomic_store(flg + flag1 * 16, tag, RLX_AGENT); }
        if constexpr (st == 2) {
          PIN(lq); PIN(lqt);
        }
        if constexpr (st == 1) {
          gs = 0.f;
#pragma unroll
          for (int i = 0; i < 5; ++i) gs = dot2(gx[i], gg[i], gs);
          PIN(gs);
        }
        if constexpr (st == 3) { if (tid == 0) __hip_atomic_store(flg + (FOA + c * 32 + h) * 16, tag, RLX_AGENT); }
        if constexpr (st == 5 || st == 7) {
#pragma unroll
          for (int j = 0; j < 5; ++j) PIN(of[(st == 7 ? 5 : 0) + j]);
          if constexpr (st == 7) PIN(gate);
        }
        if constexpr (st == 6) {
          if (keeper) {
            const lds_cptr p = (lds_cptr)lds + L_ALL + lane * 16;
            advanced = __all(*(const LAS u64*)p == tag && *(const LAS u64*)(p + 1024) == tag && *(const LAS u64*)(p + 2048) == tag && *(const LAS u64*)(p + 3072) == tag);
            if (advanced && lane == 0) __hip_atomic_store(flg, tag, RLX_AGENT);
          }
        }
        if constexpr (st == 1 || st == 4) {
          if (!__all(*(const LAS u64*)((lds_cptr)lds + L_FL + lane * 16) == tag)) wait_flags(flg, st == 1 ? fiA : fiOA, tag);
        }
      }
      __builtin_amdgcn_s_setprio(3 - sl);
      k0 = ldsr128(kp0 + nk); k1 = ldsr64(kp1 + nk);
      if constexpr (sst >= 1 && sst + 1 < 8 && sl == 0) kpiece(IC<sst + 1>{});
      if constexpr (sst == 0 && sl < 3) st128(rs, dstA[sl - 1] + BR_A, pk[2 * sl - 2]);
      if constexpr ((sst == 1 || sst == 4) && sl == 3) { if (w == 7) dma_sc1<L_FL>(ws, (sst == 1 ? fiA : fiOA) * 128, lds0); }
      if constexpr (sst == 6 && sl == 1) {
        if (keeper) sfor<0, 4>([&](auto kc) AINL { constexpr int k = decltype(kc)::value; dma_sc1<L_ALL + k * 1024>(ws + (FOA + k * 64) * 128, lane * 128, lds0); });
      }
      if constexpr (sst == 2 && sl == 0) dma_sc1<0>(ws, lalo + ((c == 0 && (w == 3 || (w < 3 && lane < 16))) ? 1024 : 0), ladst);
      if constexpr (sst == 2 && sl == 1) { dma3_sc1<1024>(ws + 1024, lalo, ladst); if (w < 3) dma_sc1<3328>(ws + 3328, lalo, ladst); }
      if constexpr ((sst == 4 || sst == 5) && (sl == 1 || sl == 2)) woquarter(IC<0>{}, IC<2 * sl - 2 + sst - 4>{}, IC<L_R + (2 * sl - 2 + sst - 4) * WQ>{});
      if constexpr (sst == 7 && sl == 0) woquarter(IC<1>{}, IC<1>{}, IC<L_RING>{});
      if constexpr (sst == 8) woquarter(IC<1>{}, IC<3>{}, IC<L_RING + AT_STAGE>{});
      if constexpr (sst == 7 && sl > 0) { wf[4 * sl - 4] = ldsr128(wl + WQ + (4 * sl - 4) * 1024); wf[4 * sl - 3] = ldsr128(wl + WQ + (4 * sl - 3) * 1024); }
      if constexpr (sst == 7 && (sl == 1 || sl == 2)) woquarter(IC<1>{}, IC<2 * sl - 2>{}, IC<L_R + (2 * sl - 2) * WQ>{});
      EX2(sa, 10); opa(IC<ha>{}, IC<2>{}); EX2(sa, 12); PIN(sa); SBAR();
      __builtin_amdgcn_s_waitcnt(0xC07F); SBAR();
      oa = smf(v0, sa, 0, oa, idx); SBAR(); EX2(sa, 14); opa(IC<ha>{}, IC<3>{}); PIN(sa); SBAR();
      EX2(sb, 0); opa(IC<ha>{}, IC<4>{}); PIN(sb); SBAR();
      oa = smf(v1, sa, 1, oa, idx); SBAR(); EX2(sb, 2); opa(IC<hb>{}, IC<0>{}); EX2(sb, 4); PIN(sb); SBAR();
      sa = MFMA32(k0, q0[0], f32x16{}); SBAR(); EX2(sb, 6); opa(IC<hb>{}, IC<1>{}); EX2(sb, 8); PIN(sb); SBAR();
      sa = MFMA8(k1, q1[0], sa); SBAR();
      if constexpr (sst >= 1 && sst + 1 < 8 && sl == 0) vpiece(IC<sst + 1>{});
      if constexpr (sst == 0 && sl < 3) st128(rs, dstA[sl - 1] + (BR_A + (cj >= 10 ? 1024 : 2 * UNIT)), pk[2 * sl - 1]);
      if constexpr (sst == 1 && sl == 1) {
        st128(rs, obm, u32x4{~0u, ~0u, ~0u, ~0u}); st64(rs, obt, u32x2{~0u, ~0u});
      }
      if constexpr (sst == 2 && sl == 0) {
        const unsigned to = WS_QKB + BR_A + (unsigned)(256 * c + 32 * w + r) * 16;
        lq = ld128(rs, (ua + hh) * UNIT + to);
        lqt = ld64(rs, us * UNIT + to + 8 * e);
      }
      if constexpr (sst == 1 && sl == 0) {
#pragma unroll
        for (int i = 0; i < 5; ++i) { gx[i] = ((const unsigned*)(x + (size_t)(256 * c + 8 * h + w) * (D_ * 2)))[lane * 5 + i]; gg[i] = ((const unsigned*)gw)[lane * 5 + i]; }
      }
      if constexpr ((sst == 5 || sst == 7) && (sl == 1 || sl == 2)) {
#pragma unroll
        for (int j = (sst == 7 ? 5 : 0) + 3 * sl - 3; j < (sst == 7 ? 5 : 0) + (sl == 1 ? 3 : 5); ++j) {
          if (j % 5 < 4) of[j] = ld128(rs, WS_OA + osrc + j * 1024);
          else { const u32x2 a = ld64(rs, WS_OA + osrc - lane * 8 + j * 1024), b = ld64(rs, WS_OA + osrc - lane * 8 + j * 1024 + 512); of[j] = u32x4{a[0], a[1], b[0], b[1]}; }
        }
        if constexpr (sst == 7 && sl == 2) gate = __uint_as_float(ld32(rs, WS_G + (rt * 64 + tq * 16 + (lane & 15)) * 4));
      }
      if constexpr (sst == 7 && sl > 0) { wf[4 * sl - 2] = ldsr128(wl + WQ + (4 * sl - 2) * 1024); wf[4 * sl - 1] = ldsr128(wl + WQ + (4 * sl - 1) * 1024); }
      v1p = v1;
      const bf16x8 v0n = ldsr128(vp + nv); v1 = ldsr128(vp + nv + 1024);
      if constexpr (sst == 8) {
        f32x4* xw = (f32x4*)(lds + L_R + (2 * kh + 1) * WQ + tq * 3072) + lane;
#pragma unroll
        for (int k = 0; k < 3; ++k) xw[k * 64] = f32x4{oa[4 * k], oa[4 * k + 1], oa[4 * k + 2], oa[4 * k + 3]};
        SBAR();
      }
      EX2(sb, 10); opa(IC<hb>{}, IC<2>{}); EX2(sb, 12); PIN(sb); SBAR();
      ob = smf(v0, sb, 0, ob, idx);
      if constexpr (sst == 8) PIN(ob);
      SBAR();
      v0 = v0n;
      EX(sb, 14); opa(IC<hb>{}, IC<3>{}); EX(sb, 15); opa(IC<hb>{}, IC<4>{}); PIN(sb); SBAR();
      if constexpr (st == 2 && i == 3) local_attention();
    });
  });
  ob = smf(v1p, sb, 1, ob, idx); PIN(ob);
  __builtin_amdgcn_s_setprio(0);
  float ofin[12];
  {
    const f32x4* xr = (const f32x4*)(lds + L_R + (3 - 2 * kh) * WQ + tq * 3072) + lane;
    wait_bar<63>();
#pragma unroll
    for (int i = 0; i < 3; ++i) { const f32x4 v = xr[i * 64]; for (int k = 0; k < 4; ++k) ofin[4 * i + k] = ob[4 * i + k] + v[k]; }
  }
  {
    auto rr = __builtin_amdgcn_permlane32_swap(__float_as_uint(ofin[8]), __float_as_uint(ofin[8]), false, false);
    float l = __uint_as_float(rr[1]);
    if (__builtin_expect(__any(!(l > 8.67e-19f && l < 1.15e18f)), 0)) {
      const bf16x8 qa = q0[1]; s16x4 qb = q1[1];
      float m = -INFINITY;
#pragma unroll 1
      for (int pass = 0; pass < 2; ++pass) {
        set_ref(qb, pass ? m : 0.f, hh != e);
        f32x16 o = f32x16{};
#pragma unroll 1
        for (int kt = 0; kt < 64; ++kt) {
          const unsigned to = (unsigned)(32 * kt + r) * 16;
          const bf16x8 a0 = __builtin_bit_cast(bf16x8, ld128(rs, WS_QKB + (80 + ua + hh) * UNIT + to));
          const s16x4 a1 = __builtin_bit_cast(s16x4, ld64(rs, hh == e ? WS_QKB + (80 + us) * UNIT + to + 8 * e : WS_C + blockIdx.x * 4096));
          f32x16 s = MFMA32(a0, qa, f32x16{}); s = MFMA8(a1, qb, s);
          if (pass == 0) { m = fmaxf(m, max16(s)); continue; }
#pragma unroll
          for (int j = 0; j < 16; ++j) EX(s, j);
#pragma unroll
          for (int s2 = 0; s2 < 2; ++s2) {
            const u32x4 vv = ld128(rs, r < 20 ? WS_VB + (h * 64 + kt) * 2048 + (2 * s2 + hh) * 512 + r * 16 : WS_C + blockIdx.x * 4096 + (r == 20 ? 0 : 16));
            o = smf(__builtin_bit_cast(bf16x8, vv), s, s2, o, idx);
          }
        }
        if (pass == 0) m = swap_max(m);
        else {
#pragma unroll
          for (int i = 0; i < 12; ++i) ofin[i] = o[i];
        }
      }
      auto r2 = __builtin_amdgcn_permlane32_swap(__float_as_uint(ofin[8]), __float_as_uint(ofin[8]), false, false);
      l = __uint_as_float(r2[1]);
    }
    const float rl = __builtin_amdgcn_rcpf(l);
    unsigned ow[6];
#pragma unroll
    for (int i = 0; i < 6; ++i) ow[i] = min(cvtpk(ofin[2 * i] * rl, ofin[2 * i + 1] * rl), 0xfffffffeu);
    st128(rs, obm, u32x4{ow[0], ow[1], ow[2], ow[3]});
    st64(rs, obt, u32x2{ow[4], ow[5]});
  }
  SBAR();
#pragma unroll
  for (int f = 12; f < 25; ++f) wf[f] = ldsr128(wl + WQ + f * 1024);
  SBAR();

  f32x4 accB[5];
  u32x4 og[8]; u32x2 ot[4];
  const unsigned ob0 = osrc + SWP * 5120, ob1 = osrc + (SWP ^ 1) * 5120, ot0 = ob0 - lane * 8 + 4096, ot1 = ob1 - lane * 8 + 4096;
#define OBLD(k, r, a, o) "s_bitcmp1_b32 %17, " #k "\n\ts_cbranch_scc0 1f\n\tglobal_load_dwordx4 %" #r ", %" #a ", %16 offset:" #o " sc1\n1:\n\t"
#define OBLT(k, r0, r1, a) "s_bitcmp1_b32 %17, " #k "\n\ts_cbranch_scc0 1f\n\tglobal_load_dwordx2 %" #r0 ", %" #a ", %16 sc1\n\tglobal_load_dwordx2 %" #r1 ", %" #a ", %16 offset:512 sc1\n1:\n\t"
#define OB_ALL OBLD(0, 0, 12, 0) OBLD(1, 1, 12, 1024) OBLD(2, 2, 12, 2048) OBLD(3, 3, 12, 3072) OBLT(4, 8, 9, 14) OBLD(5, 4, 13, 0) OBLD(6, 5, 13, 1024) OBLD(7, 6, 13, 2048) OBLD(8, 7, 13, 3072) OBLT(9, 10, 11, 15)
#define OG(c) c(og[0]), c(og[1]), c(og[2]), c(og[3]), c(og[4]), c(og[5]), c(og[6]), c(og[7]), c(ot[0]), c(ot[1]), c(ot[2]), c(ot[3])
#define OBL1(k, a, o) og[k] = ld128(rs, WS_OB + a + o)
#define OBL2(k, a, o) ot[k] = ld64(rs, WS_OB + a + o)
#define OFR(j) ((j) % 5 < 4 ? og[((j) - (j) / 5) & 7] : u32x4{ot[2 * ((j) / 5)][0], ot[2 * ((j) / 5)][1], ot[2 * ((j) / 5) + 1][0], ot[2 * ((j) / 5) + 1][1]})
#pragma unroll
  for (int j = 0; j < 5; ++j) {
    if (j == 0) __builtin_amdgcn_s_setprio(3); if (j == 2) __builtin_amdgcn_s_setprio(2); if (j == 3) __builtin_amdgcn_s_setprio(1); if (j == 4) __builtin_amdgcn_s_setprio(0);
#pragma unroll
    for (int n = 0; n < 5; ++n) accA[n] = MFMA16(wf[j * 5 + n], of[5 + j], accA[n]);
    SBAR();
#pragma unroll
    for (int n = 0; n < 5; ++n) wf[j * 5 + n] = ldsr128(wl + (j * 5 + n) * 1024);
    if (j == 4) {
      OBL1(0, ob0, 0); OBL1(1, ob0, 1024); OBL1(2, ob0, 2048); OBL1(3, ob0, 3072); OBL2(0, ot0, 0); OBL2(1, ot0, 512);
      OBL1(4, ob1, 0); OBL1(5, ob1, 1024); OBL1(6, ob1, 2048); OBL1(7, ob1, 3072); OBL2(2, ot1, 0); OBL2(3, ot1, 512);
    }
    SBAR();
  }
  waitv_bar<12>();
  SBAR();
  const lds_cptr wh = (lds_cptr)lds + L_RING + kh * AT_STAGE + lane * 16;
#pragma unroll
  for (int j = 0; j < 10; ++j) {
    if (j % 5 == 0) {
      unsigned seen = 0;
#pragma unroll
      for (int k = j; k < j + 5; ++k) { const u32x4 v = OFR(k); seen = max(max(seen, v[0]), v[1]); seen = max(max(seen, v[2]), v[3]); }
      if (__any(seen == ~0u)) {
#pragma unroll 1
        for (unsigned spins = 0; spins < (1u << 16); ++spins) {
          unsigned pend = 0;
#pragma unroll
          for (int k = j; k < 10; ++k) { const u32x4 v = OFR(k); pend |= __any(max(max(v[0], v[1]), max(v[2], v[3])) == ~0u) ? (1u << k) : 0u; }
          if (!(pend & (0x1fu << j))) break;
          asm volatile(OB_ALL "s_waitcnt vmcnt(0)" : OG("+v") : "v"(ob0), "v"(ob1), "v"(ot0), "v"(ot1), "s"(ws + WS_OB), "s"(pend) : "memory", "scc");
        }
      }
    }
    if (j == 0) __builtin_amdgcn_s_setprio(3); if (j == 3) __builtin_amdgcn_s_setprio(2); if (j == 6) __builtin_amdgcn_s_setprio(1); if (j == 8) __builtin_amdgcn_s_setprio(0);
#pragma unroll
    for (int n = 0; n < 5; ++n) accB[n] = MFMA16(wf[(j % 5) * 5 + n], OFR(j), j == 0 ? f32x4{} : accB[n]);
    if (j < 5) {
#pragma unroll
      for (int n = 0; n < 5; ++n) wf[j * 5 + n] = ldsr128(wh + (j * 5 + n) * 1024);
    }
    SBAR();
  }
#pragma unroll
  for (int n = 0; n < 5; ++n) accB[n] = accA[n] * gate + accB[n] * (1.f - gate);
  {
    char* img = lds + L_R + (2 * kh + 1) * WQ + (tq * 16 + (lane & 15)) * 336 + (lane >> 4) * 16;
#pragma unroll
    for (int n = 0; n < 5; ++n) *(f32x4*)(img + n * 64) = accB[n];
    wait_bar<63>();
#pragma unroll
    for (int it = 0; it < 2; ++it) {
      const int u = tid + 512 * it, row = u / 10, pc = u - 10 * row;
      if (u < 640) {
        const char* a = lds + L_R + WQ + row * 336 + pc * 32;
        const f32x4 v0 = *(const f32x4*)a + *(const f32x4*)(a + 2 * WQ), v1 = *(const f32x4*)(a + 16) + *(const f32x4*)(a + 2 * WQ + 16);
        const u32x4 r4 = u32x4{cvtpk(v0[0], v0[1]), cvtpk(v0[2], v0[3]), cvtpk(v1[0], v1[1]), cvtpk(v1[2], v1[3])};
        char* op = out + (size_t)(rt * 64 + row) * (D_ * 2) + ct * 160 + pc * 16;
        *(u32x4*)op = r4;
      }
    }
  }
  asm volatile("GM_CODE_END:");
  if (keeper && !advanced) {
#pragma unroll 1
    for (int i = 0; i < 4; ++i) wait_flags(flg, FOA + i * 64 + lane, tag);
    if (lane == 0) __hip_atomic_store(flg, tag, RLX_AGENT);
  }
}

extern "C" void kernel_launch(void* const* d_in, const int* in_sizes, int n_in, void* d_out, int out_size, void* d_ws,
                              size_t ws_size, hipStream_t stream) {
  (void)hipFuncSetAttribute((const void*)fused_kernel, hipFuncAttributeMaxDynamicSharedMemorySize, LDS_BYTES);
  fused_kernel<<<256, 512, LDS_BYTES, stream>>>((const char*)d_in[0], (const char*)d_in[1], (const char*)d_in[4], (const char*)d_in[2],
                                                (const char*)d_in[5], (const char*)d_in[6], (char*)d_ws, (char*)d_out);
}
```
